```python
import torch

def _softmax(x, axis=-1):
    return torch.softmax(x, dim=axis)
def _gelu(x):
    return torch.nn.functional.gelu(x, approximate="tanh")
def _silu(x):
    return torch.nn.functional.silu(x)
def _sigmoid(x):
    return torch.sigmoid(x)
def _layernorm(x, axis=-1, eps=1e-5):
    m = x.mean(dim=axis, keepdim=True)
    v = x.var(dim=axis, keepdim=True, unbiased=False)
    return (x - m) / torch.sqrt(v + eps)
def _scatter_add(tgt, idx, src, axis=0):
    return tgt.clone().index_add_(axis, idx, src)
def _segment_sum(src, seg, n):
    out = torch.zeros((n,) + src.shape[1:], dtype=src.dtype)
    return out.index_add_(0, seg, src)
def _pad(x, pw):
    flat = tuple(v for pair in reversed(pw) for v in pair)
    return torch.nn.functional.pad(x, flat)

kernel_name = 'gated_mix__spectral_fnet__softmax_full_mha__prompt'

def setup_inputs():

    _w = (lambda a: (lambda t: t.to(torch.bfloat16) if t.is_floating_point() else t)(torch.as_tensor(a)))
    def _A_setup():
        B, S, D = 1, 2048, 1024
        ns = torch.arange(S)[:, None] * torch.arange(S)[None, :]
        nd = torch.arange(D)[:, None] * torch.arange(D)[None, :]
        Ws_re = torch.cos(-2.0 * torch.pi * ns / S).to(float)
        Ws_im = torch.sin(-2.0 * torch.pi * ns / S).to(float)
        Wd_re = torch.cos(-2.0 * torch.pi * nd / D).to(float)
        Wd_im = torch.sin(-2.0 * torch.pi * nd / D).to(float)
        return {
            "x": _w((lambda *s: torch.randn(*s, dtype=torch.bfloat16) * 0.05)(B, S, D)),
            "dft_seq_re": _w(Ws_re),
            "dft_seq_im": _w(Ws_im),
            "dft_hid_re": _w(Wd_re),
            "dft_hid_im": _w(Wd_im),
        }
    _A_in = _A_setup()

    def _B_setup():
        B, S, D = 1, 2048, 1024
        return {
            "x": _w((lambda *s: torch.randn(*s, dtype=torch.bfloat16) * 0.05)(B, S, D)),
            "wqkv": _w((lambda *s: torch.randn(*s, dtype=torch.bfloat16) * 1.6)(D, 3 * D)),
            "wo": _w((lambda *s: torch.randn(*s, dtype=torch.bfloat16) * 0.2)(D, D)),
        }
    _B_in = _B_setup()

    return {
        "A_x": _A_in["x"],
        "A_dft_seq_re": _A_in["dft_seq_re"],
        "A_dft_seq_im": _A_in["dft_seq_im"],
        "A_dft_hid_re": _A_in["dft_hid_re"],
        "A_dft_hid_im": _A_in["dft_hid_im"],
        "B_x": _B_in["x"],
        "B_wqkv": _B_in["wqkv"],
        "B_wo": _B_in["wo"],
        "gate_w": _w((lambda *s: torch.randn(*s, dtype=torch.bfloat16) * 0.05)(1024) * 16.0),
    }

def reference(A_x, A_dft_seq_re, A_dft_seq_im, A_dft_hid_re, A_dft_hid_im, B_x, B_wqkv, B_wo, gate_w):
    def _A_ref(x, dft_seq_re, dft_seq_im, dft_hid_re, dft_hid_im):
        a = torch.einsum("bsd,ed->bse", x, dft_hid_re)
        b = torch.einsum("bsd,ed->bse", x, dft_hid_im)
        y = torch.einsum("ts,bse->bte", dft_seq_re, a) - torch.einsum("ts,bse->bte", dft_seq_im, b)
        return y

    def _B_ref(x, wqkv, wo):
        B, S, D = x.shape
        H = 32; Dh = D // H
        qkv = (lambda x, *d: x.permute(*d))((x @ wqkv).reshape(B, S, 3, H, Dh), 2, 0, 3, 1, 4)
        q, k, v = qkv[0], qkv[1], qkv[2]
        scale = Dh ** -0.5
        scores = torch.einsum("bhqd,bhkd->bhqk", q, k) * scale
        p = _softmax(scores, axis=-1)
        o = torch.einsum("bhqk,bhkd->bhqd", p, v)
        return (lambda x, *d: x.permute(*d))(o, 0, 2, 1, 3).reshape(B, S, D) @ wo

    x = A_x
    g = _sigmoid((x * gate_w).sum(-1, keepdims=True))
    a = _A_ref(x, A_dft_seq_re, A_dft_seq_im, A_dft_hid_re, A_dft_hid_im)
    b = _B_ref(x, B_wqkv, B_wo)
    return g * a + (1 - g) * b
```

```cpp
#include <hip/hip_runtime.h>
#include <hip/hip_bf16.h>
typedef __hip_bfloat16 bf16;
static __device__ __forceinline__ float b2f(bf16 x){ return __bfloat162float(x); }
static __device__ __forceinline__ bf16 f2b(float x){ return __float2bfloat16(x); }
constexpr int S=2048, D=1024, H=32, DH=32;

__global__ void k_gate(const bf16* x, const bf16* gw, float* g){
  int s = blockIdx.x; float acc=0.f;
  for(int d=threadIdx.x; d<D; d+=64) acc += b2f(x[s*D+d])*b2f(gw[d]);
  for(int o=32;o>=1;o>>=1) acc += __shfl_xor(acc,o);
  if(threadIdx.x==0) g[s] = 1.f/(1.f+__expf(-acc));
}
__global__ void k_gemm(const bf16* A, int lda, const bf16* B, int sbk, int sbn, bf16* C, float* Cf, int ldc, int K, float alpha, int accum){
  __shared__ float As[16][17], Bs[16][17];
  int tx=threadIdx.x, ty=threadIdx.y; int m=blockIdx.y*16+ty, n=blockIdx.x*16+tx; float acc=0.f;
  for(int k0=0;k0<K;k0+=16){
    As[ty][tx]=b2f(A[(size_t)m*lda+k0+tx]);
    Bs[ty][tx]=b2f(B[(size_t)(k0+ty)*sbk+(size_t)n*sbn]);
    __syncthreads();
    for(int k=0;k<16;++k) acc+=As[ty][k]*Bs[k][tx];
    __syncthreads();
  }
  if(Cf){ if(accum) Cf[(size_t)m*ldc+n]+=alpha*acc; else Cf[(size_t)m*ldc+n]=alpha*acc; }
  else C[(size_t)m*ldc+n]=f2b(alpha*acc);
}
__global__ void k_attn(const bf16* qkv, bf16* o){
  int q=blockIdx.x, h=blockIdx.y, lane=threadIdx.x;
  __shared__ float qs[DH]; __shared__ float p[S];
  if(lane<DH) qs[lane]=b2f(qkv[(size_t)q*3*D+h*DH+lane]);
  __syncthreads();
  float mx=-1e30f;
  for(int k=lane;k<S;k+=64){ float s=0; for(int d=0;d<DH;++d) s+=qs[d]*b2f(qkv[(size_t)k*3*D+D+h*DH+d]); s*=0.17677669529f; p[k]=s; mx=fmaxf(mx,s);}
  for(int o2=32;o2>=1;o2>>=1) mx=fmaxf(mx,__shfl_xor(mx,o2));
  float l=0; for(int k=lane;k<S;k+=64){ float e=__expf(p[k]-mx); p[k]=e; l+=e; }
  for(int o2=32;o2>=1;o2>>=1) l+=__shfl_xor(l,o2);
  __syncthreads();
  int d=lane&31, half=lane>>5; float acc=0;
  for(int k=half;k<S;k+=2) acc+=p[k]*b2f(qkv[(size_t)k*3*D+2*D+h*DH+d]);
  acc+=__shfl_xor(acc,32);
  if(half==0) o[(size_t)q*D+h*DH+d]=f2b(acc/l);
}
__global__ void k_final(const float* y, const float* bo, const float* g, bf16* out){
  int i=blockIdx.x*256+threadIdx.x; int s=i/D; float gg=g[s]; out[i]=f2b(gg*y[i]+(1.f-gg)*bo[i]);
}
extern "C" void kernel_launch(void* const* d_in, const int* in_sizes, int n_in, void* d_out, int out_size, void* d_ws, size_t ws_size, hipStream_t stream){
  const bf16* x=(const bf16*)d_in[0]; const bf16* wsr=(const bf16*)d_in[1]; const bf16* wsi=(const bf16*)d_in[2];
  const bf16* wdr=(const bf16*)d_in[3]; const bf16* wdi=(const bf16*)d_in[4]; const bf16* wqkv=(const bf16*)d_in[6]; const bf16* wo=(const bf16*)d_in[7]; const bf16* gw=(const bf16*)d_in[8];
  char* ws=(char*)d_ws;
  float* g=(float*)ws; ws+=1<<20;
  bf16* a=(bf16*)ws; ws+=(size_t)S*D*2; bf16* b=(bf16*)ws; ws+=(size_t)S*D*2; bf16* qkv=(bf16*)ws; ws+=(size_t)S*3*D*2; bf16* o=(bf16*)ws; ws+=(size_t)S*D*2;
  float* y=(float*)ws; ws+=(size_t)S*D*4; float* bo=(float*)ws; ws+=(size_t)S*D*4;
  dim3 t(16,16);
  k_gate<<<S,64,0,stream>>>(x,gw,g);
  k_gemm<<<dim3(D/16,S/16),t,0,stream>>>(x,D,wdr,1,D,a,nullptr,D,D,1.f,0);
  k_gemm<<<dim3(D/16,S/16),t,0,stream>>>(x,D,wdi,1,D,b,nullptr,D,D,1.f,0);
  k_gemm<<<dim3(3*D/16,S/16),t,0,stream>>>(x,D,wqkv,3*D,1,qkv,nullptr,3*D,D,1.f,0);
  k_gemm<<<dim3(D/16,S/16),t,0,stream>>>(wsr,S,a,D,1,nullptr,y,D,S,1.f,0);
  k_gemm<<<dim3(D/16,S/16),t,0,stream>>>(wsi,S,b,D,1,nullptr,y,D,S,-1.f,1);
  k_attn<<<dim3(S,H),64,0,stream>>>(qkv,o);
  k_gemm<<<dim3(D/16,S/16),t,0,stream>>>(o,D,wo,D,1,nullptr,bo,D,D,1.f,0);
  k_final<<<S*D/256,256,0,stream>>>(y,bo,g,(bf16*)d_out);
}
```

```cpp
#include <hip/hip_runtime.h>
#include <hip/hip_bf16.h>

typedef unsigned short u16;
typedef short bf16x8 __attribute__((ext_vector_type(8)));
typedef short s16x4 __attribute__((ext_vector_type(4)));
typedef float f32x4 __attribute__((ext_vector_type(4)));
typedef float f32x16 __attribute__((ext_vector_type(16)));
typedef unsigned u32x4 __attribute__((ext_vector_type(4)));
typedef unsigned u32x2 __attribute__((ext_vector_type(2)));
#define LAS __attribute__((address_space(3)))
#define GAS __attribute__((address_space(1)))
#define DI __device__ __forceinline__
#define SBAR() __builtin_amdgcn_sched_barrier(0)
#define PIN(x) asm volatile("" : "+v"(x))
#define MFMA16(a, b, c) __builtin_amdgcn_mfma_f32_16x16x32_bf16(a, b, c, 0, 0, 0)
#define MFMA32(a, b, c) __builtin_amdgcn_mfma_f32_32x32x16_bf16(a, b, c, 0, 0, 0)
#define WAIT_BAR(N) asm volatile("s_waitcnt vmcnt(" #N ") lgkmcnt(0)\n\ts_barrier" ::: "memory")

constexpr int SEQ = 2048, DM = 1024, NH = 32;
#define STAMP(kid, i)

typedef float f32x2 __attribute__((ext_vector_type(2)));
typedef __bf16 bf16x2_t __attribute__((ext_vector_type(2)));
DI unsigned cvtpk(float lo, float hi) { f32x2 v = {lo, hi}; bf16x2_t b = __builtin_convertvector(v, bf16x2_t); return __builtin_bit_cast(unsigned, b); }
DI void dma(unsigned voff, const char* sbase, unsigned l) {
    asm volatile("s_mov_b32 m0, %2\n\ts_nop 0\n\tglobal_load_lds_dwordx4 %0, %1" :: "v"(voff), "s"(sbase), "s"(l) : "memory"); }
DI void dma_lo32(unsigned voff, const char* sbase, unsigned l) {
    asm volatile("s_mov_b32 m0, %2\n\ts_mov_b32 exec_hi, 0\n\tglobal_load_lds_dwordx4 %0, %1\n\ts_mov_b32 exec_hi, -1" :: "v"(voff), "s"(sbase), "s"(l) : "memory"); }
DI bf16x8 ldsr128(const LAS char* p) { return *(const LAS bf16x8*)p; }
DI bf16x8 ldstr(const LAS char* p, int off2) {
    s16x4 a = __builtin_amdgcn_ds_read_tr16_b64_v4i16((LAS s16x4*)p);
    s16x4 b = __builtin_amdgcn_ds_read_tr16_b64_v4i16((LAS s16x4*)(p + off2));
    return (bf16x8){a[0], a[1], a[2], a[3], b[0], b[1], b[2], b[3]};
}
DI float bflo(unsigned u) { return __uint_as_float(u << 16); }
DI float bfhi(unsigned u) { return __uint_as_float(u & 0xffff0000u); }
DI u32x4 swap16(u32x2 a, u32x2 b) {
    auto r0 = __builtin_amdgcn_permlane16_swap(a[0], b[0], false, false);
    auto r1 = __builtin_amdgcn_permlane16_swap(a[1], b[1], false, false);
    return (u32x4){r0[0], r1[0], r0[1], r1[1]};
}
DI u32x2 pack4(f32x4 v) { return (u32x2){cvtpk(v[0], v[1]), cvtpk(v[2], v[3])}; }

constexpr int K1_STAGE = 53248, K1_A = 0, K1_WD = 32768, K1_WQ = 40960;
struct K1Frag { bf16x8 x[4], wd[2], wqk[2], wv; };

__global__ __launch_bounds__(512, 2) void k1_proj(const u16* __restrict__ x, const u16* __restrict__ wdr, const u16* __restrict__ wdi,
                                                   const u16* __restrict__ wqkv, const u16* __restrict__ gw, u16* __restrict__ Qi,
                                                   u16* __restrict__ Ki, u16* __restrict__ Vi, u16* __restrict__ abT,
                                                   float* __restrict__ gate, float* __restrict__ kmaxp) {
    __shared__ __attribute__((aligned(1024))) char smem_[3 * K1_STAGE];
    LAS char* smem = (LAS char*)smem_;
    STAMP(0, 0);
    const int tid = threadIdx.x, lane = tid & 63, wid = __builtin_amdgcn_readfirstlane(tid >> 6), wm = wid >> 1, wn = wid & 1;
    const int xcd = blockIdx.x & 7, idx = blockIdx.x >> 3;
    const int mt = (xcd & 1) * 4 + (idx & 3), hd = (xcd >> 1) * 8 + (idx >> 2);
    const char* xb = (const char*)x + (size_t)mt * 256 * 2048;
    const char* wdb = (const char*)(hd < 16 ? wdr + (size_t)hd * 64 * 1024 : wdi + (size_t)(hd - 16) * 64 * 1024);
    const char* wqb = (const char*)wqkv + hd * 64 + (wid >> 2) * 2048 + (wid & 3) * 16 * 6144;
    const char* wvb = (const char*)wqkv + hd * 64 + 2 * 2048 + (wid >> 1) * 16 * 6144;
    const unsigned voffA = (8 * wid + (lane >> 3)) * 2048 + (((lane & 7) ^ ((4 * wid + (lane >> 4)) & 7)) * 16);
    const unsigned voffW = (lane >> 2) * 6144 + (((lane & 3) ^ ((lane >> 5) << 1)) * 16);
    const unsigned voffH = (8 * wn + ((lane & 31) >> 2)) * 6144 + (((lane & 3) ^ (wn << 1)) * 16);
    const int grow = 256 * mt + 8 * hd + wid;
    u32x4 gx0, gx1, gw0, gw1;
    {
        const char* px = (const char*)x + (size_t)grow * 2048 + lane * 32;
        const char* pw = (const char*)gw + lane * 32;
        asm volatile("global_load_dwordx4 %0, %4, off\n\tglobal_load_dwordx4 %1, %4, off offset:16\n\t"
                     "global_load_dwordx4 %2, %5, off\n\tglobal_load_dwordx4 %3, %5, off offset:16"
                     : "=&v"(gx0), "=&v"(gx1), "=&v"(gw0), "=&v"(gw1) : "v"(px), "v"(pw) : "memory");
    }
    const unsigned lds0 = (unsigned)(size_t)smem + wid * 1024;
    const unsigned ldsH = (unsigned)(size_t)smem + K1_WQ + 8192 + (wid >> 1) * 1024 + wn * 512;
#define K1_DMA_P(p, t, slot) do { const size_t ka_ = (size_t)(t) * 128, kw_ = (size_t)(t) * 64 * 6144; \
        if ((p) < 4) dma(voffA + (p) * 64 * 2048, xb + ka_, lds0 + (slot) + K1_A + (p) * 8192); \
        else if ((p) == 4) dma(voffA, wdb + ka_, lds0 + (slot) + K1_WD); \
        else if ((p) == 5) dma(voffW, wqb + kw_, lds0 + (slot) + K1_WQ); \
        else dma_lo32(voffH, wvb + kw_, ldsH + (slot)); } while (0)
#define K1_DMA(t, slot) do { _Pragma("unroll") for (int p_ = 0; p_ < 7; ++p_) K1_DMA_P(p_, t, slot); } while (0)
    K1_DMA(0, 0); K1_DMA(1, K1_STAGE); K1_DMA(2, 2 * K1_STAGE);
    const int fr = lane & 15, fq = lane >> 4;
    const int cpart = ((fq ^ (fr >> 1)) & 7) * 16;
    const int aA = K1_A + (64 * wm + fr) * 128 + cpart, aD = K1_WD + (32 * wn + fr) * 128 + cpart;
    const int aT0 = (8 * fq + (fr >> 2)) * 64 + ((((lane & 3) >> 1) ^ ((fq & 1) << 1)) * 16) + (lane & 1) * 8;
    const int aQ = K1_WQ + wn * 4096 + aT0, aV = K1_WQ + 8192 + (aT0 ^ (wn * 32));
    f32x4 accQ[2][4], accV[4], accD[2][4];
#pragma unroll
    for (int m = 0; m < 4; ++m) { accQ[0][m] = f32x4{}; accQ[1][m] = f32x4{}; accV[m] = f32x4{}; accD[0][m] = f32x4{}; accD[1][m] = f32x4{}; }
    K1Frag X, Y;
#define K1_RD(F, i, slot, ks) do { const LAS char* sb_ = smem + (slot); \
        if ((i) == 0) F.wqk[0] = ldstr(sb_ + aQ + (ks) * 2048, 256); \
        else if ((i) == 1) { F.x[0] = ldsr128(sb_ + (aA ^ ((ks) * 64))); } \
        else if ((i) == 2) F.wqk[1] = ldstr(sb_ + (aQ ^ 32) + (ks) * 2048, 256); \
        else if ((i) == 3) F.wv = ldstr(sb_ + aV + (ks) * 2048, 256); \
        else if ((i) == 4) { F.wd[0] = ldsr128(sb_ + (aD ^ ((ks) * 64))); F.wd[1] = ldsr128(sb_ + (aD ^ ((ks) * 64)) + 2048); } \
        else if ((i) < 8) F.x[(i) - 4] = ldsr128(sb_ + (aA ^ ((ks) * 64)) + ((i) - 4) * 2048); } while (0)
#define K1_READ(F, slot, ks) do { _Pragma("unroll") for (int i_ = 0; i_ < 8; ++i_) K1_RD(F, i_, slot, ks); } while (0)
#define K1_MF(F, j) do { constexpr int m_ = (j) / 5, r_ = (j) % 5; \
        if (r_ == 0) accQ[0][m_] = MFMA16(F.wqk[0], F.x[m_], accQ[0][m_]); \
        else if (r_ == 1) accQ[1][m_] = MFMA16(F.wqk[1], F.x[m_], accQ[1][m_]); \
        else if (r_ == 2) accV[m_] = MFMA16(F.x[m_], F.wv, accV[m_]); \
        else if (r_ == 3) accD[0][m_] = MFMA16(F.x[m_], F.wd[0], accD[0][m_]); \
        else accD[1][m_] = MFMA16(F.x[m_], F.wd[1], accD[1][m_]); } while (0)
#define K1_G(g, C, R, rslot, ks, DM_, t, dslot) do { K1_MF(C, 2 * (g)); K1_RD(R, g, rslot, ks); K1_MF(C, 2 * (g) + 1); if (DM_ && (g) >= 2 && (g) < 9) K1_DMA_P((g) - 2, t, dslot); SBAR(); } while (0)
#define K1_PHASE(C, R, rslot, ks, DM_, t, dslot) do { K1_G(0, C, R, rslot, ks, DM_, t, dslot); K1_G(1, C, R, rslot, ks, DM_, t, dslot); K1_G(2, C, R, rslot, ks, DM_, t, dslot); \
        K1_G(3, C, R, rslot, ks, DM_, t, dslot); K1_G(4, C, R, rslot, ks, DM_, t, dslot); K1_G(5, C, R, rslot, ks, DM_, t, dslot); K1_G(6, C, R, rslot, ks, DM_, t, dslot); \
        K1_G(7, C, R, rslot, ks, DM_, t, dslot); K1_G(8, C, R, rslot, ks, DM_, t, dslot); K1_G(9, C, R, rslot, ks, DM_, t, dslot); } while (0)
    WAIT_BAR(14);
    asm volatile("" : "+v"(gx0), "+v"(gx1), "+v"(gw0), "+v"(gw1));
    float gval;
    {
        float s = 0.f;
#pragma unroll
        for (int i = 0; i < 4; ++i) {
            s = fmaf(bflo(gx0[i]), bflo(gw0[i]), s); s = fmaf(bfhi(gx0[i]), bfhi(gw0[i]), s);
            s = fmaf(bflo(gx1[i]), bflo(gw1[i]), s); s = fmaf(bfhi(gx1[i]), bfhi(gw1[i]), s);
        }
#pragma unroll
        for (int o = 32; o >= 1; o >>= 1) s += __shfl_xor(s, o);
        gval = 1.f / (1.f + __expf(-s));
    }
    STAMP(0, 1);
    K1_READ(X, 0, 0);
    int sl = 0, sn = K1_STAGE;
    SBAR();
    for (int t = 0; t < 13; ++t) {
        K1_PHASE(X, Y, sl, 1, false, 0, 0);
        WAIT_BAR(7); SBAR();
        K1_PHASE(Y, X, sn, 0, true, t + 3, sl);
        sl = sn; sn = (sn == 2 * K1_STAGE) ? 0 : sn + K1_STAGE;
    }
    K1_PHASE(X, Y, K1_STAGE, 1, false, 0, 0);
    WAIT_BAR(7); SBAR();
    K1_PHASE(Y, X, 2 * K1_STAGE, 0, false, 0, 0);
    K1_PHASE(X, Y, 2 * K1_STAGE, 1, false, 0, 0);
    WAIT_BAR(0); SBAR();
    K1_PHASE(Y, X, 0, 0, false, 0, 0);
    K1_PHASE(X, Y, 0, 1, false, 0, 0);
    K1_MF(Y, 0); K1_MF(Y, 1); K1_MF(Y, 2); K1_MF(Y, 3); K1_MF(Y, 4); K1_MF(Y, 5); K1_MF(Y, 6); K1_MF(Y, 7); K1_MF(Y, 8); K1_MF(Y, 9);
    K1_MF(Y, 10); K1_MF(Y, 11); K1_MF(Y, 12); K1_MF(Y, 13); K1_MF(Y, 14); K1_MF(Y, 15); K1_MF(Y, 16); K1_MF(Y, 17); K1_MF(Y, 18); K1_MF(Y, 19);
    STAMP(0, 2);
    const int s0 = 256 * mt + 64 * wm;
    const float qs = wn ? 1.0f : 0.17677669529663687f * 1.4426950408889634f;
    char* qk = (char*)(wn ? Ki : Qi) + (size_t)hd * 131072;
    float kmx = 0.f;
#pragma unroll
    for (int m = 0; m < 4; ++m) {
        const f32x4 a = accQ[0][m] * qs, b = accQ[1][m] * qs;
        float ss = a[0] * a[0] + a[1] * a[1] + a[2] * a[2] + a[3] * a[3] + b[0] * b[0] + b[1] * b[1] + b[2] * b[2] + b[3] * b[3];
        ss += __shfl_xor(ss, 16); ss += __shfl_xor(ss, 32); kmx = fmaxf(kmx, ss);
        const u32x4 w = swap16(pack4(a), pack4(b));
        const int s = s0 + 16 * m + fr, r = s & 31, rho = (r & 19) | ((r & 4) << 1) | ((r & 8) >> 1);
        *(u32x4*)(qk + (s >> 5) * 2048 + (fq & 1) * 1024 + ((fq >> 1) * 32 + rho) * 16) = w;
    }
    const int rv = 8 * (fr >> 2) + 4 * wn + (fr & 3);
#pragma unroll
    for (int i = 0; i < 2; ++i) {
        const u32x4 w = swap16(pack4(accV[2 * i]), pack4(accV[2 * i + 1]));
        *(u32x4*)((char*)Vi + (size_t)hd * 131072 + ((s0 >> 5) + i) * 2048 + (fq & 1) * 1024 + ((fq >> 1) * 32 + rv) * 16) = w;
#pragma unroll
        for (int n = 0; n < 2; ++n) {
            const u32x4 d = swap16(pack4(accD[n][2 * i]), pack4(accD[n][2 * i + 1]));
            *(u32x4*)(abT + (size_t)(64 * hd + 32 * wn + 16 * n + fr) * 2048 + s0 + 32 * i + 16 * (fq & 1) + 8 * (fq >> 1)) = d;
        }
    }
#pragma unroll
    for (int o = 8; o >= 1; o >>= 1) kmx = fmaxf(kmx, __shfl_xor(kmx, o));
    if (wn == 1 && lane == 0) kmaxp[hd * 32 + mt * 4 + wm] = kmx;
    if (lane == 0) gate[grow] = gval;
    STAMP(0, 3);
}

constexpr int FN_STAGE = 32768, AT_STAGE = 8192, AT_BASE = 4 * FN_STAGE;
struct FnFrag { bf16x8 w[4], a[4]; };
#define FN_PRIO 0
#define AT_PRIO 0
#define FN_PACE()
DI void fnet_role(LAS char* smem, int wid, int lane, const u16* __restrict__ wsr, const u16* __restrict__ wsi, const u16* __restrict__ abT, u16* __restrict__ Y) {
    const int wm = wid >> 1, wn = wid & 1;
    const int xcd = blockIdx.x & 7, idx = blockIdx.x >> 3;
    const int c = xcd & 1, mt = (xcd >> 1) * 4 + (idx & 3), nt = idx >> 2;
    const char* wb = (const char*)(c ? wsi : wsr) + (size_t)mt * 128 * 4096;
    const char* ab = (const char*)abT + (size_t)(c * 1024 + nt * 128) * 4096;
    const unsigned voff = (8 * wid + (lane >> 3)) * 4096 + (((lane & 7) ^ ((4 * wid + (lane >> 4)) & 7)) * 16);
    const unsigned lds0 = (unsigned)(size_t)smem + wid * 1024;
#define FN_DMA_P(p, t, slot) do { if ((p) & 1) dma(voff + ((p) >> 1) * 32 * 4096, ab + (size_t)(t) * 128, lds0 + (slot) + 16384 + ((p) >> 1) * 4096); \
        else dma(voff + ((p) >> 1) * 32 * 4096, wb + (size_t)(t) * 128, lds0 + (slot) + ((p) >> 1) * 4096); } while (0)
#define FN_DMA(t, slot) do { _Pragma("unroll") for (int p_ = 0; p_ < 8; ++p_) FN_DMA_P(p_, t, slot); } while (0)
    FN_DMA(0, 0); FN_DMA(1, FN_STAGE); FN_DMA(2, 2 * FN_STAGE); FN_DMA(3, 3 * FN_STAGE);
    const int fr = lane & 15, fq = lane >> 4;
    const int cpart = ((fq ^ (fr >> 1)) & 7) * 16;
    const int aW = (64 * wm + fr) * 128 + cpart, aA = 16384 + (64 * wn + fr) * 128 + cpart;
    f32x4 acc[4][4];
#pragma unroll
    for (int m = 0; m < 4; ++m)
#pragma unroll
        for (int n = 0; n < 4; ++n) acc[m][n] = f32x4{};
    FnFrag X, Yf;
#define FN_RD(F, i, slot, ks) do { const LAS char* sb_ = smem + (slot); \
        if ((i) == 0) F.w[0] = ldsr128(sb_ + (aW ^ ((ks) * 64))); \
        else if ((i) < 5) F.a[(i) - 1] = ldsr128(sb_ + (aA ^ ((ks) * 64)) + ((i) - 1) * 2048); \
        else F.w[(i) - 4] = ldsr128(sb_ + (aW ^ ((ks) * 64)) + ((i) - 4) * 2048); } while (0)
#define FN_READ(F, slot, ks) do { _Pragma("unroll") for (int i_ = 0; i_ < 8; ++i_) FN_RD(F, i_, slot, ks); } while (0)
#define FN_MF(F, j) acc[(j) >> 2][(j) & 3] = MFMA16(F.a[(j) & 3], F.w[(j) >> 2], acc[(j) >> 2][(j) & 3])
#define FN_G(g, C, R, rslot, ks, DM_, t, dslot) do { FN_MF(C, 2 * (g)); FN_RD(R, g, rslot, ks); FN_MF(C, 2 * (g) + 1); if (DM_) FN_DMA_P(g, t, dslot); FN_PACE(); SBAR(); } while (0)
#define FN_PHASE(C, R, rslot, ks, DM_, t, dslot) do { FN_G(0, C, R, rslot, ks, DM_, t, dslot); FN_G(1, C, R, rslot, ks, DM_, t, dslot); FN_G(2, C, R, rslot, ks, DM_, t, dslot); \
        FN_G(3, C, R, rslot, ks, DM_, t, dslot); FN_G(4, C, R, rslot, ks, DM_, t, dslot); FN_G(5, C, R, rslot, ks, DM_, t, dslot); FN_G(6, C, R, rslot, ks, DM_, t, dslot); \
        FN_G(7, C, R, rslot, ks, DM_, t, dslot); } while (0)
    __builtin_amdgcn_s_setprio(FN_PRIO);
    WAIT_BAR(24);
    FN_READ(X, 0, 0);
    int sl = 0, sn = FN_STAGE;
    SBAR();
    for (int t = 0; t < 31; ++t) {
        FN_PHASE(X, Yf, sl, 1, false, 0, 0);
        WAIT_BAR(16); SBAR();
        const int tn = t + 4 < 31 ? t + 4 : 31;
        FN_PHASE(Yf, X, sn, 0, true, tn, sl);
        sl = sn; sn = (sn + FN_STAGE) & (4 * FN_STAGE - 1);
    }
    FN_PHASE(X, Yf, 3 * FN_STAGE, 1, false, 0, 0);
#pragma unroll
    for (int j = 0; j < 16; ++j) FN_MF(Yf, j);
    u16* yb = Y + (size_t)c * SEQ * DM + (size_t)(mt * 128 + 64 * wm + fr) * DM + nt * 128 + 64 * wn + 16 * (fq & 1) + 8 * (fq >> 1);
#pragma unroll
    for (int m = 0; m < 4; ++m)
#pragma unroll
        for (int i = 0; i < 2; ++i) *(u32x4*)(yb + (size_t)m * 16 * DM + 32 * i) = swap16(pack4(acc[m][2 * i]), pack4(acc[m][2 * i + 1]));
}

DI float qnorm2(bf16x8 q0, bf16x8 q1) {
    float qq = 0.f;
#pragma unroll
    for (int j = 0; j < 8; ++j) { const float a = __uint_as_float((unsigned)(u16)q0[j] << 16), b = __uint_as_float((unsigned)(u16)q1[j] << 16); qq = fmaf(a, a, qq); qq = fmaf(b, b, qq); }
    return qq + __shfl_xor(qq, 32);
}
DI void attn_store(u16* op, const f32x16& o, float l) {
    const float rl = 1.0f / l; u32x4 w0, w1;
#pragma unroll
    for (int i = 0; i < 4; ++i) { w0[i] = cvtpk(o[2 * i] * rl, o[2 * i + 1] * rl); w1[i] = cvtpk(o[8 + 2 * i] * rl, o[9 + 2 * i] * rl); }
    *(u32x4*)op = w0; *(u32x4*)(op + 8) = w1;
}
DI bf16x8 attn_sel(int lane) {
    const short v = (((lane >> 2) & 1) == ((lane >> 4) & 1)) ? (short)0x3f80 : (short)0;
    return (bf16x8){v, v, v, v, v, v, v, v};
}
DI void attn_slow(const char* kh, const char* vh, const char* qp, int lane, u16* op) {
    const bf16x8 q0 = *(const bf16x8*)qp, q1 = *(const bf16x8*)(qp + 1024), sel = attn_sel(lane);
    float mx = -INFINITY;
    const f32x16 z = f32x16{};
#pragma unroll 1
    for (int j = 0; j < 64; ++j) {
        const bf16x8 a = *(const bf16x8*)(kh + j * 2048 + lane * 16), b = *(const bf16x8*)(kh + j * 2048 + 1024 + lane * 16);
        f32x16 s = MFMA32(a, q0, z); s = MFMA32(b, q1, s);
#pragma unroll
        for (int r = 0; r < 16; ++r) mx = fmaxf(mx, s[r]);
    }
    mx = fmaxf(mx, __shfl_xor(mx, 32));
    f32x16 negm, o = f32x16{}; f32x4 l = f32x4{};
#pragma unroll
    for (int r = 0; r < 16; ++r) negm[r] = -mx;
#pragma unroll 1
    for (int j = 0; j < 64; ++j) {
        const bf16x8 a = *(const bf16x8*)(kh + j * 2048 + lane * 16), b = *(const bf16x8*)(kh + j * 2048 + 1024 + lane * 16);
        const bf16x8 v0 = *(const bf16x8*)(vh + j * 2048 + lane * 16), v1 = *(const bf16x8*)(vh + j * 2048 + 1024 + lane * 16);
        f32x16 s = MFMA32(a, q0, negm); s = MFMA32(b, q1, s);
        u32x4 p0, p1;
#pragma unroll
        for (int r = 0; r < 16; ++r) s[r] = __builtin_amdgcn_exp2f(s[r]);
#pragma unroll
        for (int r = 0; r < 4; ++r) { p0[r] = cvtpk(s[2 * r], s[2 * r + 1]); p1[r] = cvtpk(s[8 + 2 * r], s[9 + 2 * r]); }
        o = MFMA32(v0, __builtin_bit_cast(bf16x8, p0), o); l = MFMA16(sel, __builtin_bit_cast(bf16x8, p0), l);
        o = MFMA32(v1, __builtin_bit_cast(bf16x8, p1), o); l = MFMA16(sel, __builtin_bit_cast(bf16x8, p1), l);
    }
    attn_store(op, o, l[0]);
}

DI void attn_role(LAS char* smem, int wid, int lane, const u16* __restrict__ Qi, const u16* __restrict__ Ki, const u16* __restrict__ Vi,
                  const float* __restrict__ kmaxp, u16* __restrict__ O) {
    const int xcd = blockIdx.x & 7, idx = blockIdx.x >> 3;
    const int hd = xcd * 4 + (idx >> 3), qb = idx & 7;
    const char* kh = (const char*)Ki + (size_t)hd * 131072;
    const char* vh = (const char*)Vi + (size_t)hd * 131072;
    const unsigned lds0 = (unsigned)(size_t)smem + AT_BASE + wid * 1024, voff = lane * 16 + wid * 1024;
#define AT_DMA(T, slot) do { dma(voff, kh + 2048 + (size_t)(T) * 4096, lds0 + (slot)); dma(voff, vh + (size_t)(T) * 4096, lds0 + (slot) + 4096); } while (0)
    const int qt = qb * 8 + wid * 2;
    const char* qp = (const char*)Qi + (size_t)hd * 131072 + qt * 2048 + lane * 16;
    const bf16x8 qa0 = *(const bf16x8*)qp, qa1 = *(const bf16x8*)(qp + 1024), qb0 = *(const bf16x8*)(qp + 2048), qb1 = *(const bf16x8*)(qp + 3072);
    bf16x8 kA0 = *(const bf16x8*)(kh + lane * 16), kA1 = *(const bf16x8*)(kh + 1024 + lane * 16), kB0, kB1, vA0, vA1, vB0, vB1;
    float km = kmaxp[hd * 32 + (lane & 31)];
    AT_DMA(0, 0); AT_DMA(1, AT_STAGE); AT_DMA(2, 2 * AT_STAGE);
#pragma unroll
    for (int o = 16; o >= 1; o >>= 1) km = fmaxf(km, __shfl_xor(km, o));
    const float ma = sqrtf(qnorm2(qa0, qa1) * km) * 1.01f - 32.f, mb = sqrtf(qnorm2(qb0, qb1) * km) * 1.01f - 32.f;
    const bf16x8 sel = attn_sel(lane);
    f32x16 nma, nmb, oa = f32x16{}, ob = f32x16{}, sA, sB; f32x4 la = f32x4{}, lb = f32x4{};
#pragma unroll
    for (int r = 0; r < 16; ++r) { nma[r] = -ma; nmb[r] = -mb; }
    u32x4 p0, p1 = u32x4{};
    vB1 = bf16x8{};
    const LAS char* fb = smem + AT_BASE + lane * 16;
    sA = MFMA32(kA0, qa0, nma); sA = MFMA32(kA1, qa1, sA);
    __builtin_amdgcn_s_setprio(AT_PRIO);
#define EX(S_, i) S_[i] = __builtin_amdgcn_exp2f(S_[i])
#define BC(x) __builtin_bit_cast(bf16x8, x)
#define AT_STEP(SC, SN, K0, K1, QN0, QN1, NMN, OC, LC, VC0, OP, LP, VP1, RD0, RD1, RD2, RD3) do { \
        SN = MFMA32(K0, QN0, NMN); SBAR(); RD0; EX(SC, 0); EX(SC, 1); EX(SC, 2); EX(SC, 3); SBAR(); \
        OP = MFMA32(VP1, BC(p1), OP); PIN(OP); SBAR(); RD1; EX(SC, 4); EX(SC, 5); EX(SC, 6); EX(SC, 7); p0[0] = cvtpk(SC[0], SC[1]); p0[1] = cvtpk(SC[2], SC[3]); SBAR(); \
        LP = MFMA16(sel, BC(p1), LP); PIN(LP); SBAR(); p0[2] = cvtpk(SC[4], SC[5]); p0[3] = cvtpk(SC[6], SC[7]); EX(SC, 8); SBAR(); \
        SN = MFMA32(K1, QN1, SN); SBAR(); RD2; EX(SC, 9); EX(SC, 10); EX(SC, 11); EX(SC, 12); SBAR(); \
        OC = MFMA32(VC0, BC(p0), OC); PIN(OC); SBAR(); RD3; EX(SC, 13); EX(SC, 14); EX(SC, 15); p1[0] = cvtpk(SC[8], SC[9]); SBAR(); \
        LC = MFMA16(sel, BC(p0), LC); PIN(LC); SBAR(); p1[1] = cvtpk(SC[10], SC[11]); p1[2] = cvtpk(SC[12], SC[13]); p1[3] = cvtpk(SC[14], SC[15]); SBAR(); } while (0)
#define AT_U_QB_ODD(sl_)  AT_STEP(sB, sA, kA0, kA1, qa0, qa1, nma, ob, lb, vB0, oa, la, vB1, kB0 = ldsr128(fb + (sl_)), kB1 = ldsr128(fb + (sl_) + 1024), vA0 = ldsr128(fb + (sl_) + 4096), vA1 = ldsr128(fb + (sl_) + 5120))
#define AT_U_QA_EVEN()    AT_STEP(sA, sB, kA0, kA1, qb0, qb1, nmb, oa, la, vA0, ob, lb, vB1, , , , )
#define AT_U_QB_EVEN(sl_) AT_STEP(sB, sA, kB0, kB1, qa0, qa1, nma, ob, lb, vA0, oa, la, vA1, kA0 = ldsr128(fb + (sl_) + 2048), kA1 = ldsr128(fb + (sl_) + 3072), vB0 = ldsr128(fb + (sl_) + 6144), vB1 = ldsr128(fb + (sl_) + 7168))
#define AT_U_QA_ODD()     AT_STEP(sA, sB, kB0, kB1, qb0, qb1, nmb, oa, la, vB0, ob, lb, vA1, , , , )
    WAIT_BAR(4);
    STAMP(1, 1);
    AT_DMA(3, 3 * AT_STAGE);
    kB0 = ldsr128(fb); kB1 = ldsr128(fb + 1024); vA0 = ldsr128(fb + 4096); vA1 = ldsr128(fb + 5120);
    SBAR();
    AT_U_QA_EVEN(); AT_U_QB_EVEN(0); AT_U_QA_ODD();
    int sl = AT_STAGE;
    for (int T = 1; T < 32; ++T) {
        WAIT_BAR(4); SBAR();
        const int Tn = T + 3 < 31 ? T + 3 : 31;
        AT_DMA(Tn, (sl + 3 * AT_STAGE) & (4 * AT_STAGE - 1)); SBAR();
        AT_U_QB_ODD(sl); AT_U_QA_EVEN(); AT_U_QB_EVEN(sl); AT_U_QA_ODD();
        sl = (sl + AT_STAGE) & (4 * AT_STAGE - 1);
    }
    oa = MFMA32(vB1, BC(p1), oa); la = MFMA16(sel, BC(p1), la);
#pragma unroll
    for (int r = 0; r < 16; ++r) EX(sB, r);
#pragma unroll
    for (int r = 0; r < 4; ++r) { p0[r] = cvtpk(sB[2 * r], sB[2 * r + 1]); p1[r] = cvtpk(sB[8 + 2 * r], sB[9 + 2 * r]); }
    ob = MFMA32(vB0, BC(p0), ob); lb = MFMA16(sel, BC(p0), lb);
    ob = MFMA32(vB1, BC(p1), ob); lb = MFMA16(sel, BC(p1), lb);
    STAMP(1, 2);
    bool bada = !(la[0] >= 7.8886e-31f && la[0] <= 3.0e38f), badb = !(lb[0] >= 7.8886e-31f && lb[0] <= 3.0e38f);
#pragma unroll
    for (int r = 0; r < 16; ++r) { bada |= !(fabsf(oa[r]) <= 3.0e38f); badb |= !(fabsf(ob[r]) <= 3.0e38f); }
    const int r = lane & 31, qrow = qt * 32 + ((r & 19) | ((r & 4) << 1) | ((r & 8) >> 1));
    u16* op = O + (size_t)qrow * 1024 + hd * 32 + (lane >> 5) * 16;
    const int badm = (__any(bada) ? 1 : 0) | (__any(badb) ? 2 : 0);
    attn_store(op, oa, la[0]); attn_store(op + 32 * 1024, ob, lb[0]);
    if (__builtin_expect(badm != 0, 0)) {
#pragma unroll 1
        for (int i = 0; i < 2; ++i) if ((badm >> i) & 1) attn_slow(kh, vh, qp + i * 2048, lane, op + i * 32 * 1024);
    }
    STAMP(1, 3);
}

__global__ __launch_bounds__(512, 2) void k2_mix(const u16* __restrict__ Qi, const u16* __restrict__ Ki, const u16* __restrict__ Vi,
                                                  const float* __restrict__ kmaxp, u16* __restrict__ O,
                                                  const u16* __restrict__ wsr, const u16* __restrict__ wsi, const u16* __restrict__ abT, u16* __restrict__ Y) {
    __shared__ __attribute__((aligned(1024))) char smem_[4 * FN_STAGE + 4 * AT_STAGE];
    LAS char* smem = (LAS char*)smem_;
    const int tid = threadIdx.x, lane = tid & 63, wid = __builtin_amdgcn_readfirstlane(tid >> 6);
    STAMP(1, 0);
    if (wid < 4) attn_role(smem, wid, lane, Qi, Ki, Vi, kmaxp, O);
    else fnet_role(smem, wid - 4, lane, wsr, wsi, abT, Y);
}

constexpr int FI_STAGE = 24576;
struct FiFrag { bf16x8 o[4], w[2]; };
__global__ __launch_bounds__(256, 1) void k3_final(const u16* __restrict__ O, const u16* __restrict__ wo, const u16* __restrict__ Y,
                                                    const float* __restrict__ gate, u16* __restrict__ out) {
    __shared__ __attribute__((aligned(1024))) char smem_[6 * FI_STAGE];
    LAS char* smem = (LAS char*)smem_;
    const int tid = threadIdx.x, lane = tid & 63, wid = __builtin_amdgcn_readfirstlane(tid >> 6), wm = wid >> 1, wn = wid & 1;
    const int xcd = blockIdx.x & 7, idx = blockIdx.x >> 3;
    STAMP(3, 0);
    const int mt = (xcd >> 1) * 4 + (idx & 3), nt = (xcd & 1) * 8 + (idx >> 2);
    const char* ob = (const char*)O + (size_t)mt * 128 * 2048;
    const char* wb = (const char*)wo + nt * 128;
    const unsigned voffO = (8 * wid + (lane >> 3)) * 2048 + (((lane & 7) ^ ((4 * wid + (lane >> 4)) & 7)) * 16);
    const unsigned voffW = (8 * wid + (lane >> 3)) * 2048 + (((lane & 7) ^ ((((lane >> 4) & 1) | ((wid & 1) << 1)) << 1)) * 16);
    const unsigned lds0 = (unsigned)(size_t)smem + wid * 1024;
#define FI_DMA(t, slot) do { \
        _Pragma("unroll") for (int i_ = 0; i_ < 4; ++i_) dma(voffO + i_ * 32 * 2048, ob + (size_t)(t) * 128, lds0 + (slot) + i_ * 4096); \
        _Pragma("unroll") for (int i_ = 0; i_ < 2; ++i_) dma(voffW + i_ * 32 * 2048, wb + (size_t)(t) * 64 * 2048, lds0 + (slot) + 16384 + i_ * 4096); } while (0)
    FI_DMA(0, 0); FI_DMA(1, FI_STAGE); FI_DMA(2, 2 * FI_STAGE); FI_DMA(3, 3 * FI_STAGE); FI_DMA(4, 4 * FI_STAGE); FI_DMA(5, 5 * FI_STAGE);
    const int fr = lane & 15, fq = lane >> 4;
    const int cpart = ((fq ^ (fr >> 1)) & 7) * 16;
    const int aO = (64 * wm + fr) * 128 + cpart;
    const int kq = fr >> 2, swz = ((kq >> 1) & 1) | ((fq & 1) << 1);
    const int aW = 16384 + (8 * fq + kq) * 128 + (((4 * wn + ((lane & 3) >> 1)) ^ (swz << 1)) * 16) + (lane & 1) * 8;
    f32x4 acc[4][2];
#pragma unroll
    for (int m = 0; m < 4; ++m) { acc[m][0] = f32x4{}; acc[m][1] = f32x4{}; }
    FiFrag X, Yf;
#define FI_READ(F, slot, ks) do { const LAS char* sb_ = smem + (slot); \
        F.w[0] = ldstr(sb_ + aW + (ks) * 4096, 512); F.w[1] = ldstr(sb_ + (aW ^ 32) + (ks) * 4096, 512); \
        _Pragma("unroll") for (int m_ = 0; m_ < 4; ++m_) F.o[m_] = ldsr128(sb_ + (aO ^ ((ks) * 64)) + m_ * 2048); } while (0)
#define FI_MMA(F) do { _Pragma("unroll") for (int m_ = 0; m_ < 4; ++m_) { acc[m_][0] = MFMA16(F.w[0], F.o[m_], acc[m_][0]); acc[m_][1] = MFMA16(F.w[1], F.o[m_], acc[m_][1]); } } while (0)
    WAIT_BAR(30);
    STAMP(3, 1);
    FI_READ(X, 0, 0);
    int sl = 0, sn = FI_STAGE;
    SBAR();
    for (int t = 0; t < 10; ++t) {
        FI_READ(Yf, sl, 1); FI_MMA(X); SBAR();
        WAIT_BAR(24); SBAR();
        FI_DMA(t + 6, sl); SBAR();
        FI_READ(X, sn, 0); FI_MMA(Yf); SBAR();
        sl = sn; sn = (sn == 5 * FI_STAGE) ? 0 : sn + FI_STAGE;
    }
#define FI_TAIL(S0, S1, N) do { FI_READ(Yf, (S0) * FI_STAGE, 1); FI_MMA(X); SBAR(); WAIT_BAR(N); SBAR(); FI_READ(X, (S1) * FI_STAGE, 0); FI_MMA(Yf); SBAR(); } while (0)
    FI_TAIL(4, 5, 24); FI_TAIL(5, 0, 18); FI_TAIL(0, 1, 12); FI_TAIL(1, 2, 6); FI_TAIL(2, 3, 0);
    FI_READ(Yf, 3 * FI_STAGE, 1); FI_MMA(X); SBAR();
    FI_MMA(Yf);
    STAMP(3, 2);
    const int col = nt * 64 + 32 * wn + 16 * (fq & 1) + 8 * (fq >> 1);
#pragma unroll
    for (int m = 0; m < 4; ++m) {
        const int t = mt * 128 + 64 * wm + 16 * m + fr;
        const float g = gate[t], g1 = 1.f - g;
        const u32x4 y0 = *(const u32x4*)(Y + (size_t)t * DM + col), y1 = *(const u32x4*)(Y + (size_t)SEQ * DM + (size_t)t * DM + col);
        float v[8];
#pragma unroll
        for (int j = 0; j < 4; ++j) {
            auto r = __builtin_amdgcn_permlane16_swap(__float_as_uint(acc[m][0][j]), __float_as_uint(acc[m][1][j]), false, false);
            v[j] = __uint_as_float(r[0]); v[4 + j] = __uint_as_float(r[1]);
        }
        u32x4 w;
#pragma unroll
        for (int j = 0; j < 4; ++j) {
            const float lo = g * (bflo(y0[j]) - bflo(y1[j])) + g1 * v[2 * j], hi = g * (bfhi(y0[j]) - bfhi(y1[j])) + g1 * v[2 * j + 1];
            w[j] = cvtpk(lo, hi);
        }
        *(u32x4*)(out + (size_t)t * DM + col) = w;
    }
    STAMP(3, 3);
#undef FI_DMA
#undef FI_READ
#undef FI_MMA
#undef FI_TAIL
}

extern "C" void kernel_launch(void* const* d_in, const int* in_sizes, int n_in, void* d_out, int out_size, void* d_ws, size_t ws_size, hipStream_t stream) {
    const u16* x = (const u16*)d_in[0]; const u16* wsr = (const u16*)d_in[1]; const u16* wsi = (const u16*)d_in[2];
    const u16* wdr = (const u16*)d_in[3]; const u16* wdi = (const u16*)d_in[4]; const u16* wqkv = (const u16*)d_in[6];
    const u16* wo = (const u16*)d_in[7]; const u16* gw = (const u16*)d_in[8];
    char* ws = (char*)d_ws;
    constexpr size_t MB = 1u << 20;
    u16* Qi = (u16*)(ws); u16* Ki = (u16*)(ws + 4 * MB); u16* Vi = (u16*)(ws + 8 * MB); u16* abT = (u16*)(ws + 12 * MB);
    u16* O = (u16*)(ws + 20 * MB); u16* Y = (u16*)(ws + 24 * MB); float* gate = (float*)(ws + 32 * MB); float* kmaxp = (float*)(ws + 32 * MB + 65536);
    k1_proj<<<256, 512, 0, stream>>>(x, wdr, wdi, wqkv, gw, Qi, Ki, Vi, abT, gate, kmaxp);
    k2_mix<<<256, 512, 0, stream>>>(Qi, Ki, Vi, kmaxp, O, wsr, wsi, abT, Y);
    k3_final<<<256, 256, 0, stream>>>(O, wo, Y, gate, (u16*)d_out);
}
```

```cpp
#include <hip/hip_runtime.h>
#include <hip/hip_bf16.h>

typedef unsigned short u16;
typedef short bf16x8 __attribute__((ext_vector_type(8)));
typedef short s16x4 __attribute__((ext_vector_type(4)));
typedef float f32x2 __attribute__((ext_vector_type(2)));
typedef float f32x4 __attribute__((ext_vector_type(4)));
typedef float f32x16 __attribute__((ext_vector_type(16)));
typedef unsigned u32x4 __attribute__((ext_vector_type(4)));
typedef unsigned u32x2 __attribute__((ext_vector_type(2)));
typedef __bf16 bf16x2_t __attribute__((ext_vector_type(2)));
#define LAS __attribute__((address_space(3)))
#define GAS __attribute__((address_space(1)))
#define DI __device__ __forceinline__
#define SBAR() __builtin_amdgcn_sched_barrier(0)
#define PIN(x) asm volatile("" : "+v"(x))
#define MFMA16(a, b, c) __builtin_amdgcn_mfma_f32_16x16x32_bf16(a, b, c, 0, 0, 0)
#define MFMA32(a, b, c) __builtin_amdgcn_mfma_f32_32x32x16_bf16(a, b, c, 0, 0, 0)
#define WAIT_BAR(N) asm volatile("s_waitcnt vmcnt(" #N ") lgkmcnt(0)\n\ts_barrier" ::: "memory")
#define LGKM_BAR() asm volatile("s_waitcnt lgkmcnt(0)\n\ts_barrier" ::: "memory")
#define RLX_AGENT __ATOMIC_RELAXED, __HIP_MEMORY_SCOPE_AGENT
#define BC(x) __builtin_bit_cast(bf16x8, x)

constexpr int SEQ = 2048, DM = 1024;
constexpr unsigned MB = 1u << 20;
constexpr unsigned WS_CNT_BYTES = 16384, WS_GATE = 65536, WS_Q = 1 * MB, WS_K = 5 * MB, WS_V = 9 * MB, WS_AB = 13 * MB, WS_O = 21 * MB, WS_Y = 25 * MB, WS_END = 33 * MB;

#define STAMP(kid, i)
#define TICK(k, i)

DI unsigned cvtpk(float lo, float hi) { f32x2 v = {lo, hi}; bf16x2_t b = __builtin_convertvector(v, bf16x2_t); return __builtin_bit_cast(unsigned, b); }
DI void dma(unsigned voff, const char* sbase, unsigned l) {
    asm volatile("s_mov_b32 m0, %2\n\ts_nop 0\n\tglobal_load_lds_dwordx4 %0, %1" :: "v"(voff), "s"(sbase), "s"(l) : "memory"); }
DI void dma_dev(unsigned voff, const char* sbase, unsigned l) {
    asm volatile("s_mov_b32 m0, %2\n\ts_nop 0\n\tglobal_load_lds_dwordx4 %0, %1 sc1" :: "v"(voff), "s"(sbase), "s"(l) : "memory"); }
DI void ldsw128(LAS char* p, u32x4 v) { *(LAS u32x4*)p = v; }
DI bf16x8 ldsr128(const LAS char* p) { return *(const LAS bf16x8*)p; }
DI bf16x8 ldstr(const LAS char* p, int off2) {
    s16x4 a = __builtin_amdgcn_ds_read_tr16_b64_v4i16((LAS s16x4*)p);
    s16x4 b = __builtin_amdgcn_ds_read_tr16_b64_v4i16((LAS s16x4*)(p + off2));
    return (bf16x8){a[0], a[1], a[2], a[3], b[0], b[1], b[2], b[3]};
}
DI int lane_now() { int l; asm volatile("v_mbcnt_lo_u32_b32 %0, -1, 0\n\tv_mbcnt_hi_u32_b32 %0, -1, %0" : "=v"(l)); return l; }
DI float bflo(unsigned u) { return __uint_as_float(u << 16); }
DI float bfhi(unsigned u) { return __uint_as_float(u & 0xffff0000u); }
DI u32x4 swap16(u32x2 a, u32x2 b) {
    auto r0 = __builtin_amdgcn_permlane16_swap(a[0], b[0], false, false);
    auto r1 = __builtin_amdgcn_permlane16_swap(a[1], b[1], false, false);
    return (u32x4){r0[0], r1[0], r0[1], r1[1]};
}
DI u32x2 pack4(f32x4 v) { return (u32x2){cvtpk(v[0], v[1]), cvtpk(v[2], v[3])}; }
typedef __amdgpu_buffer_rsrc_t rsrc_t;
DI void st_wt(rsrc_t r, unsigned off, u32x4 v) { __builtin_amdgcn_raw_buffer_store_b128(v, r, off, 0, 16); }
DI u32x4 ld_dev(rsrc_t r, unsigned off) { return __builtin_amdgcn_raw_buffer_load_b128(r, off, 0, 16); }
DI void wait_count(const unsigned* word, unsigned want) {
    const unsigned long long t0 = __builtin_amdgcn_s_memrealtime();
    while ((unsigned)__builtin_amdgcn_readfirstlane(__hip_atomic_load((const GAS unsigned*)word, RLX_AGENT)) < want) {
        if (__builtin_amdgcn_s_memrealtime() - t0 > 2000000ull) break;
        __builtin_amdgcn_s_sleep(1);
    }
}

#define ABL_FDMA 0
#define ABL_FMFMA 0
#define ABL_ATT 0
#define FN_PRIO 1
#define PF_MODE 0
#define PF_SLEEP 8
constexpr int K1_STAGE = 53248, K1_A = 0, K1_WD = 32768, K1_WQ = 40960;
constexpr int AT_STAGE = 8192, FB_BASE = 32768, FW_STAGE = 32768, FW_TOP = 131072;
struct K1Frag { bf16x8 x[4], wd[2], wqk[2], wv; };
struct FnFrag { bf16x8 w[4], a[4]; };

DI void attn_store(rsrc_t rs, unsigned off, const f32x16& o, float l) {
    const float rl = 1.0f / l; u32x4 w0, w1;
#pragma unroll
    for (int i = 0; i < 4; ++i) { w0[i] = cvtpk(o[2 * i] * rl, o[2 * i + 1] * rl); w1[i] = cvtpk(o[8 + 2 * i] * rl, o[9 + 2 * i] * rl); }
    st_wt(rs, off, w0); st_wt(rs, off + 256, w1);
}
DI bf16x8 attn_sel(int lane) {
    const short v = (((lane >> 2) & 1) == ((lane >> 4) & 1)) ? (short)0x3f80 : (short)0;
    return (bf16x8){v, v, v, v, v, v, v, v};
}
DI float max16(const f32x16& s) {
    float a = fmaxf(fmaxf(s[0], s[1]), s[2]), b = fmaxf(fmaxf(s[3], s[4]), s[5]);
#pragma unroll
    for (int r = 6; r < 16; r += 4) { a = fmaxf(fmaxf(a, s[r]), s[r + 1]); if (r + 3 < 16) b = fmaxf(fmaxf(b, s[r + 2]), s[r + 3]); }
    return fmaxf(a, b);
}
DI void attn_slow(rsrc_t rs, const char* kh, const char* vh, bf16x8 q0, bf16x8 q1, int lane, unsigned ooff) {
    __builtin_amdgcn_fence(__ATOMIC_ACQUIRE, "agent");
    const bf16x8 sel = attn_sel(lane);
    float mx = -INFINITY;
    const f32x16 z = f32x16{};
#pragma unroll 1
    for (int j = 0; j < 64; ++j) {
        const bf16x8 a = *(const bf16x8*)(kh + j * 2048 + lane * 16), b = *(const bf16x8*)(kh + j * 2048 + 1024 + lane * 16);
        f32x16 s = MFMA32(a, q0, z); s = MFMA32(b, q1, s);
        mx = fmaxf(mx, max16(s));
    }
    mx = fmaxf(mx, __shfl_xor(mx, 32));
    f32x16 negm, o = f32x16{}; f32x4 l = f32x4{};
#pragma unroll
    for (int r = 0; r < 16; ++r) negm[r] = -mx;
#pragma unroll 1
    for (int j = 0; j < 64; ++j) {
        const bf16x8 a = *(const bf16x8*)(kh + j * 2048 + lane * 16), b = *(const bf16x8*)(kh + j * 2048 + 1024 + lane * 16);
        const bf16x8 v0 = *(const bf16x8*)(vh + j * 2048 + lane * 16), v1 = *(const bf16x8*)(vh + j * 2048 + 1024 + lane * 16);
        f32x16 s = MFMA32(a, q0, negm); s = MFMA32(b, q1, s);
        u32x4 p0, p1;
#pragma unroll
        for (int r = 0; r < 16; ++r) s[r] = __builtin_amdgcn_exp2f(s[r]);
#pragma unroll
        for (int r = 0; r < 4; ++r) { p0[r] = cvtpk(s[2 * r], s[2 * r + 1]); p1[r] = cvtpk(s[8 + 2 * r], s[9 + 2 * r]); }
        o = MFMA32(v0, BC(p0), o); l = MFMA16(sel, BC(p0), l);
        o = MFMA32(v1, BC(p1), o); l = MFMA16(sel, BC(p1), l);
    }
    attn_store(rs, ooff, o, l[0]);
}

DI void touch8k(const char* p, unsigned voff, unsigned dump) {
    asm volatile("s_mov_b32 m0, %2\n\ts_nop 0\n\tglobal_load_lds_dword %0, %1" :: "v"(voff), "s"(p), "s"(dump) : "memory"); }
DI void main_waves(LAS char* smem, int wid, int lane, int mt, int hd, char* ws, rsrc_t rs, u16* __restrict__ out,
                   const u16* __restrict__ x, const u16* __restrict__ gw, const char* pf0, const char* wsb, const char* pf2) {
    const unsigned dump = (unsigned)(size_t)smem + 3 * K1_STAGE + wid * 256;
    {
        const int grow = 256 * mt + 8 * hd + wid;
        u32x4 gx0, gx1, gw0, gw1;
        asm volatile("global_load_dwordx4 %0, %4, off\n\tglobal_load_dwordx4 %1, %4, off offset:16\n\t"
                     "global_load_dwordx4 %2, %5, off\n\tglobal_load_dwordx4 %3, %5, off offset:16"
                     : "=&v"(gx0), "=&v"(gx1), "=&v"(gw0), "=&v"(gw1)
                     : "v"((const char*)x + (size_t)grow * 2048 + lane * 32), "v"((const char*)gw + lane * 32) : "memory");
        __builtin_amdgcn_s_sleep(PF_SLEEP);
        touch8k(pf0, lane * 128, dump);
        asm volatile("s_waitcnt vmcnt(1)" : "+v"(gx0), "+v"(gx1), "+v"(gw0), "+v"(gw1) :: "memory");
        float s = 0.f;
#pragma unroll
        for (int i = 0; i < 4; ++i) {
            s = fmaf(bflo(gx0[i]), bflo(gw0[i]), s); s = fmaf(bfhi(gx0[i]), bfhi(gw0[i]), s);
            s = fmaf(bflo(gx1[i]), bflo(gw1[i]), s); s = fmaf(bfhi(gx1[i]), bfhi(gw1[i]), s);
        }
#pragma unroll
        for (int o = 32; o >= 1; o >>= 1) s += __shfl_xor(s, o);
        if (lane == 0) __builtin_amdgcn_raw_buffer_store_b32(__float_as_uint(1.f / (1.f + __expf(-s))), rs, WS_GATE + grow * 4, 0, 16);
    }
#define TW(l_) (unsigned)(((hd & 15) * 16 + ((l_) >> 2)) * 4096 + ((l_) & 3) * 128)
    int fr = lane & 15, fq = lane >> 4;
    {
        const int wm = wid >> 1, wn = wid & 1;
        const int cpart = ((fq ^ (fr >> 1)) & 7) * 16;
        const int aA = K1_A + (64 * wm + fr) * 128 + cpart, aD = K1_WD + (32 * wn + fr) * 128 + cpart;
        const int aT0 = (8 * fq + (fr >> 2)) * 64 + ((((lane & 3) >> 1) ^ ((fq & 1) << 1)) * 16) + (lane & 1) * 8;
        const int aQ = K1_WQ + wn * 4096 + aT0, aV = K1_WQ + 8192 + (aT0 ^ (wn * 32));
        f32x4 accQ[2][4], accV[4], accD[2][4];
#pragma unroll
        for (int m = 0; m < 4; ++m) { accQ[0][m] = f32x4{}; accQ[1][m] = f32x4{}; accV[m] = f32x4{}; accD[0][m] = f32x4{}; accD[1][m] = f32x4{}; }
        K1Frag X, Y;
#define K1_RD(F, i, slot, ks) do { const LAS char* sb_ = smem + (slot); \
        if ((i) == 0) F.wqk[0] = ldstr(sb_ + aQ + (ks) * 2048, 256); \
        else if ((i) == 1) { F.x[0] = ldsr128(sb_ + (aA ^ ((ks) * 64))); } \
        else if ((i) == 2) F.wqk[1] = ldstr(sb_ + (aQ ^ 32) + (ks) * 2048, 256); \
        else if ((i) == 3) F.wv = ldstr(sb_ + aV + (ks) * 2048, 256); \
        else if ((i) == 4) { F.wd[0] = ldsr128(sb_ + (aD ^ ((ks) * 64))); F.wd[1] = ldsr128(sb_ + (aD ^ ((ks) * 64)) + 2048); } \
        else if ((i) < 8) F.x[(i) - 4] = ldsr128(sb_ + (aA ^ ((ks) * 64)) + ((i) - 4) * 2048); } while (0)
#define K1_MF(F, j) do { constexpr int m_ = (j) / 5, r_ = (j) % 5; \
        if (r_ == 0) accQ[0][m_] = MFMA16(F.wqk[0], F.x[m_], accQ[0][m_]); \
        else if (r_ == 1) accQ[1][m_] = MFMA16(F.wqk[1], F.x[m_], accQ[1][m_]); \
        else if (r_ == 2) accV[m_] = MFMA16(F.x[m_], F.wv, accV[m_]); \
        else if (r_ == 3) accD[0][m_] = MFMA16(F.x[m_], F.wd[0], accD[0][m_]); \
        else accD[1][m_] = MFMA16(F.x[m_], F.wd[1], accD[1][m_]); } while (0)
#define K1_G(g, C, R, rslot, ks) do { K1_MF(C, 2 * (g)); K1_RD(R, g, rslot, ks); K1_MF(C, 2 * (g) + 1); SBAR(); } while (0)
#define K1_PHASE(C, R, rslot, ks) do { K1_G(0, C, R, rslot, ks); K1_G(1, C, R, rslot, ks); K1_G(2, C, R, rslot, ks); K1_G(3, C, R, rslot, ks); K1_G(4, C, R, rslot, ks); \
        K1_G(5, C, R, rslot, ks); K1_G(6, C, R, rslot, ks); K1_G(7, C, R, rslot, ks); K1_G(8, C, R, rslot, ks); K1_G(9, C, R, rslot, ks); } while (0)
        LGKM_BAR();
        if (wid == 0) STAMP(0, 1);
#pragma unroll
        for (int i = 0; i < 8; ++i) K1_RD(X, i, 0, 0);
        int sl = 0, sn = K1_STAGE;
        SBAR();
        for (int t = 0; t < 15; ++t) {
            K1_PHASE(X, Y, sl, 1);
            if (t == 14) asm volatile("s_waitcnt vmcnt(0)" ::: "memory");
            LGKM_BAR(); SBAR();
            TICK(0, t);
            if (t == 7 + 2 * wid) { const int l_ = lane_now(); touch8k(wsb + ((mt + wid) & 7) * 512, TW(l_), dump); }
            SBAR();
            K1_PHASE(Y, X, sn, 0);
            sl = sn; sn = (sn == 2 * K1_STAGE) ? 0 : sn + K1_STAGE;
        }
        K1_PHASE(X, Y, 0, 1);
        LGKM_BAR(); SBAR();
        K1_MF(Y, 0); K1_MF(Y, 1); K1_MF(Y, 2); K1_MF(Y, 3); K1_MF(Y, 4); K1_MF(Y, 5); K1_MF(Y, 6); K1_MF(Y, 7); K1_MF(Y, 8); K1_MF(Y, 9);
        K1_MF(Y, 10); K1_MF(Y, 11); K1_MF(Y, 12); K1_MF(Y, 13); K1_MF(Y, 14); K1_MF(Y, 15); K1_MF(Y, 16); K1_MF(Y, 17); K1_MF(Y, 18); K1_MF(Y, 19);
        if (wid == 0) STAMP(0, 2);
        lane = lane_now(); fr = lane & 15; fq = lane >> 4;
        const int s0 = 256 * mt + 64 * wm;
        const float qs = wn ? 1.0f : 0.17677669529663687f * 1.4426950408889634f;
#pragma unroll
        for (int m = 0; m < 4; ++m) {
            const u32x4 w = swap16(pack4(accQ[0][m] * qs), pack4(accQ[1][m] * qs));
            const int r = (16 * m + fr) & 31, rho = (r & 19) | ((r & 4) << 1) | ((r & 8) >> 1), j = 2 * wm + (m >> 1);
            const int in = (fq & 1) * 1024 + ((fq >> 1) * 32 + rho) * 16;
            if (wn) { st_wt(rs, WS_K + hd * 131072 + (8 * mt + j) * 2048 + in, w); ldsw128(smem + wm * AT_STAGE + (m >> 1) * 2048 + in, w); }
            else { ldsw128(smem + FW_TOP - 2 * FW_STAGE + j * 2048 + in, w); }
        }
        const int rv = 8 * (fr >> 2) + 4 * wn + (fr & 3);
#pragma unroll
        for (int i = 0; i < 2; ++i) {
            const int in = (fq & 1) * 1024 + ((fq >> 1) * 32 + rv) * 16;
            const u32x4 w = swap16(pack4(accV[2 * i]), pack4(accV[2 * i + 1]));
            st_wt(rs, WS_V + hd * 131072 + ((s0 >> 5) + i) * 2048 + in, w);
            ldsw128(smem + ((wm + i) & 3) * AT_STAGE + 6144 - i * 2048 + in, w);
#pragma unroll
            for (int n = 0; n < 2; ++n) {
                const u32x4 d = swap16(pack4(accD[n][2 * i]), pack4(accD[n][2 * i + 1]));
                const int el = 32 * wn + 16 * n + fr, ch = 4 * i + 2 * (fq & 1) + (fq >> 1);
                st_wt(rs, WS_AB + ((64 * hd + el) * 2048 + s0) * 2 + ch * 16, d);
                ldsw128(smem + FB_BASE + wm * 8192 + el * 128 + ((ch ^ (fr >> 1)) * 16), d);
            }
        }
        if (wid == 0) asm volatile("buffer_inv sc1" ::: "memory");
        LGKM_BAR();
        if (wid == 0) STAMP(0, 3);
    }
    {
        const char* kh = ws + WS_K + hd * 131072;
        const char* vh = ws + WS_V + hd * 131072;
        const int qt = mt * 8 + wid;
        const bf16x8 sel = attn_sel(lane);
        const LAS char* fb = smem + lane * 16;
        const bf16x8 q0 = ldsr128(fb + FW_TOP - 2 * FW_STAGE + wid * 2048), q1 = ldsr128(fb + FW_TOP - 2 * FW_STAGE + wid * 2048 + 1024);
        bf16x8 kA0, kA1, kB0, kB1, vA0, vA1, vB0, vB1 = bf16x8{};
        f32x16 negm, o = f32x16{}, sA, sB; f32x4 l = f32x4{};
        u32x4 p0, p1 = u32x4{};
        unsigned sink = 0;
        kA0 = ldsr128(fb); kA1 = ldsr128(fb + 1024); kB0 = ldsr128(fb + 2048); kB1 = ldsr128(fb + 3072); vA0 = ldsr128(fb + 6144); vA1 = ldsr128(fb + 7168);
        LGKM_BAR();
        {
            sA = MFMA32(kA0, q0, o); sA = MFMA32(kA1, q1, sA);
            float mx = max16(sA); mx = fmaxf(mx, __shfl_xor(mx, 32)) + 40.f;
#pragma unroll
            for (int r = 0; r < 16; ++r) { negm[r] = -mx; sA[r] -= mx; }
        }
        if (wid == 0) STAMP(1, 0);
#define EX(S_, i) S_[i] = __builtin_amdgcn_exp2f(S_[i])
#define AT_STEP(SC, SN, K0, K1, VC0, VP1, RD0, RD1, RD2, RD3) do { \
        SN = MFMA32(K0, q0, negm); SBAR(); RD0; EX(SC, 0); EX(SC, 1); EX(SC, 2); EX(SC, 3); SBAR(); \
        o = MFMA32(VP1, BC(p1), o); PIN(o); SBAR(); RD1; EX(SC, 4); EX(SC, 5); EX(SC, 6); EX(SC, 7); p0[0] = cvtpk(SC[0], SC[1]); p0[1] = cvtpk(SC[2], SC[3]); SBAR(); \
        l = MFMA16(sel, BC(p1), l); PIN(l); SBAR(); p0[2] = cvtpk(SC[4], SC[5]); p0[3] = cvtpk(SC[6], SC[7]); EX(SC, 8); SBAR(); \
        SN = MFMA32(K1, q1, SN); SBAR(); RD2; EX(SC, 9); EX(SC, 10); EX(SC, 11); EX(SC, 12); SBAR(); \
        o = MFMA32(VC0, BC(p0), o); PIN(o); SBAR(); RD3; EX(SC, 13); EX(SC, 14); EX(SC, 15); p1[0] = cvtpk(SC[8], SC[9]); SBAR(); \
        l = MFMA16(sel, BC(p0), l); PIN(l); SBAR(); p1[1] = cvtpk(SC[10], SC[11]); p1[2] = cvtpk(SC[12], SC[13]); p1[3] = cvtpk(SC[14], SC[15]); SBAR(); } while (0)
        int sl = AT_STAGE;
        for (int T = 1; T <= 32; ++T) {
            if (T == 2) asm volatile("s_waitcnt vmcnt(0)" ::: "memory");
            LGKM_BAR(); SBAR();
            TICK(1, T);
            if (T == 4 * wid - 8 && T > 0)
                { asm volatile("global_load_dword %0, %1, %2" : "+v"(sink) : "v"(TW(lane)), "s"(wsb + ((mt + wid) & 7) * 512) : "memory");
                  if (wid == 7) asm volatile("global_load_dword %0, %1, %2" : "+v"(sink) : "v"(lane * 128), "s"(pf2) : "memory"); }
            SBAR();
            if (!ABL_ATT) {
            AT_STEP(sA, sB, kB0, kB1, vA0, vB1, kA0 = ldsr128(fb + sl), kA1 = ldsr128(fb + sl + 1024), vB0 = ldsr128(fb + sl + 4096), vB1 = ldsr128(fb + sl + 5120));
            AT_STEP(sB, sA, kA0, kA1, vB0, vA1, kB0 = ldsr128(fb + sl + 2048), kB1 = ldsr128(fb + sl + 3072), vA0 = ldsr128(fb + sl + 6144), vA1 = ldsr128(fb + sl + 7168)); }
            sl = (sl + AT_STAGE) & (4 * AT_STAGE - 1);
        }
        o = MFMA32(vB1, BC(p1), o); l = MFMA16(sel, BC(p1), l);
        if (wid == 0) STAMP(1, 1);
        bool bad = !(l[0] >= 7.8886e-31f && l[0] <= 3.0e38f);
#pragma unroll
        for (int r = 0; r < 16; ++r) bad |= !(fabsf(o[r]) <= 3.0e38f);
        const int r = lane & 31, qrow = qt * 32 + ((r & 19) | ((r & 4) << 1) | ((r & 8) >> 1));
        const unsigned ooff = WS_O + ((qrow >> 4) * 32 + hd) * 1024 + ((lane >> 5) * 32 + (qrow & 15)) * 16;
        if (!ABL_ATT && !ABL_FDMA && __builtin_expect(__any(bad), 0)) attn_slow(rs, kh, vh, q0, q1, lane, ooff);
        else attn_store(rs, ooff, o, l[0]);
        WAIT_BAR(0);
        asm volatile("" :: "v"(sink));
        if (wid == 0) STAMP(1, 2);
    }
    {
        if (wid == 0) { wait_count((const unsigned*)(ws + 8192 + mt * 256), 32u); __builtin_amdgcn_fence(__ATOMIC_ACQUIRE, "wavefront"); STAMP(1, 3); }
        LGKM_BAR();
        const int R0 = mt * 256 + (hd >> 4) * 128 + 16 * wid, nb = (hd & 15) * 64;
        u32x4 of[32];
#pragma unroll
        for (int ks = 0; ks < 32; ++ks) of[ks] = ld_dev(rs, WS_O + ((R0 >> 4) * 32 + ks) * 1024 + lane * 16);
        const int t = R0 + fr, col = nb + 16 * (fq & 1) + 8 * (fq >> 1);
        const int kq = fr >> 2, swz = ((kq >> 1) & 1) | ((fq & 1) << 1);
        const int aW = (8 * fq + kq) * 128 + ((((lane & 3) >> 1) ^ (swz << 1)) * 16) + (lane & 1) * 8;
        f32x4 acc[4];
#pragma unroll
        for (int n = 0; n < 4; ++n) acc[n] = f32x4{};
#pragma unroll
        for (int ks = 0; ks < 32; ++ks) {
            const int kt = ks >> 1;
            const int base = (kt < 4 ? FW_TOP - 2 * FW_STAGE + kt * 8192 : kt == 4 ? FB_BASE : kt < 9 ? FW_TOP + (kt - 5) * 8192 : kt == 9 ? FB_BASE + 8192
                              : kt < 14 ? FW_TOP - FW_STAGE + (kt - 10) * 8192 : FB_BASE + (kt - 12) * 8192) + (ks & 1) * 4096;
#pragma unroll
            for (int n = 0; n < 4; ++n) acc[n] = MFMA16(ldstr(smem + base + (aW ^ (n * 32)), 512), BC(of[ks]), acc[n]);
        }
        const float g = __uint_as_float(__builtin_amdgcn_raw_buffer_load_b32(rs, WS_GATE + t * 4, 0, 16)), g1 = 1.f - g;
#pragma unroll
        for (int i = 0; i < 2; ++i) {
            const u32x4 y0 = ld_dev(rs, WS_Y + (t * DM + col + 32 * i) * 2), y1 = ld_dev(rs, WS_Y + 4 * MB + (t * DM + col + 32 * i) * 2);
            float v[8];
#pragma unroll
            for (int j = 0; j < 4; ++j) {
                auto r = __builtin_amdgcn_permlane16_swap(__float_as_uint(acc[2 * i][j]), __float_as_uint(acc[2 * i + 1][j]), false, false);
                v[j] = __uint_as_float(r[0]); v[4 + j] = __uint_as_float(r[1]);
            }
            u32x4 w;
#pragma unroll
            for (int j = 0; j < 4; ++j)
                w[j] = cvtpk(g * (bflo(y0[j]) - bflo(y1[j])) + g1 * v[2 * j], g * (bfhi(y0[j]) - bfhi(y1[j])) + g1 * v[2 * j + 1]);
            *(u32x4*)(out + (size_t)t * DM + col + 32 * i) = w;
        }
        if (wid == 0) STAMP(1, 4);
    }
}

DI void side_waves(LAS char* smem, int lw, int lane, int mt, int hd, const u16* __restrict__ x, const u16* __restrict__ wsr, const u16* __restrict__ wsi,
                   const u16* __restrict__ wdr, const u16* __restrict__ wdi, const u16* __restrict__ wqkv, const u16* __restrict__ wo,
                   const u16* __restrict__ gw, char* ws, rsrc_t rs) {
    const unsigned lds0 = (unsigned)(size_t)smem + lw * 1024;
    const unsigned rowl = 8 * lw + (lane >> 3), chk = ((lane & 7) ^ ((4 * lw + (lane >> 4)) & 7)) * 16;
    const int c = hd >> 4;
    const char* wb = (const char*)(c ? wsi : wsr) + (size_t)mt * 256 * 4096;
    unsigned vo[10];
    {
        const char* xb = (const char*)x + (size_t)mt * 256 * 2048;
        const char* wdb = (const char*)(c ? wdi : wdr) + (size_t)(hd & 15) * 64 * 2048;
        const char* wqb = (const char*)wqkv + hd * 64 + lw * 16 * 6144;
#pragma unroll
        for (int i = 0; i < 8; ++i) vo[i] = (rowl + 32 * i) * 2048 + chk;
        const unsigned voffW = (lane >> 2) * 6144 + (((lane & 3) ^ ((lane >> 5) << 1)) * 16);
#define K1_DMA(t, slot) do { const char* xa_ = xb + (size_t)(t) * 128; const char* wd_ = wdb + (size_t)(t) * 128; const char* wq_ = wqb + (size_t)(t) * 64 * 6144; \
        _Pragma("unroll") for (int u_ = 0; u_ < 3; ++u_) dma(voffW, wq_ + u_ * 2048, lds0 + (slot) + K1_WQ + u_ * 4096); \
        _Pragma("unroll") for (int i_ = 0; i_ < 2; ++i_) dma(vo[i_], wd_, lds0 + (slot) + K1_WD + i_ * 4096); \
        _Pragma("unroll") for (int i_ = 0; i_ < 8; ++i_) dma(vo[i_], xa_, lds0 + (slot) + K1_A + i_ * 4096); } while (0)
        K1_DMA(0, 0); K1_DMA(1, K1_STAGE); K1_DMA(2, 2 * K1_STAGE);
        WAIT_BAR(26);
        int sl = 0;
        for (int t = 0; t < 13; ++t) {
            WAIT_BAR(13);
            K1_DMA(t + 3, sl);
            sl = (sl == 2 * K1_STAGE) ? 0 : sl + K1_STAGE;
        }
        WAIT_BAR(13);
        WAIT_BAR(0);
#pragma unroll
        for (int i = 0; i < 8; ++i) vo[i] = (rowl + 32 * i) * 4096 + chk;
        vo[8] = vo[0]; vo[9] = vo[1];
#pragma unroll
        for (int s = 0; s < 2; ++s)
#pragma unroll
            for (int i = 0; i < 8; ++i) dma(vo[i], wb + (size_t)((4 * mt + s) & 31) * 128, lds0 + FW_TOP - s * FW_STAGE + i * 4096);
        LGKM_BAR();
        WAIT_BAR(8);
    }
    {
        const char* abb = ws + WS_AB + (size_t)hd * 64 * 4096;
        const char* kh = ws + WS_K + hd * 131072 + (lw & 1) * 1024;
        const char* vh = ws + WS_V + hd * 131072 + (lw & 1) * 1024;
        const unsigned vl = lane * 16;
        const char *bW = wb, *bA = abb; int kmask = 31, kstep = 128, koff = 4 * mt;
#define FN_DMA_P(p, s, fslot, aslot) do { \
        if ((p) < 8) dma(vo[p], bW + (size_t)((koff + (s)) & kmask) * kstep, lds0 + (fslot) + (p) * 4096); \
        else if ((s) >= 4) { \
        if ((p) < 10) dma_dev(vo[p], bA + (size_t)((koff + (s)) & kmask) * kstep, lds0 + FB_BASE + (aslot) + ((p) - 8) * 4096); \
        else if ((p) == 10) dma_dev(vl, kh + ((8 * mt + 2 * (s) + (lw >> 1)) & 63) * 2048, lds0 + (aslot)); \
        else dma_dev(vl, vh + ((8 * mt + 2 * (s) - 1 + (lw >> 1)) & 63) * 2048, lds0 + (aslot) + 4096); } } while (0)
        const int fr = lane & 15, fq = lane >> 4;
        const int cpart = ((fq ^ (fr >> 1)) & 7) * 16;
        const int aW = (64 * lw + fr) * 128 + cpart, aA = FB_BASE + fr * 128 + cpart;
        f32x4 acc[4][4];
#pragma unroll
        for (int m = 0; m < 4; ++m)
#pragma unroll
            for (int n = 0; n < 4; ++n) acc[m][n] = f32x4{};
        FnFrag X, Yf;
#define FN_RD(F, i, slot, bslot, ks) do { \
        if ((i) == 0) F.w[0] = ldsr128(smem + (slot) + (aW ^ ((ks) * 64))); \
        else if ((i) < 5) F.a[(i) - 1] = ldsr128(smem + (bslot) + (aA ^ ((ks) * 64)) + ((i) - 1) * 2048); \
        else F.w[(i) - 4] = ldsr128(smem + (slot) + (aW ^ ((ks) * 64)) + ((i) - 4) * 2048); } while (0)
#define FN_MF(F, j) do { if (!ABL_FMFMA) acc[(j) >> 2][(j) & 3] = MFMA16(F.a[(j) & 3], F.w[(j) >> 2], acc[(j) >> 2][(j) & 3]); else asm volatile("" :: "v"(F.a[(j) & 3]), "v"(F.w[(j) >> 2])); } while (0)
#define FN_G(g, C, R, rslot, rb, ks, P0, s, fslot, aslot) do { FN_MF(C, 2 * (g)); FN_RD(R, g, rslot, rb, ks); FN_MF(C, 2 * (g) + 1); \
        if (!ABL_FDMA && ((g) & 3) != 0 && (P0) + (g) - 1 - ((g) >> 2) < PMAX) FN_DMA_P((P0) + (g) - 1 - ((g) >> 2), s, fslot, aslot); SBAR(); } while (0)
#define FN_PHASE(C, R, rslot, rb, ks, P0, s, fslot, aslot) do { FN_G(0, C, R, rslot, rb, ks, P0, s, fslot, aslot); FN_G(1, C, R, rslot, rb, ks, P0, s, fslot, aslot); \
        FN_G(2, C, R, rslot, rb, ks, P0, s, fslot, aslot); FN_G(3, C, R, rslot, rb, ks, P0, s, fslot, aslot); FN_G(4, C, R, rslot, rb, ks, P0, s, fslot, aslot); \
        FN_G(5, C, R, rslot, rb, ks, P0, s, fslot, aslot); FN_G(6, C, R, rslot, rb, ks, P0, s, fslot, aslot); FN_G(7, C, R, rslot, rb, ks, P0, s, fslot, aslot); } while (0)
#define PMAX 12
        __builtin_amdgcn_s_setprio(FN_PRIO);
#pragma unroll
        for (int i = 0; i < 8; ++i) FN_RD(X, i, FW_TOP, 0, 0);
        LGKM_BAR();
        int f0 = FW_TOP, f1 = FW_TOP - FW_STAGE, f2 = FW_TOP - 2 * FW_STAGE;
        FN_DMA_P(0, 2, f2, 0); FN_DMA_P(1, 2, f2, 0); FN_DMA_P(2, 2, f2, 0); FN_DMA_P(3, 2, f2, 0); FN_DMA_P(4, 2, f2, 0); FN_DMA_P(5, 2, f2, 0);
        SBAR();
        for (int t = 0; t < 31; ++t) {
            if (t == 2) wait_count((const unsigned*)(ws + hd * 256), 8u);
            SBAR();
            FN_PHASE(X, Yf, f0, (t & 3) * 8192, 1, 6, t + 2, f2, ((t + 2) & 3) * 8192);
            if (t < 2) { WAIT_BAR(8); } else { WAIT_BAR(12); }
            SBAR();
            if (t == 1 && lw == 0 && lane == 0) __hip_atomic_fetch_add((GAS unsigned*)(ws + hd * 256), 1u, RLX_AGENT);
            if (t == 29) {
                bW = bA = (const char*)wo + (hd & 15) * 128; kmask = 3; kstep = 320 * 2048; koff = 0;
                const unsigned cw = ((lane & 7) ^ ((((lane >> 4) & 1) | ((lw & 1) << 1)) << 1)) * 16;
#pragma unroll
                for (int i = 0; i < 10; ++i) vo[i] = (rowl + 32 * i) * 2048 + cw;
            }
            SBAR();
            FN_PHASE(Yf, X, f1, ((t + 1) & 3) * 8192, 0, 0, t + 3, f0, 0);
            const int ft = f0; f0 = f1; f1 = f2; f2 = ft;
        }
#undef PMAX
#define PMAX 10
        FN_PHASE(X, Yf, f0, 3 * 8192, 1, 6, 33, f2, 8192);
        WAIT_BAR(10); SBAR();
#pragma unroll
        for (int i = 0; i < 8; ++i) dma(vo[i], bW + (size_t)640 * 2048, lds0 + FW_TOP - FW_STAGE + i * 4096);
#pragma unroll
        for (int i = 0; i < 4; ++i) dma(vo[i], bW + (size_t)896 * 2048, lds0 + FB_BASE + 2 * 8192 + i * 4096);
#pragma unroll
        for (int j = 0; j < 16; ++j) FN_MF(Yf, j);
        const unsigned yb = WS_Y + c * 4 * MB + ((mt * 256 + 64 * lw + fr) * DM + (hd & 15) * 64 + 16 * (fq & 1) + 8 * (fq >> 1)) * 2;
#pragma unroll
        for (int m = 0; m < 4; ++m)
#pragma unroll
            for (int i = 0; i < 2; ++i) st_wt(rs, yb + (m * 16 * DM + 32 * i) * 2, swap16(pack4(acc[m][2 * i]), pack4(acc[m][2 * i + 1])));
        WAIT_BAR(0);
        if (lw == 0 && lane == 0) __hip_atomic_fetch_add((GAS unsigned*)(ws + 8192 + mt * 256), 1u, RLX_AGENT);
    }
}

__global__ __launch_bounds__(768, 3) void fused_kernel(const u16* __restrict__ x, const u16* __restrict__ wsr, const u16* __restrict__ wsi,
                                                        const u16* __restrict__ wdr, const u16* __restrict__ wdi, const u16* __restrict__ wqkv,
                                                        const u16* __restrict__ wo, const u16* __restrict__ gw, u16* __restrict__ out, char* ws) {
    __shared__ __attribute__((aligned(1024))) char smem_[163840];
    LAS char* smem = (LAS char*)smem_;
    const int tid = threadIdx.x, lane = tid & 63, wid = __builtin_amdgcn_readfirstlane(tid >> 6);
    const int xcd = blockIdx.x & 7, idx = blockIdx.x >> 3;
    const int hd = xcd * 4 + (idx >> 3), mt = idx & 7;
    const rsrc_t rs = __builtin_amdgcn_make_buffer_rsrc(ws, 0, WS_END, 0x00020000);
    if (wid == 0) STAMP(0, 0);
    if (wid < 8) {
        const unsigned s0 = blockIdx.x * 7 + (wid < 7 ? wid : 6);
        const char* pf0 = s0 < 768 ? (const char*)wqkv + (size_t)s0 * 8192 : s0 < 1280 ? (const char*)x + (size_t)(s0 - 768) * 8192
                        : s0 < 1536 ? (const char*)wdr + (size_t)(s0 - 1280) * 8192 : (const char*)wdi + (size_t)((s0 - 1536) & 255) * 8192;
        const char* wsb = (const char*)((hd >> 4) ? wsi : wsr) + (size_t)mt * 256 * 4096;
        const char* pf2 = (const char*)wo + (size_t)blockIdx.x * 8192;
        main_waves(smem, wid, lane, mt, hd, ws, rs, out, x, gw, pf0, wsb, pf2);
    }
    else side_waves(smem, wid - 8, lane, mt, hd, x, wsr, wsi, wdr, wdi, wqkv, wo, gw, ws, rs);
}

extern "C" void kernel_launch(void* const* d_in, const int* in_sizes, int n_in, void* d_out, int out_size, void* d_ws, size_t ws_size, hipStream_t stream) {
    const u16* x = (const u16*)d_in[0]; const u16* wsr = (const u16*)d_in[1]; const u16* wsi = (const u16*)d_in[2];
    const u16* wdr = (const u16*)d_in[3]; const u16* wdi = (const u16*)d_in[4]; const u16* wqkv = (const u16*)d_in[6];
    const u16* wo = (const u16*)d_in[7]; const u16* gw = (const u16*)d_in[8];
    (void)hipMemsetAsync(d_ws, 0, WS_CNT_BYTES, stream);
    fused_kernel<<<256, 768, 0, stream>>>(x, wsr, wsi, wdr, wdi, wqkv, wo, gw, (u16*)d_out, (char*)d_ws);
}
```

```cpp
#include <hip/hip_runtime.h>
#include <hip/hip_bf16.h>

typedef unsigned short u16;
typedef short bf16x8 __attribute__((ext_vector_type(8)));
typedef short s16x4 __attribute__((ext_vector_type(4)));
typedef float f32x2 __attribute__((ext_vector_type(2)));
typedef float f32x4 __attribute__((ext_vector_type(4)));
typedef float f32x16 __attribute__((ext_vector_type(16)));
typedef unsigned u32x4 __attribute__((ext_vector_type(4)));
typedef unsigned u32x2 __attribute__((ext_vector_type(2)));
typedef __bf16 bf16x2_t __attribute__((ext_vector_type(2)));
#define LAS __attribute__((address_space(3)))
#define GAS __attribute__((address_space(1)))
#define DI __device__ __forceinline__
#define SBAR() __builtin_amdgcn_sched_barrier(0)
#define PIN(x) asm volatile("" : "+v"(x))
#define MFMA16(a, b, c) __builtin_amdgcn_mfma_f32_16x16x32_bf16(a, b, c, 0, 0, 0)
#define MFMA32(a, b, c) __builtin_amdgcn_mfma_f32_32x32x16_bf16(a, b, c, 0, 0, 0)
#define WAIT_BAR(N) asm volatile("s_waitcnt vmcnt(" #N ") lgkmcnt(0)\n\ts_barrier" ::: "memory")
#define LGKM_BAR() asm volatile("s_waitcnt lgkmcnt(0)\n\ts_barrier" ::: "memory")
#define RLX_AGENT __ATOMIC_RELAXED, __HIP_MEMORY_SCOPE_AGENT
#define BC(x) __builtin_bit_cast(bf16x8, x)

constexpr int SEQ = 2048, DM = 1024;
constexpr unsigned MB = 1u << 20;
constexpr unsigned WS_F1 = 4096, WS_F2 = 4096 + 16384, WS_GATE = 65536, WS_KV = 1 * MB, WS_AB = 9 * MB, WS_O = 17 * MB, WS_Y = 21 * MB, WS_END = 29 * MB;
typedef unsigned long long u64;

#define STAMP(kid, i)
#define TICK(k, i)

DI unsigned cvtpk(float lo, float hi) { f32x2 v = {lo, hi}; bf16x2_t b = __builtin_convertvector(v, bf16x2_t); return __builtin_bit_cast(unsigned, b); }
DI void dma(unsigned voff, const char* sbase, unsigned l) {
    asm volatile("s_mov_b32 m0, %2\n\ts_nop 0\n\tglobal_load_lds_dwordx4 %0, %1" :: "v"(voff), "s"(sbase), "s"(l) : "memory"); }
DI void dma_dev(unsigned voff, const char* sbase, unsigned l) {
    asm volatile("s_mov_b32 m0, %2\n\ts_nop 0\n\tglobal_load_lds_dwordx4 %0, %1 sc1" :: "v"(voff), "s"(sbase), "s"(l) : "memory"); }
DI void ldsw128(LAS char* p, u32x4 v) { *(LAS u32x4*)p = v; }
DI bf16x8 ldsr128(const LAS char* p) { return *(const LAS bf16x8*)p; }
DI bf16x8 ldstr(const LAS char* p, int off2) {
    s16x4 a = __builtin_amdgcn_ds_read_tr16_b64_v4i16((LAS s16x4*)p);
    s16x4 b = __builtin_amdgcn_ds_read_tr16_b64_v4i16((LAS s16x4*)(p + off2));
    return (bf16x8){a[0], a[1], a[2], a[3], b[0], b[1], b[2], b[3]};
}
DI int lane_now() { int l; asm volatile("v_mbcnt_lo_u32_b32 %0, -1, 0\n\tv_mbcnt_hi_u32_b32 %0, -1, %0" : "=v"(l)); return l; }
DI float bflo(unsigned u) { return __uint_as_float(u << 16); }
DI float bfhi(unsigned u) { return __uint_as_float(u & 0xffff0000u); }
DI u32x4 swap16(u32x2 a, u32x2 b) {
    auto r0 = __builtin_amdgcn_permlane16_swap(a[0], b[0], false, false);
    auto r1 = __builtin_amdgcn_permlane16_swap(a[1], b[1], false, false);
    return (u32x4){r0[0], r1[0], r0[1], r1[1]};
}
DI u32x2 pack4(f32x4 v) { return (u32x2){cvtpk(v[0], v[1]), cvtpk(v[2], v[3])}; }
typedef __amdgpu_buffer_rsrc_t rsrc_t;
DI void st_wt(rsrc_t r, unsigned off, u32x4 v) { __builtin_amdgcn_raw_buffer_store_b128(v, r, off, 0, 16); }
DI u32x4 ld_dev(rsrc_t r, unsigned off) { return __builtin_amdgcn_raw_buffer_load_b128(r, off, 0, 16); }
DI void wait_flags(const char* first, int n, u64 lo, u64 span, int lane) {
    const GAS u64* p = (const GAS u64*)(first + (lane < n ? lane : 0) * 64);
    for (unsigned spins = 0; spins < (1u << 16); ++spins) {
        const u64 v = __hip_atomic_load(p, RLX_AGENT);
        if (__all(v - lo <= span)) break;
        __builtin_amdgcn_s_sleep(1);
    }
}
DI void set_flag(char* p, u64 v, int lane) { if (lane == 0) __hip_atomic_store((GAS u64*)p, v, RLX_AGENT); }

#define ABL_FDMA 0
#define ABL_FMFMA 0
#define ABL_ATT 0
#define PADN 0
#define FN_PAD(g) do { _Pragma("unroll") for (int z_ = 0; z_ < PADN; ++z_) asm volatile("s_nop 3"); SBAR(); } while (0)
#define FN_PRIO 1
#define PF_MODE 0
#define PF_SLEEP 8
constexpr int K1_STAGE = 53248, K1_A = 0, K1_WD = 32768, K1_WQ = 40960;
constexpr int AT_STAGE = 8192, FB_BASE = 32768, FW_STAGE = 32768, FW_TOP = 131072;
struct K1Frag { bf16x8 x[4], wd[2], wqk[2], wv; };
struct FnFrag { bf16x8 w[4], a[4]; };

DI void attn_store(rsrc_t rs, unsigned off, const f32x16& o, float l) {
    const float rl = 1.0f / l; u32x4 w0, w1;
#pragma unroll
    for (int i = 0; i < 4; ++i) { w0[i] = cvtpk(o[2 * i] * rl, o[2 * i + 1] * rl); w1[i] = cvtpk(o[8 + 2 * i] * rl, o[9 + 2 * i] * rl); }
    st_wt(rs, off, w0); st_wt(rs, off + 256, w1);
}
DI bf16x8 attn_sel(int lane) {
    const short v = (((lane >> 2) & 1) == ((lane >> 4) & 1)) ? (short)0x3f80 : (short)0;
    return (bf16x8){v, v, v, v, v, v, v, v};
}
DI float max16(const f32x16& s) {
    float a = fmaxf(fmaxf(s[0], s[1]), s[2]), b = fmaxf(fmaxf(s[3], s[4]), s[5]);
#pragma unroll
    for (int r = 6; r < 16; r += 4) { a = fmaxf(fmaxf(a, s[r]), s[r + 1]); if (r + 3 < 16) b = fmaxf(fmaxf(b, s[r + 2]), s[r + 3]); }
    return fmaxf(a, b);
}
DI void attn_slow(rsrc_t rs, const char* kv, bf16x8 q0, bf16x8 q1, int lane, unsigned ooff) {
    __builtin_amdgcn_fence(__ATOMIC_ACQUIRE, "agent");
    const bf16x8 sel = attn_sel(lane);
    kv += lane * 16;
    float mx = -INFINITY;
    const f32x16 z = f32x16{};
#pragma unroll 1
    for (int j = 0; j < 64; ++j) {
        const char* kp = kv + (j >> 1) * 8192 + (j & 1) * 2048;
        f32x16 s = MFMA32(*(const bf16x8*)kp, q0, z); s = MFMA32(*(const bf16x8*)(kp + 1024), q1, s);
        mx = fmaxf(mx, max16(s));
    }
    mx = fmaxf(mx, __shfl_xor(mx, 32));
    f32x16 negm, o = f32x16{}; f32x4 l = f32x4{};
#pragma unroll
    for (int r = 0; r < 16; ++r) negm[r] = -mx;
#pragma unroll 1
    for (int j = 0; j < 64; ++j) {
        const char* kp = kv + (j >> 1) * 8192 + (j & 1) * 2048;
        const char* vp = kv + ((j & 1) ? (((j & 7) == 7 ? (j >> 3) * 4 : (j + 1) >> 1) * 8192 + 4096) : ((j >> 1) * 8192 + 6144));
        const bf16x8 v0 = *(const bf16x8*)vp, v1 = *(const bf16x8*)(vp + 1024);
        f32x16 s = MFMA32(*(const bf16x8*)kp, q0, negm); s = MFMA32(*(const bf16x8*)(kp + 1024), q1, s);
        u32x4 p0, p1;
#pragma unroll
        for (int r = 0; r < 16; ++r) s[r] = __builtin_amdgcn_exp2f(s[r]);
#pragma unroll
        for (int r = 0; r < 4; ++r) { p0[r] = cvtpk(s[2 * r], s[2 * r + 1]); p1[r] = cvtpk(s[8 + 2 * r], s[9 + 2 * r]); }
        o = MFMA32(v0, BC(p0), o); l = MFMA16(sel, BC(p0), l);
        o = MFMA32(v1, BC(p1), o); l = MFMA16(sel, BC(p1), l);
    }
    attn_store(rs, ooff, o, l[0]);
}

DI void touch8k(const char* p, unsigned voff, unsigned dump) {
    asm volatile("s_mov_b32 m0, %2\n\ts_nop 0\n\tglobal_load_lds_dword %0, %1" :: "v"(voff), "s"(p), "s"(dump) : "memory"); }
DI void main_waves(LAS char* smem, int wid, int lane, int mt, int hd, char* ws, rsrc_t rs, u16* __restrict__ out,
                   const u16* __restrict__ x, const u16* __restrict__ gw, const char* pf0, const char* wsb, const char* pf2) {
    const unsigned dump = (unsigned)(size_t)smem + 3 * K1_STAGE + wid * 256;
    u64 base;
    {
        const int grow = 256 * mt + 8 * hd + wid;
        u32x4 gx0, gx1, gw0, gw1; u32x2 ep;
        asm volatile("global_load_dwordx2 %4, %7, off sc1\n\tglobal_load_dwordx4 %0, %5, off\n\tglobal_load_dwordx4 %1, %5, off offset:16\n\t"
                     "global_load_dwordx4 %2, %6, off\n\tglobal_load_dwordx4 %3, %6, off offset:16"
                     : "=&v"(gx0), "=&v"(gx1), "=&v"(gw0), "=&v"(gw1), "=&v"(ep)
                     : "v"((const char*)x + (size_t)grow * 2048 + lane * 32), "v"((const char*)gw + lane * 32), "v"(ws) : "memory");
        __builtin_amdgcn_s_sleep(PF_SLEEP);
        touch8k(pf0, lane * 128, dump);
        asm volatile("s_waitcnt vmcnt(1)" : "+v"(gx0), "+v"(gx1), "+v"(gw0), "+v"(gw1), "+v"(ep) :: "memory");
        base = (((u64)(unsigned)__builtin_amdgcn_readfirstlane(ep[1]) << 32) | (unsigned)__builtin_amdgcn_readfirstlane(ep[0])) + 8;
        float s = 0.f;
#pragma unroll
        for (int i = 0; i < 4; ++i) {
            s = fmaf(bflo(gx0[i]), bflo(gw0[i]), s); s = fmaf(bfhi(gx0[i]), bfhi(gw0[i]), s);
            s = fmaf(bflo(gx1[i]), bflo(gw1[i]), s); s = fmaf(bfhi(gx1[i]), bfhi(gw1[i]), s);
        }
#pragma unroll
        for (int o = 32; o >= 1; o >>= 1) s += __shfl_xor(s, o);
        if (lane == 0) __builtin_amdgcn_raw_buffer_store_b32(__float_as_uint(1.f / (1.f + __expf(-s))), rs, WS_GATE + grow * 4, 0, 16);
    }
#define TW(l_) (unsigned)(((hd & 15) * 16 + ((l_) >> 2)) * 4096 + ((l_) & 3) * 128)
    int fr = lane & 15, fq = lane >> 4;
    {
        const int wm = wid >> 1, wn = wid & 1;
        const int cpart = ((fq ^ (fr >> 1)) & 7) * 16;
        const int aA = K1_A + (64 * wm + fr) * 128 + cpart, aD = K1_WD + (32 * wn + fr) * 128 + cpart;
        const int aT0 = (8 * fq + (fr >> 2)) * 64 + ((((lane & 3) >> 1) ^ ((fq & 1) << 1)) * 16) + (lane & 1) * 8;
        const int aQ = K1_WQ + wn * 4096 + aT0, aV = K1_WQ + 8192 + (aT0 ^ (wn * 32));
        f32x4 accQ[2][4], accV[4], accD[2][4];
#pragma unroll
        for (int m = 0; m < 4; ++m) { accQ[0][m] = f32x4{}; accQ[1][m] = f32x4{}; accV[m] = f32x4{}; accD[0][m] = f32x4{}; accD[1][m] = f32x4{}; }
        K1Frag X, Y;
#define K1_RD(F, i, slot, ks) do { const LAS char* sb_ = smem + (slot); \
        if ((i) == 0) F.wqk[0] = ldstr(sb_ + aQ + (ks) * 2048, 256); \
        else if ((i) == 1) { F.x[0] = ldsr128(sb_ + (aA ^ ((ks) * 64))); } \
        else if ((i) == 2) F.wqk[1] = ldstr(sb_ + (aQ ^ 32) + (ks) * 2048, 256); \
        else if ((i) == 3) F.wv = ldstr(sb_ + aV + (ks) * 2048, 256); \
        else if ((i) == 4) { F.wd[0] = ldsr128(sb_ + (aD ^ ((ks) * 64))); F.wd[1] = ldsr128(sb_ + (aD ^ ((ks) * 64)) + 2048); } \
        else if ((i) < 8) F.x[(i) - 4] = ldsr128(sb_ + (aA ^ ((ks) * 64)) + ((i) - 4) * 2048); } while (0)
#define K1_MF(F, j) do { constexpr int m_ = (j) / 5, r_ = (j) % 5; \
        if (r_ == 0) accQ[0][m_] = MFMA16(F.wqk[0], F.x[m_], accQ[0][m_]); \
        else if (r_ == 1) accQ[1][m_] = MFMA16(F.wqk[1], F.x[m_], accQ[1][m_]); \
        else if (r_ == 2) accV[m_] = MFMA16(F.x[m_], F.wv, accV[m_]); \
        else if (r_ == 3) accD[0][m_] = MFMA16(F.x[m_], F.wd[0], accD[0][m_]); \
        else accD[1][m_] = MFMA16(F.x[m_], F.wd[1], accD[1][m_]); } while (0)
#define K1_G(g, C, R, rslot, ks) do { K1_MF(C, 2 * (g)); K1_RD(R, g, rslot, ks); K1_MF(C, 2 * (g) + 1); SBAR(); } while (0)
#define K1_PHASE(C, R, rslot, ks) do { K1_G(0, C, R, rslot, ks); K1_G(1, C, R, rslot, ks); K1_G(2, C, R, rslot, ks); K1_G(3, C, R, rslot, ks); K1_G(4, C, R, rslot, ks); \
        K1_G(5, C, R, rslot, ks); K1_G(6, C, R, rslot, ks); K1_G(7, C, R, rslot, ks); K1_G(8, C, R, rslot, ks); K1_G(9, C, R, rslot, ks); } while (0)
        LGKM_BAR();
        if (wid == 0) STAMP(0, 1);
#pragma unroll
        for (int i = 0; i < 8; ++i) K1_RD(X, i, 0, 0);
        int sl = 0, sn = K1_STAGE;
        SBAR();
        for (int t = 0; t < 15; ++t) {
            K1_PHASE(X, Y, sl, 1);
            if (t == 14) asm volatile("s_waitcnt vmcnt(0)" ::: "memory");
            LGKM_BAR(); SBAR();
            TICK(0, t);
            if (t == 7 + 2 * wid) { const int l_ = lane_now(); touch8k(wsb + ((mt + wid) & 7) * 512, TW(l_), dump); }
            SBAR();
            K1_PHASE(Y, X, sn, 0);
            sl = sn; sn = (sn == 2 * K1_STAGE) ? 0 : sn + K1_STAGE;
        }
        K1_PHASE(X, Y, 0, 1);
        LGKM_BAR(); SBAR();
        K1_MF(Y, 0); K1_MF(Y, 1); K1_MF(Y, 2); K1_MF(Y, 3); K1_MF(Y, 4); K1_MF(Y, 5); K1_MF(Y, 6); K1_MF(Y, 7); K1_MF(Y, 8); K1_MF(Y, 9);
        K1_MF(Y, 10); K1_MF(Y, 11); K1_MF(Y, 12); K1_MF(Y, 13); K1_MF(Y, 14); K1_MF(Y, 15); K1_MF(Y, 16); K1_MF(Y, 17); K1_MF(Y, 18); K1_MF(Y, 19);
        if (wid == 0) STAMP(0, 2);
        lane = lane_now(); fr = lane & 15; fq = lane >> 4;
        const float qs = wn ? 1.0f : 0.17677669529663687f * 1.4426950408889634f;
#pragma unroll
        for (int m = 0; m < 4; ++m) {
            const u32x4 w = swap16(pack4(accQ[0][m] * qs), pack4(accQ[1][m] * qs));
            const int r = (16 * m + fr) & 31, rho = (r & 19) | ((r & 4) << 1) | ((r & 8) >> 1), j = 2 * wm + (m >> 1);
            const int in = (fq & 1) * 1024 + ((fq >> 1) * 32 + rho) * 16;
            ldsw128(smem + (wn ? wm * AT_STAGE + (m >> 1) * 2048 : FW_TOP - 2 * FW_STAGE + j * 2048) + in, w);
        }
        const int rv = 8 * (fr >> 2) + 4 * wn + (fr & 3);
#pragma unroll
        for (int i = 0; i < 2; ++i) {
            const int in = (fq & 1) * 1024 + ((fq >> 1) * 32 + rv) * 16;
            const u32x4 w = swap16(pack4(accV[2 * i]), pack4(accV[2 * i + 1]));
            ldsw128(smem + ((wm + i) & 3) * AT_STAGE + 6144 - i * 2048 + in, w);
#pragma unroll
            for (int n = 0; n < 2; ++n) {
                const u32x4 d = swap16(pack4(accD[n][2 * i]), pack4(accD[n][2 * i + 1]));
                const int el = 32 * wn + 16 * n + fr, ch = 4 * i + 2 * (fq & 1) + (fq >> 1);
                ldsw128(smem + FB_BASE + wm * 8192 + el * 128 + ((ch ^ (fr >> 1)) * 16), d);
            }
        }
        LGKM_BAR();
        if (wid == 0) STAMP(0, 3);
    }
    {
        const int qt = mt * 8 + wid;
        char* f1 = ws + WS_F1 + hd * 512;
        const unsigned rec0 = (hd * 32 + 4 * mt) * 8192 + wid * 1024 + lane * 16;
#define PUBLISH(q) do { const u32x4 a_ = *(const LAS u32x4*)(smem + (q) * 8192 + wid * 1024 + lane * 16), b_ = *(const LAS u32x4*)(smem + FB_BASE + (q) * 8192 + wid * 1024 + lane * 16); \
        st_wt(rs, WS_KV + rec0 + (q) * 8192, a_); st_wt(rs, WS_AB + rec0 + (q) * 8192, b_); } while (0)
        PUBLISH(0); PUBLISH(1);
        const bf16x8 sel = attn_sel(lane);
        const LAS char* fb = smem + lane * 16;
        const bf16x8 q0 = ldsr128(fb + FW_TOP - 2 * FW_STAGE + wid * 2048), q1 = ldsr128(fb + FW_TOP - 2 * FW_STAGE + wid * 2048 + 1024);
        bf16x8 kA0, kA1, kB0, kB1, vA0, vA1, vB0, vB1 = bf16x8{};
        f32x16 negm, o = f32x16{}, sA, sB; f32x4 l = f32x4{};
        u32x4 p0, p1 = u32x4{};
        unsigned sink = 0; u64 fv = 0;
        kA0 = ldsr128(fb); kA1 = ldsr128(fb + 1024); kB0 = ldsr128(fb + 2048); kB1 = ldsr128(fb + 3072); vA0 = ldsr128(fb + 6144); vA1 = ldsr128(fb + 7168);
        LGKM_BAR();
        {
            sA = MFMA32(kA0, q0, o); sA = MFMA32(kA1, q1, sA);
            float mx = max16(sA); mx = fmaxf(mx, __shfl_xor(mx, 32)) + 40.f;
#pragma unroll
            for (int r = 0; r < 16; ++r) { negm[r] = -mx; sA[r] -= mx; }
        }
        if (wid == 0) STAMP(1, 0);
#define EX(S_, i) S_[i] = __builtin_amdgcn_exp2f(S_[i])
#define AT_STEP(SC, SN, K0, K1, VC0, VP1, RD0, RD1, RD2, RD3) do { \
        SN = MFMA32(K0, q0, negm); SBAR(); RD0; EX(SC, 0); EX(SC, 1); EX(SC, 2); EX(SC, 3); SBAR(); \
        o = MFMA32(VP1, BC(p1), o); PIN(o); SBAR(); RD1; EX(SC, 4); EX(SC, 5); EX(SC, 6); EX(SC, 7); p0[0] = cvtpk(SC[0], SC[1]); p0[1] = cvtpk(SC[2], SC[3]); SBAR(); \
        l = MFMA16(sel, BC(p1), l); PIN(l); SBAR(); p0[2] = cvtpk(SC[4], SC[5]); p0[3] = cvtpk(SC[6], SC[7]); EX(SC, 8); SBAR(); \
        SN = MFMA32(K1, q1, SN); SBAR(); RD2; EX(SC, 9); EX(SC, 10); EX(SC, 11); EX(SC, 12); SBAR(); \
        o = MFMA32(VC0, BC(p0), o); PIN(o); SBAR(); RD3; EX(SC, 13); EX(SC, 14); EX(SC, 15); p1[0] = cvtpk(SC[8], SC[9]); SBAR(); \
        l = MFMA16(sel, BC(p0), l); PIN(l); SBAR(); p1[1] = cvtpk(SC[10], SC[11]); p1[2] = cvtpk(SC[12], SC[13]); p1[3] = cvtpk(SC[14], SC[15]); SBAR(); } while (0)
        int sl = AT_STAGE;
        for (int T = 1; T <= 32; ++T) {
            if (T <= 4) {
                asm volatile("s_waitcnt vmcnt(0)" : "+v"(fv) :: "memory");
                if (wid == 1 && (T == 2 || (T == 4 && !__all(fv == base + 2)))) wait_flags(f1, 8, base + (T >> 1), 2 - (T >> 1), lane);
            }
            LGKM_BAR(); SBAR();
            TICK(1, T);
            if (T <= 3) {
                if (T <= 2 && wid == 0) set_flag(f1 + mt * 64, base + T, lane);
                if (T == 1) { PUBLISH(2); PUBLISH(3); }
                if (T == 3 && wid == 1) asm volatile("global_load_dwordx2 %0, %1, off sc1" : "+v"(fv) : "v"(f1 + (lane & 7) * 64) : "memory");
            }
            if (T == 4 * wid - 8 && T > 5)
                { asm volatile("global_load_dword %0, %1, %2" : "+v"(sink) : "v"(TW(lane)), "s"(wsb + ((mt + wid) & 7) * 512) : "memory");
                  if (wid == 7) asm volatile("global_load_dword %0, %1, %2" : "+v"(sink) : "v"(lane * 128), "s"(pf2) : "memory"); }
            SBAR();
            if (!ABL_ATT) {
            AT_STEP(sA, sB, kB0, kB1, vA0, vB1, kA0 = ldsr128(fb + sl), kA1 = ldsr128(fb + sl + 1024), vB0 = ldsr128(fb + sl + 4096), vB1 = ldsr128(fb + sl + 5120));
            AT_STEP(sB, sA, kA0, kA1, vB0, vA1, kB0 = ldsr128(fb + sl + 2048), kB1 = ldsr128(fb + sl + 3072), vA0 = ldsr128(fb + sl + 6144), vA1 = ldsr128(fb + sl + 7168)); }
            sl = (sl + AT_STAGE) & (4 * AT_STAGE - 1);
        }
        o = MFMA32(vB1, BC(p1), o); l = MFMA16(sel, BC(p1), l);
        if (wid == 0) STAMP(1, 1);
        bool bad = !(l[0] >= 7.8886e-31f && l[0] <= 3.0e38f);
#pragma unroll
        for (int r = 0; r < 16; ++r) bad |= !(fabsf(o[r]) <= 3.0e38f);
        const int r = lane & 31, qrow = qt * 32 + ((r & 19) | ((r & 4) << 1) | ((r & 8) >> 1));
        const unsigned ooff = WS_O + ((qrow >> 4) * 32 + hd) * 1024 + ((lane >> 5) * 32 + (qrow & 15)) * 16;
        if (!ABL_ATT && !ABL_FDMA && __builtin_expect(__any(bad), 0)) attn_slow(rs, ws + WS_KV + hd * 262144, q0, q1, lane, ooff);
        else attn_store(rs, ooff, o, l[0]);
        WAIT_BAR(0);
        asm volatile("" :: "v"(sink));
        if (wid == 0) STAMP(1, 2);
    }
    {
        const int rt = 2 * (blockIdx.x & 7) + (blockIdx.x >> 7), nb = ((blockIdx.x >> 3) & 15) * 64;
        if (wid == 0) {
            set_flag(ws + WS_F2 + (mt * 32 + hd) * 64, base + 5, lane);
            wait_flags(ws + WS_F2 + (rt >> 1) * 2048, 32, base + 5, 0, lane);
            __builtin_amdgcn_fence(__ATOMIC_ACQUIRE, "wavefront");
            if (blockIdx.x == 0) set_flag(ws, base, lane);
            STAMP(1, 3);
        }
        LGKM_BAR();
        const int R0 = rt * 128 + 16 * wid;
        const int t = R0 + fr, col = nb + 16 * (fq & 1) + 8 * (fq >> 1);
        u32x4 of[32], y0[2], y1[2]; float g;
#pragma unroll
        for (int ks = 0; ks < 32; ++ks) of[ks] = ld_dev(rs, WS_O + ((R0 >> 4) * 32 + ks) * 1024 + lane * 16);
        SBAR();
        const int kq = fr >> 2, swz = ((kq >> 1) & 1) | ((fq & 1) << 1);
        const int aW = (8 * fq + kq) * 128 + ((((lane & 3) >> 1) ^ (swz << 1)) * 16) + (lane & 1) * 8;
        f32x4 acc[4];
#pragma unroll
        for (int n = 0; n < 4; ++n) acc[n] = f32x4{};
#pragma unroll
        for (int ks = 0; ks < 32; ++ks) {
            const int kt = ks >> 1;
            const int wbase = (kt < 4 ? FW_TOP - 2 * FW_STAGE + kt * 8192 : kt == 4 ? FB_BASE : kt < 9 ? FW_TOP + (kt - 5) * 8192 : kt == 9 ? FB_BASE + 8192
                              : kt < 14 ? FW_TOP - FW_STAGE + (kt - 10) * 8192 : FB_BASE + (kt - 12) * 8192) + (ks & 1) * 4096;
#pragma unroll
            for (int n = 0; n < 4; ++n) acc[n] = MFMA16(ldstr(smem + wbase + (aW ^ (n * 32)), 512), BC(of[ks]), acc[n]);
            if (ks == 7) {
                SBAR();
                g = __uint_as_float(__builtin_amdgcn_raw_buffer_load_b32(rs, WS_GATE + t * 4, 0, 16));
#pragma unroll
                for (int i = 0; i < 2; ++i) { y0[i] = ld_dev(rs, WS_Y + (t * DM + col + 32 * i) * 2); y1[i] = ld_dev(rs, WS_Y + 4 * MB + (t * DM + col + 32 * i) * 2); }
                SBAR();
            }
        }
        const float g1 = 1.f - g;
#pragma unroll
        for (int i = 0; i < 2; ++i) {
            float v[8];
#pragma unroll
            for (int j = 0; j < 4; ++j) {
                auto r = __builtin_amdgcn_permlane16_swap(__float_as_uint(acc[2 * i][j]), __float_as_uint(acc[2 * i + 1][j]), false, false);
                v[j] = __uint_as_float(r[0]); v[4 + j] = __uint_as_float(r[1]);
            }
            u32x4 w;
#pragma unroll
            for (int j = 0; j < 4; ++j)
                w[j] = cvtpk(g * (bflo(y0[i][j]) - bflo(y1[i][j])) + g1 * v[2 * j], g * (bfhi(y0[i][j]) - bfhi(y1[i][j])) + g1 * v[2 * j + 1]);
            *(u32x4*)(out + (size_t)t * DM + col + 32 * i) = w;
        }
        if (wid == 0) STAMP(1, 4);
    }
}

DI void side_waves(LAS char* smem, int lw, int lane, int mt, int hd, const u16* __restrict__ x, const u16* __restrict__ wsr, const u16* __restrict__ wsi,
                   const u16* __restrict__ wdr, const u16* __restrict__ wdi, const u16* __restrict__ wqkv, const u16* __restrict__ wo,
                   const u16* __restrict__ gw, char* ws, rsrc_t rs) {
    const unsigned lds0 = (unsigned)(size_t)smem + lw * 1024;
    const unsigned rowl = 8 * lw + (lane >> 3), chk = ((lane & 7) ^ ((4 * lw + (lane >> 4)) & 7)) * 16;
    const int c = hd >> 4;
    const char* wb = (const char*)(c ? wsi : wsr) + (size_t)mt * 256 * 4096;
    unsigned vo[10];
    {
        const char* xb = (const char*)x + (size_t)mt * 256 * 2048;
        const char* wdb = (const char*)(c ? wdi : wdr) + (size_t)(hd & 15) * 64 * 2048;
        const char* wqb = (const char*)wqkv + hd * 64 + lw * 16 * 6144;
#pragma unroll
        for (int i = 0; i < 8; ++i) vo[i] = (rowl + 32 * i) * 2048 + chk;
        const unsigned voffW = (lane >> 2) * 6144 + (((lane & 3) ^ ((lane >> 5) << 1)) * 16);
#define K1_DMA(t, slot) do { const char* xa_ = xb + (size_t)(t) * 128; const char* wd_ = wdb + (size_t)(t) * 128; const char* wq_ = wqb + (size_t)(t) * 64 * 6144; \
        _Pragma("unroll") for (int u_ = 0; u_ < 3; ++u_) dma(voffW, wq_ + u_ * 2048, lds0 + (slot) + K1_WQ + u_ * 4096); \
        _Pragma("unroll") for (int i_ = 0; i_ < 2; ++i_) dma(vo[i_], wd_, lds0 + (slot) + K1_WD + i_ * 4096); \
        _Pragma("unroll") for (int i_ = 0; i_ < 8; ++i_) dma(vo[i_], xa_, lds0 + (slot) + K1_A + i_ * 4096); } while (0)
        K1_DMA(0, 0); K1_DMA(1, K1_STAGE); K1_DMA(2, 2 * K1_STAGE);
        WAIT_BAR(26);
        int sl = 0;
        for (int t = 0; t < 13; ++t) {
            WAIT_BAR(13);
            K1_DMA(t + 3, sl);
            sl = (sl == 2 * K1_STAGE) ? 0 : sl + K1_STAGE;
        }
        WAIT_BAR(13);
        WAIT_BAR(0);
#pragma unroll
        for (int i = 0; i < 8; ++i) vo[i] = (rowl + 32 * i) * 4096 + chk;
        vo[8] = lane * 16 + lw * 1024; vo[9] = vo[8] + 4096;
#pragma unroll
        for (int s = 0; s < 2; ++s)
#pragma unroll
            for (int i = 0; i < 8; ++i) dma(vo[i], wb + (size_t)((4 * mt + s) & 31) * 128, lds0 + FW_TOP - s * FW_STAGE + i * 4096);
        LGKM_BAR();
        WAIT_BAR(8);
    }
    {
        const char* kvb = ws + WS_KV + hd * 262144;
        const unsigned vl = lane * 16 + lw * 1024;
        const char *bW = wb, *bA = ws + WS_AB + hd * 262144; int kmask = 31, kstep = 128, kstepA = 8192, koff = 4 * mt;
#define FN_DMA_P(p, s, fslot, aslot) do { \
        if ((p) < 8) dma(vo[p], bW + (size_t)((koff + (s)) & kmask) * kstep, lds0 + (fslot) + (p) * 4096); \
        else if ((p) < 10) dma_dev(vo[p], bA + (size_t)((koff + (s)) & kmask) * kstepA, lds0 + FB_BASE + (aslot) + ((p) - 8) * 4096); \
        else if ((p) == 10) dma_dev(vl, kvb + ((4 * mt + (s)) & 31) * 8192, lds0 + (aslot)); \
        else dma_dev(vl, kvb + ((4 * mt + (s) - ((((s) & 3) == 0 && lw < 2) ? 4 : 0)) & 31) * 8192 + 4096, lds0 + (aslot) + 4096); } while (0)
        const int fr = lane & 15, fq = lane >> 4;
        const int cpart = ((fq ^ (fr >> 1)) & 7) * 16;
        const int aW = (64 * lw + fr) * 128 + cpart, aA = FB_BASE + fr * 128 + cpart;
        f32x4 acc[4][4];
#pragma unroll
        for (int m = 0; m < 4; ++m)
#pragma unroll
            for (int n = 0; n < 4; ++n) acc[m][n] = f32x4{};
        FnFrag X, Yf;
#define FN_RD(F, i, slot, bslot, ks) do { \
        if ((i) == 0) F.w[0] = ldsr128(smem + (slot) + (aW ^ ((ks) * 64))); \
        else if ((i) < 5) F.a[(i) - 1] = ldsr128(smem + (bslot) + (aA ^ ((ks) * 64)) + ((i) - 1) * 2048); \
        else F.w[(i) - 4] = ldsr128(smem + (slot) + (aW ^ ((ks) * 64)) + ((i) - 4) * 2048); } while (0)
#define FN_MF(F, j) do { if (!ABL_FMFMA) acc[(j) >> 2][(j) & 3] = MFMA16(F.a[(j) & 3], F.w[(j) >> 2], acc[(j) >> 2][(j) & 3]); else asm volatile("" :: "v"(F.a[(j) & 3]), "v"(F.w[(j) >> 2])); } while (0)
#define FN_G(g, C, R, rslot, rb, ks, P0, s, fslot, aslot) do { FN_MF(C, 2 * (g)); FN_RD(R, g, rslot, rb, ks); FN_MF(C, 2 * (g) + 1); \
        if (!ABL_FDMA && ((g) & 3) != 0 && (P0) + (g) - 1 - ((g) >> 2) < PMAX) FN_DMA_P((P0) + (g) - 1 - ((g) >> 2), s, fslot, aslot); SBAR(); FN_PAD(g); } while (0)
#define FN_PHASE(C, R, rslot, rb, ks, P0, s, fslot, aslot) do { FN_G(0, C, R, rslot, rb, ks, P0, s, fslot, aslot); FN_G(1, C, R, rslot, rb, ks, P0, s, fslot, aslot); \
        FN_G(2, C, R, rslot, rb, ks, P0, s, fslot, aslot); FN_G(3, C, R, rslot, rb, ks, P0, s, fslot, aslot); FN_G(4, C, R, rslot, rb, ks, P0, s, fslot, aslot); \
        FN_G(5, C, R, rslot, rb, ks, P0, s, fslot, aslot); FN_G(6, C, R, rslot, rb, ks, P0, s, fslot, aslot); FN_G(7, C, R, rslot, rb, ks, P0, s, fslot, aslot); } while (0)
#define PMAX 12
        __builtin_amdgcn_s_setprio(FN_PRIO);
#pragma unroll
        for (int i = 0; i < 8; ++i) FN_RD(X, i, FW_TOP, 0, 0);
        LGKM_BAR();
        int f0 = FW_TOP, f1 = FW_TOP - FW_STAGE, f2 = FW_TOP - 2 * FW_STAGE;
        FN_DMA_P(0, 2, f2, 0); FN_DMA_P(1, 2, f2, 0); FN_DMA_P(2, 2, f2, 0); FN_DMA_P(3, 2, f2, 0); FN_DMA_P(4, 2, f2, 0); FN_DMA_P(5, 2, f2, 0);
        SBAR();
#define FN_ITER(t, WAITN) do { \
            FN_PHASE(X, Yf, f0, ((t) & 3) * 8192, 1, 6, (t) + 2, f2, (((t) + 2) & 3) * 8192);       \
            WAIT_BAR(WAITN); SBAR();                                                                \
            FN_SWITCH(t); \
            FN_PHASE(Yf, X, f1, (((t) + 1) & 3) * 8192, 0, 0, (t) + 3, f0, 0);                      \
            const int ft = f0; f0 = f1; f1 = f2; f2 = ft; } while (0)
#define FN_SWITCH(t)
#undef PMAX
#define PMAX 8
        FN_ITER(0, 8); FN_ITER(1, 8);
#undef PMAX
#define PMAX 12
#undef FN_SWITCH
#define FN_SWITCH(t) do { if ((t) == 29) { bW = bA = (const char*)wo + ((blockIdx.x >> 3) & 15) * 128; kmask = 3; kstep = kstepA = 320 * 2048; koff = 0; \
                const unsigned cw = ((lane & 7) ^ ((((lane >> 4) & 1) | ((lw & 1) << 1)) << 1)) * 16; \
                _Pragma("unroll") for (int i = 0; i < 10; ++i) vo[i] = (rowl + 32 * i) * 2048 + cw; } SBAR(); } while (0)
        for (int t = 2; t < 31; ++t) FN_ITER(t, 12);
#undef PMAX
#define PMAX 10
        FN_PHASE(X, Yf, f0, 3 * 8192, 1, 6, 33, f2, 8192);
        WAIT_BAR(10); SBAR();
#pragma unroll
        for (int i = 0; i < 8; ++i) dma(vo[i], bW + (size_t)640 * 2048, lds0 + FW_TOP - FW_STAGE + i * 4096);
#pragma unroll
        for (int i = 0; i < 4; ++i) dma(vo[i], bW + (size_t)896 * 2048, lds0 + FB_BASE + 2 * 8192 + i * 4096);
#pragma unroll
        for (int j = 0; j < 16; ++j) FN_MF(Yf, j);
        const unsigned yb = WS_Y + c * 4 * MB + ((mt * 256 + 64 * lw + fr) * DM + (hd & 15) * 64 + 16 * (fq & 1) + 8 * (fq >> 1)) * 2;
#pragma unroll
        for (int m = 0; m < 4; ++m)
#pragma unroll
            for (int i = 0; i < 2; ++i) st_wt(rs, yb + (m * 16 * DM + 32 * i) * 2, swap16(pack4(acc[m][2 * i]), pack4(acc[m][2 * i + 1])));
        WAIT_BAR(0);
    }
}

__global__ __launch_bounds__(768, 3) void fused_kernel(const u16* __restrict__ x, const u16* __restrict__ wsr, const u16* __restrict__ wsi,
                                                        const u16* __restrict__ wdr, const u16* __restrict__ wdi, const u16* __restrict__ wqkv,
                                                        const u16* __restrict__ wo, const u16* __restrict__ gw, u16* __restrict__ out, char* ws) {
    __shared__ __attribute__((aligned(1024))) char smem_[163840];
    LAS char* smem = (LAS char*)smem_;
    const int tid = threadIdx.x, lane = tid & 63, wid = __builtin_amdgcn_readfirstlane(tid >> 6);
    const int xcd = blockIdx.x & 7, idx = blockIdx.x >> 3;
    const int hd = xcd * 4 + (idx >> 3), mt = idx & 7;
    const rsrc_t rs = __builtin_amdgcn_make_buffer_rsrc(ws, 0, WS_END, 0x00020000);
    if (wid == 0) STAMP(0, 0);
    if (wid < 8) {
        const unsigned s0 = blockIdx.x * 7 + (wid < 7 ? wid : 6);
        const char* pf0 = s0 < 768 ? (const char*)wqkv + (size_t)s0 * 8192 : s0 < 1280 ? (const char*)x + (size_t)(s0 - 768) * 8192
                        : s0 < 1536 ? (const char*)wdr + (size_t)(s0 - 1280) * 8192 : (const char*)wdi + (size_t)((s0 - 1536) & 255) * 8192;
        const char* wsb = (const char*)((hd >> 4) ? wsi : wsr) + (size_t)mt * 256 * 4096;
        const char* pf2 = (const char*)wo + (size_t)blockIdx.x * 8192;
        main_waves(smem, wid, lane, mt, hd, ws, rs, out, x, gw, pf0, wsb, pf2);
    }
    else side_waves(smem, wid - 8, lane, mt, hd, x, wsr, wsi, wdr, wdi, wqkv, wo, gw, ws, rs);
}

extern "C" void kernel_launch(void* const* d_in, const int* in_sizes, int n_in, void* d_out, int out_size, void* d_ws, size_t ws_size, hipStream_t stream) {
    const u16* x = (const u16*)d_in[0]; const u16* wsr = (const u16*)d_in[1]; const u16* wsi = (const u16*)d_in[2];
    const u16* wdr = (const u16*)d_in[3]; const u16* wdi = (const u16*)d_in[4]; const u16* wqkv = (const u16*)d_in[6];
    const u16* wo = (const u16*)d_in[7]; const u16* gw = (const u16*)d_in[8];
    fused_kernel<<<256, 768, 0, stream>>>(x, wsr, wsi, wdr, wdi, wqkv, wo, gw, (u16*)d_out, (char*)d_ws);
}
```

```cpp
#include <hip/hip_runtime.h>
#include <hip/hip_bf16.h>

typedef unsigned short u16;
typedef short bf16x8 __attribute__((ext_vector_type(8)));
typedef short s16x4 __attribute__((ext_vector_type(4)));
typedef float f32x2 __attribute__((ext_vector_type(2)));
typedef float f32x4 __attribute__((ext_vector_type(4)));
typedef float f32x16 __attribute__((ext_vector_type(16)));
typedef unsigned u32x4 __attribute__((ext_vector_type(4)));
typedef unsigned u32x2 __attribute__((ext_vector_type(2)));
typedef __bf16 bf16x2_t __attribute__((ext_vector_type(2)));
#define LAS __attribute__((address_space(3)))
#define GAS __attribute__((address_space(1)))
#define DI __device__ __forceinline__
#define SBAR() __builtin_amdgcn_sched_barrier(0)
#define PIN(x) asm volatile("" : "+v"(x))
#define MFMA16(a, b, c) __builtin_amdgcn_mfma_f32_16x16x32_bf16(a, b, c, 0, 0, 0)
#define MFMA32(a, b, c) __builtin_amdgcn_mfma_f32_32x32x16_bf16(a, b, c, 0, 0, 0)
#define WAIT_BAR(N) asm volatile("s_waitcnt vmcnt(" #N ") lgkmcnt(0)\n\ts_barrier" ::: "memory")
#define LGKM_BAR() asm volatile("s_waitcnt lgkmcnt(0)\n\ts_barrier" ::: "memory")
#define RLX_AGENT __ATOMIC_RELAXED, __HIP_MEMORY_SCOPE_AGENT
#define BC(x) __builtin_bit_cast(bf16x8, x)

constexpr int SEQ = 2048, DM = 1024;
constexpr unsigned MB = 1u << 20;
constexpr unsigned WS_F1 = 4096, WS_F2 = 4096 + 16384, WS_GATE = 65536, WS_KV = 1 * MB, WS_AB = 9 * MB, WS_O = 17 * MB, WS_Y = 21 * MB, WS_END = 29 * MB;
typedef unsigned long long u64;

#define STAMP(kid, i)
#define TICK(k, i)

DI unsigned cvtpk(float lo, float hi) { f32x2 v = {lo, hi}; bf16x2_t b = __builtin_convertvector(v, bf16x2_t); return __builtin_bit_cast(unsigned, b); }
DI void dma(unsigned voff, const char* sbase, unsigned l) {
    asm volatile("s_mov_b32 m0, %2\n\ts_nop 0\n\tglobal_load_lds_dwordx4 %0, %1" :: "v"(voff), "s"(sbase), "s"(l) : "memory"); }
DI void dma_dev(unsigned voff, const char* sbase, unsigned l) {
    asm volatile("s_mov_b32 m0, %2\n\ts_nop 0\n\tglobal_load_lds_dwordx4 %0, %1 sc1" :: "v"(voff), "s"(sbase), "s"(l) : "memory"); }
DI void ldsw128(LAS char* p, u32x4 v) { *(LAS u32x4*)p = v; }
DI bf16x8 ldsr128(const LAS char* p) { return *(const LAS bf16x8*)p; }
DI bf16x8 ldstr(const LAS char* p, int off2) {
    s16x4 a = __builtin_amdgcn_ds_read_tr16_b64_v4i16((LAS s16x4*)p);
    s16x4 b = __builtin_amdgcn_ds_read_tr16_b64_v4i16((LAS s16x4*)(p + off2));
    return (bf16x8){a[0], a[1], a[2], a[3], b[0], b[1], b[2], b[3]};
}
DI int lane_now() { int l; asm volatile("v_mbcnt_lo_u32_b32 %0, -1, 0\n\tv_mbcnt_hi_u32_b32 %0, -1, %0" : "=v"(l)); return l; }
DI float bflo(unsigned u) { return __uint_as_float(u << 16); }
DI float bfhi(unsigned u) { return __uint_as_float(u & 0xffff0000u); }
DI u32x4 swap16(u32x2 a, u32x2 b) {
    auto r0 = __builtin_amdgcn_permlane16_swap(a[0], b[0], false, false);
    auto r1 = __builtin_amdgcn_permlane16_swap(a[1], b[1], false, false);
    return (u32x4){r0[0], r1[0], r0[1], r1[1]};
}
DI u32x2 pack4(f32x4 v) { return (u32x2){cvtpk(v[0], v[1]), cvtpk(v[2], v[3])}; }
typedef __amdgpu_buffer_rsrc_t rsrc_t;
DI void st_wt(rsrc_t r, unsigned off, u32x4 v) { __builtin_amdgcn_raw_buffer_store_b128(v, r, off, 0, 16); }
DI u32x4 ld_dev(rsrc_t r, unsigned off) { return __builtin_amdgcn_raw_buffer_load_b128(r, off, 0, 16); }
DI void wait_flags(const char* first, int n, u64 lo, u64 span, int lane) {
    const GAS u64* p = (const GAS u64*)(first + (lane < n ? lane : 0) * 64);
    for (unsigned spins = 0; spins < (1u << 16); ++spins) {
        const u64 v = __hip_atomic_load(p, RLX_AGENT);
        if (__all(v - lo <= span)) break;
        __builtin_amdgcn_s_sleep(1);
    }
}
DI void set_flag(char* p, u64 v, int lane) { if (lane == 0) __hip_atomic_store((GAS u64*)p, v, RLX_AGENT); }

#define ABL_FDMA 0
#define ABL_FMFMA 0
#define ABL_ATT 0
#define PADN 0
#define FN_PAD(g) do { _Pragma("unroll") for (int z_ = 0; z_ < PADN; ++z_) asm volatile("s_nop 3"); SBAR(); } while (0)
#define FN_PRIO 1
#define PF_MODE 0
#define TLB_WARM 2
#define TLB_SUB 0
#define PF_N0 3
#define PF_LEAD 8
#define PF_SLEEP 16
constexpr int K1_STAGE = 53248, K1_A = 0, K1_WD = 32768, K1_WQ = 40960;
constexpr int AT_STAGE = 8192, FB_BASE = 32768, FW_STAGE = 32768, FW_TOP = 131072;
struct K1Frag { bf16x8 x[4], wd[2], wqk[2], wv; };
struct FnFrag { bf16x8 w[4], a[4]; };

DI void attn_store(rsrc_t rs, unsigned off, const f32x16& o, float l) {
    const float rl = 1.0f / l; u32x4 w0, w1;
#pragma unroll
    for (int i = 0; i < 4; ++i) { w0[i] = cvtpk(o[2 * i] * rl, o[2 * i + 1] * rl); w1[i] = cvtpk(o[8 + 2 * i] * rl, o[9 + 2 * i] * rl); }
    st_wt(rs, off, w0); st_wt(rs, off + 256, w1);
}
DI bf16x8 attn_sel(int lane) {
    const short v = (((lane >> 2) & 1) == ((lane >> 4) & 1)) ? (short)0x3f80 : (short)0;
    return (bf16x8){v, v, v, v, v, v, v, v};
}
DI float max16(const f32x16& s) {
    float a = fmaxf(fmaxf(s[0], s[1]), s[2]), b = fmaxf(fmaxf(s[3], s[4]), s[5]);
#pragma unroll
    for (int r = 6; r < 16; r += 4) { a = fmaxf(fmaxf(a, s[r]), s[r + 1]); if (r + 3 < 16) b = fmaxf(fmaxf(b, s[r + 2]), s[r + 3]); }
    return fmaxf(a, b);
}
DI void attn_slow(rsrc_t rs, const char* kv, bf16x8 q0, bf16x8 q1, int lane, unsigned ooff) {
    __builtin_amdgcn_fence(__ATOMIC_ACQUIRE, "agent");
    const bf16x8 sel = attn_sel(lane);
    kv += lane * 16;
    float mx = -INFINITY;
    const f32x16 z = f32x16{};
#pragma unroll 1
    for (int j = 0; j < 64; ++j) {
        const char* kp = kv + (j >> 1) * 8192 + (j & 1) * 2048;
        f32x16 s = MFMA32(*(const bf16x8*)kp, q0, z); s = MFMA32(*(const bf16x8*)(kp + 1024), q1, s);
        mx = fmaxf(mx, max16(s));
    }
    mx = fmaxf(mx, __shfl_xor(mx, 32));
    f32x16 negm, o = f32x16{}; f32x4 l = f32x4{};
#pragma unroll
    for (int r = 0; r < 16; ++r) negm[r] = -mx;
#pragma unroll 1
    for (int j = 0; j < 64; ++j) {
        const char* kp = kv + (j >> 1) * 8192 + (j & 1) * 2048;
        const char* vp = kv + ((j & 1) ? (((j & 7) == 7 ? (j >> 3) * 4 : (j + 1) >> 1) * 8192 + 4096) : ((j >> 1) * 8192 + 6144));
        const bf16x8 v0 = *(const bf16x8*)vp, v1 = *(const bf16x8*)(vp + 1024);
        f32x16 s = MFMA32(*(const bf16x8*)kp, q0, negm); s = MFMA32(*(const bf16x8*)(kp + 1024), q1, s);
        u32x4 p0, p1;
#pragma unroll
        for (int r = 0; r < 16; ++r) s[r] = __builtin_amdgcn_exp2f(s[r]);
#pragma unroll
        for (int r = 0; r < 4; ++r) { p0[r] = cvtpk(s[2 * r], s[2 * r + 1]); p1[r] = cvtpk(s[8 + 2 * r], s[9 + 2 * r]); }
        o = MFMA32(v0, BC(p0), o); l = MFMA16(sel, BC(p0), l);
        o = MFMA32(v1, BC(p1), o); l = MFMA16(sel, BC(p1), l);
    }
    attn_store(rs, ooff, o, l[0]);
}

DI void touch8k(const char* p, unsigned voff, unsigned dump) {
    asm volatile("s_mov_b32 m0, %2\n\ts_nop 0\n\tglobal_load_lds_dword %0, %1" :: "v"(voff), "s"(p), "s"(dump) : "memory"); }
DI void touch_pair(int p, int l, int mt, int hd, const char* x, const char* wdb, const char* wqkv, unsigned dump) {
    const int n = l >= 56 ? l - 8 : l, i = (mt * 2 + (hd & 1)) * 24 + (n - 32);
    const char* a = n < 16 ? x + (size_t)(mt * 256 + hd * 8 + (n >> 1)) * 2048 + p * 256 + (n & 1) * 128
                  : n < 32 ? wdb + (size_t)(mt * 8 + ((n - 16) >> 1)) * 2048 + p * 256 + (n & 1) * 128
                           : wqkv + (size_t)(p * 128 + i / 3) * 6144 + (i % 3) * 2048 + (hd >> 1) * 128;
    asm volatile("s_mov_b32 m0, %1\n\ts_nop 0\n\tglobal_load_lds_dword %0, off" :: "v"(a), "s"(dump) : "memory");
}
DI void main_waves(LAS char* smem, int wid, int lane, int mt, int hd, char* ws, rsrc_t rs, u16* __restrict__ out,
                   const u16* __restrict__ x, const u16* __restrict__ gw, const char* wdb, const char* wqkv, const char* wsb, const char* pf2) {
    const unsigned dump = (unsigned)(size_t)smem + 3 * K1_STAGE + wid * 256;
    if (wid >= 1 && wid <= 3) { for (int z = 0; z < wid; ++z) __builtin_amdgcn_s_sleep(PF_SLEEP); touch_pair(wid, lane, mt, hd, (const char*)x, wdb, wqkv, dump); }
    if (wid == 0 && lane < 8) {
        const int rt_ = 2 * (blockIdx.x & 7) + (blockIdx.x >> 7), nb_ = ((blockIdx.x >> 3) & 15) * 64;
        char* p = lane == 0 ? ws + WS_KV + (hd * 32 + 4 * mt) * 8192 : lane == 1 ? ws + WS_AB + (hd * 32 + 4 * mt) * 8192
                : lane == 2 ? ws + WS_O + (mt * 16 * 32 + hd) * 1024 : lane == 3 ? ws + WS_Y + (hd >> 4) * 4 * MB + (mt * 256 * DM + (hd & 15) * 64) * 2
                : lane == 4 ? ws + WS_GATE + (256 * mt + 8 * hd) * 4 : (char*)(out + (size_t)rt_ * 128 * DM + nb_);
        if (lane < 6) __hip_atomic_store((GAS unsigned*)p, 0u, RLX_AGENT);
        else {
            const char* q = lane == 6 ? pf2 : ws + WS_Y + ((hd >> 4) ^ 1) * 4 * MB + (size_t)((rt_ ^ 2) * 128 * DM) * 2;
            asm volatile("s_mov_b32 m0, %1\n\ts_nop 0\n\tglobal_load_lds_dword %0, off" :: "v"(q), "s"(dump) : "memory");
        }
    }
    const int t_act = wid < 4 ? 7 + 2 * wid : 2 * wid - PF_LEAD;
#define TW(l_) (unsigned)(((hd & 15) * 16 + ((l_) >> 2)) * 4096 + ((l_) & 3) * 128)
    int fr = lane & 15, fq = lane >> 4;
    u32x2 ep; u64 base; u32x4 gx0, gx1, gw0, gw1;
    {
        const int wm = wid >> 1, wn = wid & 1;
        const int cpart = ((fq ^ (fr >> 1)) & 7) * 16;
        const int aA = K1_A + (64 * wm + fr) * 128 + cpart, aD = K1_WD + (32 * wn + fr) * 128 + cpart;
        const int aT0 = (8 * fq + (fr >> 2)) * 64 + ((((lane & 3) >> 1) ^ ((fq & 1) << 1)) * 16) + (lane & 1) * 8;
        const int aQ = K1_WQ + wn * 4096 + aT0, aV = K1_WQ + 8192 + (aT0 ^ (wn * 32));
        f32x4 accQ[2][4], accV[4], accD[2][4];
#pragma unroll
        for (int m = 0; m < 4; ++m) { accQ[0][m] = f32x4{}; accQ[1][m] = f32x4{}; accV[m] = f32x4{}; accD[0][m] = f32x4{}; accD[1][m] = f32x4{}; }
        K1Frag X, Y;
#define K1_RD(F, i, slot, ks) do { const LAS char* sb_ = smem + (slot); \
        if ((i) == 0) F.wqk[0] = ldstr(sb_ + aQ + (ks) * 2048, 256); \
        else if ((i) == 1) { F.x[0] = ldsr128(sb_ + (aA ^ ((ks) * 64))); } \
        else if ((i) == 2) F.wqk[1] = ldstr(sb_ + (aQ ^ 32) + (ks) * 2048, 256); \
        else if ((i) == 3) F.wv = ldstr(sb_ + aV + (ks) * 2048, 256); \
        else if ((i) == 4) { F.wd[0] = ldsr128(sb_ + (aD ^ ((ks) * 64))); F.wd[1] = ldsr128(sb_ + (aD ^ ((ks) * 64)) + 2048); } \
        else if ((i) < 8) F.x[(i) - 4] = ldsr128(sb_ + (aA ^ ((ks) * 64)) + ((i) - 4) * 2048); } while (0)
#define K1_MF(F, j) do { constexpr int m_ = (j) / 5, r_ = (j) % 5; \
        if (r_ == 0) accQ[0][m_] = MFMA16(F.wqk[0], F.x[m_], accQ[0][m_]); \
        else if (r_ == 1) accQ[1][m_] = MFMA16(F.wqk[1], F.x[m_], accQ[1][m_]); \
        else if (r_ == 2) accV[m_] = MFMA16(F.x[m_], F.wv, accV[m_]); \
        else if (r_ == 3) accD[0][m_] = MFMA16(F.x[m_], F.wd[0], accD[0][m_]); \
        else accD[1][m_] = MFMA16(F.x[m_], F.wd[1], accD[1][m_]); } while (0)
#define K1_G(g, C, R, rslot, ks) do { K1_MF(C, 2 * (g)); K1_RD(R, g, rslot, ks); K1_MF(C, 2 * (g) + 1); SBAR(); } while (0)
#define K1_PHASE(C, R, rslot, ks) do { K1_G(0, C, R, rslot, ks); K1_G(1, C, R, rslot, ks); K1_G(2, C, R, rslot, ks); K1_G(3, C, R, rslot, ks); K1_G(4, C, R, rslot, ks); \
        K1_G(5, C, R, rslot, ks); K1_G(6, C, R, rslot, ks); K1_G(7, C, R, rslot, ks); K1_G(8, C, R, rslot, ks); K1_G(9, C, R, rslot, ks); } while (0)
        LGKM_BAR();
        if (wid == 0) STAMP(0, 1);
#pragma unroll
        for (int i = 0; i < 8; ++i) K1_RD(X, i, 0, 0);
        int sl = 0, sn = K1_STAGE;
        SBAR();
        for (int t = 0; t < 15; ++t) {
            K1_PHASE(X, Y, sl, 1);
            LGKM_BAR(); SBAR();
            TICK(0, t);
            if (t == t_act) {
                const int l_ = lane_now();
                if (wid < 4) touch8k(wsb + ((mt + wid) & 7) * 512, TW(l_), dump);
                else touch_pair(wid, l_, mt, hd, (const char*)x, wdb, wqkv, dump);
            }
            SBAR();
            K1_PHASE(Y, X, sn, 0);
            sl = sn; sn = (sn == 2 * K1_STAGE) ? 0 : sn + K1_STAGE;
        }
        K1_PHASE(X, Y, 0, 1);
        WAIT_BAR(0); SBAR();
        lane = lane_now();
        asm volatile("global_load_dwordx2 %4, %7, off sc1\n\tglobal_load_dwordx4 %0, %5, off\n\tglobal_load_dwordx4 %1, %5, off offset:16\n\t"
                     "global_load_dwordx4 %2, %6, off\n\tglobal_load_dwordx4 %3, %6, off offset:16"
                     : "=&v"(gx0), "=&v"(gx1), "=&v"(gw0), "=&v"(gw1), "=&v"(ep)
                     : "v"((const char*)x + (size_t)(256 * mt + 8 * hd + wid) * 2048 + lane * 32), "v"((const char*)gw + lane * 32), "v"(ws) : "memory");
        SBAR();
        K1_MF(Y, 0); K1_MF(Y, 1); K1_MF(Y, 2); K1_MF(Y, 3); K1_MF(Y, 4); K1_MF(Y, 5); K1_MF(Y, 6); K1_MF(Y, 7); K1_MF(Y, 8); K1_MF(Y, 9);
        K1_MF(Y, 10); K1_MF(Y, 11); K1_MF(Y, 12); K1_MF(Y, 13); K1_MF(Y, 14); K1_MF(Y, 15); K1_MF(Y, 16); K1_MF(Y, 17); K1_MF(Y, 18); K1_MF(Y, 19);
        if (wid == 0) STAMP(0, 2);
        fr = lane & 15; fq = lane >> 4;
        const float qs = wn ? 1.0f : 0.17677669529663687f * 1.4426950408889634f;
#pragma unroll
        for (int m = 0; m < 4; ++m) {
            const u32x4 w = swap16(pack4(accQ[0][m] * qs), pack4(accQ[1][m] * qs));
            const int r = (16 * m + fr) & 31, rho = (r & 19) | ((r & 4) << 1) | ((r & 8) >> 1), j = 2 * wm + (m >> 1);
            const int in = (fq & 1) * 1024 + ((fq >> 1) * 32 + rho) * 16;
            ldsw128(smem + (wn ? wm * AT_STAGE + (m >> 1) * 2048 : FW_TOP - 2 * FW_STAGE + j * 2048) + in, w);
        }
        const int rv = 8 * (fr >> 2) + 4 * wn + (fr & 3);
#pragma unroll
        for (int i = 0; i < 2; ++i) {
            const int in = (fq & 1) * 1024 + ((fq >> 1) * 32 + rv) * 16;
            const u32x4 w = swap16(pack4(accV[2 * i]), pack4(accV[2 * i + 1]));
            ldsw128(smem + ((wm + i) & 3) * AT_STAGE + 6144 - i * 2048 + in, w);
#pragma unroll
            for (int n = 0; n < 2; ++n) {
                const u32x4 d = swap16(pack4(accD[n][2 * i]), pack4(accD[n][2 * i + 1]));
                const int el = 32 * wn + 16 * n + fr, ch = 4 * i + 2 * (fq & 1) + (fq >> 1);
                ldsw128(smem + FB_BASE + wm * 8192 + el * 128 + ((ch ^ (fr >> 1)) * 16), d);
            }
        }
        LGKM_BAR();
        if (wid == 0) STAMP(0, 3);
    }
    {
        const int qt = mt * 8 + wid;
        char* f1 = ws + WS_F1 + hd * 512;
        const unsigned rec0 = (hd * 32 + 4 * mt) * 8192 + wid * 1024 + lane * 16;
#define PUBLISH(q) do { const u32x4 a_ = *(const LAS u32x4*)(smem + (q) * 8192 + wid * 1024 + lane * 16), b_ = *(const LAS u32x4*)(smem + FB_BASE + (q) * 8192 + wid * 1024 + lane * 16); \
        st_wt(rs, WS_KV + rec0 + (q) * 8192, a_); st_wt(rs, WS_AB + rec0 + (q) * 8192, b_); } while (0)
        PUBLISH(0); PUBLISH(1);
        const bf16x8 sel = attn_sel(lane);
        const LAS char* fb = smem + lane * 16;
        const bf16x8 q0 = ldsr128(fb + FW_TOP - 2 * FW_STAGE + wid * 2048), q1 = ldsr128(fb + FW_TOP - 2 * FW_STAGE + wid * 2048 + 1024);
        bf16x8 kA0, kA1, kB0, kB1, vA0, vA1, vB0, vB1 = bf16x8{};
        f32x16 negm, o = f32x16{}, sA, sB; f32x4 l = f32x4{};
        u32x4 p0, p1 = u32x4{};
        unsigned sink = 0; u64 fv = 0;
        kA0 = ldsr128(fb); kA1 = ldsr128(fb + 1024); kB0 = ldsr128(fb + 2048); kB1 = ldsr128(fb + 3072); vA0 = ldsr128(fb + 6144); vA1 = ldsr128(fb + 7168);
        LGKM_BAR();
        {
            sA = MFMA32(kA0, q0, o); sA = MFMA32(kA1, q1, sA);
            float mx = max16(sA); mx = fmaxf(mx, __shfl_xor(mx, 32)) + 40.f;
#pragma unroll
            for (int r = 0; r < 16; ++r) { negm[r] = -mx; sA[r] -= mx; }
        }
        asm volatile("s_waitcnt vmcnt(4)" : "+v"(ep), "+v"(gx0), "+v"(gx1), "+v"(gw0), "+v"(gw1) :: "memory");
        {
            float s = 0.f;
#pragma unroll
            for (int i = 0; i < 4; ++i) {
                s = fmaf(bflo(gx0[i]), bflo(gw0[i]), s); s = fmaf(bfhi(gx0[i]), bfhi(gw0[i]), s);
                s = fmaf(bflo(gx1[i]), bflo(gw1[i]), s); s = fmaf(bfhi(gx1[i]), bfhi(gw1[i]), s);
            }
#pragma unroll
            for (int o = 32; o >= 1; o >>= 1) s += __shfl_xor(s, o);
            if (lane == 0) __builtin_amdgcn_raw_buffer_store_b32(__float_as_uint(1.f / (1.f + __expf(-s))), rs, WS_GATE + (256 * mt + 8 * hd + wid) * 4, 0, 16);
        }
        base = (((u64)(unsigned)__builtin_amdgcn_readfirstlane(ep[1]) << 32) | (unsigned)__builtin_amdgcn_readfirstlane(ep[0])) + 8;
        if (wid == 0) STAMP(1, 0);
#define EX(S_, i) S_[i] = __builtin_amdgcn_exp2f(S_[i])
#define AT_STEP(SC, SN, K0, K1, VC0, VP1, RD0, RD1, RD2, RD3) do { \
        SN = MFMA32(K0, q0, negm); SBAR(); RD0; EX(SC, 0); EX(SC, 1); EX(SC, 2); EX(SC, 3); SBAR(); \
        o = MFMA32(VP1, BC(p1), o); PIN(o); SBAR(); RD1; EX(SC, 4); EX(SC, 5); EX(SC, 6); EX(SC, 7); p0[0] = cvtpk(SC[0], SC[1]); p0[1] = cvtpk(SC[2], SC[3]); SBAR(); \
        l = MFMA16(sel, BC(p1), l); PIN(l); SBAR(); p0[2] = cvtpk(SC[4], SC[5]); p0[3] = cvtpk(SC[6], SC[7]); EX(SC, 8); SBAR(); \
        SN = MFMA32(K1, q1, SN); SBAR(); RD2; EX(SC, 9); EX(SC, 10); EX(SC, 11); EX(SC, 12); SBAR(); \
        o = MFMA32(VC0, BC(p0), o); PIN(o); SBAR(); RD3; EX(SC, 13); EX(SC, 14); EX(SC, 15); p1[0] = cvtpk(SC[8], SC[9]); SBAR(); \
        l = MFMA16(sel, BC(p0), l); PIN(l); SBAR(); p1[1] = cvtpk(SC[10], SC[11]); p1[2] = cvtpk(SC[12], SC[13]); p1[3] = cvtpk(SC[14], SC[15]); SBAR(); } while (0)
        int sl = AT_STAGE;
        for (int T = 1; T <= 32; ++T) {
            if (T <= 4) {
                asm volatile("s_waitcnt vmcnt(0)" : "+v"(fv) :: "memory");
                if (wid == 1 && (T == 2 || (T == 4 && !__all(fv == base + 2)))) wait_flags(f1, 8, base + (T >> 1), 2 - (T >> 1), lane);
            }
            LGKM_BAR(); SBAR();
            TICK(1, T);
            if (T <= 3) {
                if (T <= 2 && wid == 0) set_flag(f1 + mt * 64, base + T, lane);
                if (T == 1) { PUBLISH(2); PUBLISH(3); }
                if (T == 3 && wid == 1) asm volatile("global_load_dwordx2 %0, %1, off sc1" : "+v"(fv) : "v"(f1 + (lane & 7) * 64) : "memory");
            }
            if (T == 4 * wid - 8 && T > 5)
                { asm volatile("global_load_dword %0, %1, %2" : "+v"(sink) : "v"(TW(lane)), "s"(wsb + ((mt + wid) & 7) * 512) : "memory");
                  if (wid == 7) asm volatile("global_load_dword %0, %1, %2" : "+v"(sink) : "v"(lane * 128), "s"(pf2) : "memory"); }
            SBAR();
            if (!ABL_ATT) {
            AT_STEP(sA, sB, kB0, kB1, vA0, vB1, kA0 = ldsr128(fb + sl), kA1 = ldsr128(fb + sl + 1024), vB0 = ldsr128(fb + sl + 4096), vB1 = ldsr128(fb + sl + 5120));
            AT_STEP(sB, sA, kA0, kA1, vB0, vA1, kB0 = ldsr128(fb + sl + 2048), kB1 = ldsr128(fb + sl + 3072), vA0 = ldsr128(fb + sl + 6144), vA1 = ldsr128(fb + sl + 7168)); }
            sl = (sl + AT_STAGE) & (4 * AT_STAGE - 1);
        }
        o = MFMA32(vB1, BC(p1), o); l = MFMA16(sel, BC(p1), l);
        if (wid == 0) STAMP(1, 1);
        bool bad = !(l[0] >= 7.8886e-31f && l[0] <= 3.0e38f);
#pragma unroll
        for (int r = 0; r < 16; ++r) bad |= !(fabsf(o[r]) <= 3.0e38f);
        const int r = lane & 31, qrow = qt * 32 + ((r & 19) | ((r & 4) << 1) | ((r & 8) >> 1));
        const unsigned ooff = WS_O + ((qrow >> 4) * 32 + hd) * 1024 + ((lane >> 5) * 32 + (qrow & 15)) * 16;
        if (!ABL_ATT && !ABL_FDMA && __builtin_expect(__any(bad), 0)) attn_slow(rs, ws + WS_KV + hd * 262144, q0, q1, lane, ooff);
        else attn_store(rs, ooff, o, l[0]);
        WAIT_BAR(0);
        asm volatile("" :: "v"(sink));
        if (wid == 0) STAMP(1, 2);
    }
    {
        const int rt = 2 * (blockIdx.x & 7) + (blockIdx.x >> 7), nb = ((blockIdx.x >> 3) & 15) * 64;
        const int kq = fr >> 2, swz = ((kq >> 1) & 1) | ((fq & 1) << 1);
        const int aW = (8 * fq + kq) * 128 + ((((lane & 3) >> 1) ^ (swz << 1)) * 16) + (lane & 1) * 8;
#define P3_RD(W_, ks_) do { const int kt_ = (ks_) >> 1; \
            const int wb_ = (kt_ < 4 ? FW_TOP - 2 * FW_STAGE + kt_ * 8192 : kt_ == 4 ? FB_BASE : kt_ < 9 ? FW_TOP + (kt_ - 5) * 8192 : kt_ == 9 ? FB_BASE + 8192 \
                              : kt_ < 14 ? FW_TOP - FW_STAGE + (kt_ - 10) * 8192 : FB_BASE + (kt_ - 12) * 8192) + ((ks_) & 1) * 4096; \
            _Pragma("unroll") for (int n_ = 0; n_ < 4; ++n_) W_[n_] = ldstr(smem + wb_ + (aW ^ (n_ * 32)), 512); } while (0)
        bf16x8 wf[2][4];
        P3_RD(wf[0], 0);
        if (wid == 0) {
            set_flag(ws + WS_F2 + (mt * 32 + hd) * 64, base + 5, lane);
            wait_flags(ws + WS_F2 + (rt >> 1) * 2048, 32, base + 5, 0, lane);
            __builtin_amdgcn_fence(__ATOMIC_ACQUIRE, "wavefront");
            if (blockIdx.x == 0) set_flag(ws, base, lane);
            STAMP(1, 3);
        }
        LGKM_BAR();
        const int R0 = rt * 128 + 16 * wid;
        const int t = R0 + fr, col = nb + 16 * (fq & 1) + 8 * (fq >> 1);
        u32x4 of[24], y0[2], y1[2]; float g;
#pragma unroll
        for (int ks = 0; ks < 24; ++ks) of[ks] = ld_dev(rs, WS_O + ((R0 >> 4) * 32 + ks) * 1024 + lane * 16);
        SBAR();
        f32x4 acc[4];
#pragma unroll
        for (int n = 0; n < 4; ++n) acc[n] = f32x4{};
#pragma unroll
        for (int ks = 0; ks < 32; ++ks) {
            if (ks + 1 < 32) P3_RD(wf[(ks + 1) & 1], ks + 1);
            SBAR();
#pragma unroll
            for (int n = 0; n < 4; ++n) acc[n] = MFMA16(wf[ks & 1][n], BC(of[ks % 24]), acc[n]);
            if (ks < 8) of[ks] = ld_dev(rs, WS_O + ((R0 >> 4) * 32 + 24 + ks) * 1024 + lane * 16);
            if (ks == 12) {
                g = __uint_as_float(__builtin_amdgcn_raw_buffer_load_b32(rs, WS_GATE + t * 4, 0, 16));
#pragma unroll
                for (int i = 0; i < 2; ++i) { y0[i] = ld_dev(rs, WS_Y + (t * DM + col + 32 * i) * 2); y1[i] = ld_dev(rs, WS_Y + 4 * MB + (t * DM + col + 32 * i) * 2); }
            }
            SBAR();
        }
        const float g1 = 1.f - g;
#pragma unroll
        for (int i = 0; i < 2; ++i) {
            float v[8];
#pragma unroll
            for (int j = 0; j < 4; ++j) {
                auto r = __builtin_amdgcn_permlane16_swap(__float_as_uint(acc[2 * i][j]), __float_as_uint(acc[2 * i + 1][j]), false, false);
                v[j] = __uint_as_float(r[0]); v[4 + j] = __uint_as_float(r[1]);
            }
            u32x4 w;
#pragma unroll
            for (int j = 0; j < 4; ++j)
                w[j] = cvtpk(g * (bflo(y0[i][j]) - bflo(y1[i][j])) + g1 * v[2 * j], g * (bfhi(y0[i][j]) - bfhi(y1[i][j])) + g1 * v[2 * j + 1]);
            *(u32x4*)(out + (size_t)t * DM + col + 32 * i) = w;
        }
        if (wid == 0) STAMP(1, 4);
    }
}

DI void side_waves(LAS char* smem, int lw, int lane, int mt, int hd, const u16* __restrict__ x, const u16* __restrict__ wsr, const u16* __restrict__ wsi,
                   const u16* __restrict__ wdr, const u16* __restrict__ wdi, const u16* __restrict__ wqkv, const u16* __restrict__ wo,
                   const u16* __restrict__ gw, char* ws, rsrc_t rs) {
    const unsigned lds0 = (unsigned)(size_t)smem + lw * 1024;
    const unsigned rowl = 8 * lw + (lane >> 3), chk = ((lane & 7) ^ ((4 * lw + (lane >> 4)) & 7)) * 16;
    const int c = hd >> 4;
    const char* wb = (const char*)(c ? wsi : wsr) + (size_t)mt * 256 * 4096;
    unsigned vo[10];
    {
        const char* xb = (const char*)x + (size_t)mt * 256 * 2048;
        const char* wdb = (const char*)(c ? wdi : wdr) + (size_t)(hd & 15) * 64 * 2048;
        const char* wqb = (const char*)wqkv + hd * 64 + lw * 16 * 6144;
#pragma unroll
        for (int i = 0; i < 8; ++i) vo[i] = (rowl + 32 * i) * 2048 + chk;
        const unsigned voffW = (lane >> 2) * 6144 + (((lane & 3) ^ ((lane >> 5) << 1)) * 16);
#define K1_DMA(t, slot) do { const char* xa_ = xb + (size_t)(t) * 128; const char* wd_ = wdb + (size_t)(t) * 128; const char* wq_ = wqb + (size_t)(t) * 64 * 6144; \
        _Pragma("unroll") for (int u_ = 0; u_ < 3; ++u_) dma(voffW, wq_ + u_ * 2048, lds0 + (slot) + K1_WQ + u_ * 4096); \
        _Pragma("unroll") for (int i_ = 0; i_ < 2; ++i_) dma(vo[i_], wd_, lds0 + (slot) + K1_WD + i_ * 4096); \
        _Pragma("unroll") for (int i_ = 0; i_ < 8; ++i_) dma(vo[i_], xa_, lds0 + (slot) + K1_A + i_ * 4096); } while (0)
        K1_DMA(0, 0); K1_DMA(1, K1_STAGE); K1_DMA(2, 2 * K1_STAGE);
        WAIT_BAR(26);
        int sl = 0;
        for (int t = 0; t < 13; ++t) {
            WAIT_BAR(13);
            K1_DMA(t + 3, sl);
            sl = (sl == 2 * K1_STAGE) ? 0 : sl + K1_STAGE;
        }
        WAIT_BAR(13);
        WAIT_BAR(0);
#pragma unroll
        for (int i = 0; i < 8; ++i) vo[i] = (rowl + 32 * i) * 4096 + chk;
        vo[8] = lane * 16 + lw * 1024; vo[9] = vo[8] + 4096;
#pragma unroll
        for (int i = 0; i < 7; ++i) dma(vo[i], wb + (size_t)((4 * mt) & 31) * 128, lds0 + FW_TOP + i * 4096);
#pragma unroll
        for (int i = 0; i < 8; ++i) dma(vo[i], wb + (size_t)((4 * mt + 1) & 31) * 128, lds0 + FW_TOP - FW_STAGE + i * 4096);
        LGKM_BAR();
        dma(vo[7], wb + (size_t)((4 * mt) & 31) * 128, lds0 + FW_TOP + 7 * 4096);
        WAIT_BAR(0);
    }
    {
        const char* kvb = ws + WS_KV + hd * 262144;
        const unsigned vl = lane * 16 + lw * 1024;
        const char *bW = wb, *bA = ws + WS_AB + hd * 262144; int kmask = 31, kstep = 128, kstepA = 8192, koff = 4 * mt;
#define FN_DMA_P(p, s, fslot, aslot) do { \
        if ((p) < 8) dma(vo[p], bW + (size_t)((koff + (s)) & kmask) * kstep, lds0 + (fslot) + (p) * 4096); \
        else if ((p) < 10) dma_dev(vo[p], bA + (size_t)((koff + (s)) & kmask) * kstepA, lds0 + FB_BASE + (aslot) + ((p) - 8) * 4096); \
        else if ((p) == 10) dma_dev(vl, kvb + ((4 * mt + (s)) & 31) * 8192, lds0 + (aslot)); \
        else dma_dev(vl, kvb + ((4 * mt + (s) - ((((s) & 3) == 0 && lw < 2) ? 4 : 0)) & 31) * 8192 + 4096, lds0 + (aslot) + 4096); } while (0)
        const int fr = lane & 15, fq = lane >> 4;
        const int cpart = ((fq ^ (fr >> 1)) & 7) * 16;
        const int aW = (64 * lw + fr) * 128 + cpart, aA = FB_BASE + fr * 128 + cpart;
        f32x4 acc[4][4];
#pragma unroll
        for (int m = 0; m < 4; ++m)
#pragma unroll
            for (int n = 0; n < 4; ++n) acc[m][n] = f32x4{};
        FnFrag X, Yf;
#define FN_RD(F, i, slot, bslot, ks) do { \
        if ((i) == 0) F.w[0] = ldsr128(smem + (slot) + (aW ^ ((ks) * 64))); \
        else if ((i) < 5) F.a[(i) - 1] = ldsr128(smem + (bslot) + (aA ^ ((ks) * 64)) + ((i) - 1) * 2048); \
        else F.w[(i) - 4] = ldsr128(smem + (slot) + (aW ^ ((ks) * 64)) + ((i) - 4) * 2048); } while (0)
#define FN_MF(F, j) do { if (!ABL_FMFMA) acc[(j) >> 2][(j) & 3] = MFMA16(F.a[(j) & 3], F.w[(j) >> 2], acc[(j) >> 2][(j) & 3]); else asm volatile("" :: "v"(F.a[(j) & 3]), "v"(F.w[(j) >> 2])); } while (0)
#define FN_G(g, C, R, rslot, rb, ks, P0, s, fslot, aslot) do { FN_MF(C, 2 * (g)); FN_RD(R, g, rslot, rb, ks); FN_MF(C, 2 * (g) + 1); \
        if (!ABL_FDMA && ((g) & 3) != 0 && (P0) + (g) - 1 - ((g) >> 2) < PMAX) FN_DMA_P((P0) + (g) - 1 - ((g) >> 2), s, fslot, aslot); SBAR(); FN_PAD(g); } while (0)
#define FN_PHASE(C, R, rslot, rb, ks, P0, s, fslot, aslot) do { FN_G(0, C, R, rslot, rb, ks, P0, s, fslot, aslot); FN_G(1, C, R, rslot, rb, ks, P0, s, fslot, aslot); \
        FN_G(2, C, R, rslot, rb, ks, P0, s, fslot, aslot); FN_G(3, C, R, rslot, rb, ks, P0, s, fslot, aslot); FN_G(4, C, R, rslot, rb, ks, P0, s, fslot, aslot); \
        FN_G(5, C, R, rslot, rb, ks, P0, s, fslot, aslot); FN_G(6, C, R, rslot, rb, ks, P0, s, fslot, aslot); FN_G(7, C, R, rslot, rb, ks, P0, s, fslot, aslot); } while (0)
#define PMAX 12
        __builtin_amdgcn_s_setprio(FN_PRIO);
#pragma unroll
        for (int i = 0; i < 8; ++i) FN_RD(X, i, FW_TOP, 0, 0);
        LGKM_BAR();
        int f0 = FW_TOP, f1 = FW_TOP - FW_STAGE, f2 = FW_TOP - 2 * FW_STAGE;
        FN_DMA_P(0, 2, f2, 0); FN_DMA_P(1, 2, f2, 0); FN_DMA_P(2, 2, f2, 0); FN_DMA_P(3, 2, f2, 0); FN_DMA_P(4, 2, f2, 0); FN_DMA_P(5, 2, f2, 0);
        SBAR();
#define FN_ITER(t, WAITN) do { \
            FN_PHASE(X, Yf, f0, ((t) & 3) * 8192, 1, 6, (t) + 2, f2, (((t) + 2) & 3) * 8192);       \
            WAIT_BAR(WAITN); SBAR();                                                                \
            FN_SWITCH(t); \
            FN_PHASE(Yf, X, f1, (((t) + 1) & 3) * 8192, 0, 0, (t) + 3, f0, 0);                      \
            const int ft = f0; f0 = f1; f1 = f2; f2 = ft; } while (0)
#define FN_SWITCH(t)
#undef PMAX
#define PMAX 8
        FN_ITER(0, 8); FN_ITER(1, 8);
#undef PMAX
#define PMAX 12
#undef FN_SWITCH
#define FN_SWITCH(t) do { if ((t) == 29) { bW = bA = (const char*)wo + ((blockIdx.x >> 3) & 15) * 128; kmask = 3; kstep = kstepA = 320 * 2048; koff = 0; \
                const unsigned cw = ((lane & 7) ^ ((((lane >> 4) & 1) | ((lw & 1) << 1)) << 1)) * 16; \
                _Pragma("unroll") for (int i = 0; i < 10; ++i) vo[i] = (rowl + 32 * i) * 2048 + cw; } SBAR(); } while (0)
        for (int t = 2; t < 31; ++t) FN_ITER(t, 12);
#undef PMAX
#define PMAX 10
        FN_PHASE(X, Yf, f0, 3 * 8192, 1, 6, 33, f2, 8192);
        WAIT_BAR(10); SBAR();
#pragma unroll
        for (int i = 0; i < 8; ++i) dma(vo[i], bW + (size_t)640 * 2048, lds0 + FW_TOP - FW_STAGE + i * 4096);
#pragma unroll
        for (int i = 0; i < 4; ++i) dma(vo[i], bW + (size_t)896 * 2048, lds0 + FB_BASE + 2 * 8192 + i * 4096);
#pragma unroll
        for (int j = 0; j < 16; ++j) FN_MF(Yf, j);
        const unsigned yb = WS_Y + c * 4 * MB + ((mt * 256 + 64 * lw + fr) * DM + (hd & 15) * 64 + 16 * (fq & 1) + 8 * (fq >> 1)) * 2;
#pragma unroll
        for (int m = 0; m < 4; ++m)
#pragma unroll
            for (int i = 0; i < 2; ++i) st_wt(rs, yb + (m * 16 * DM + 32 * i) * 2, swap16(pack4(acc[m][2 * i]), pack4(acc[m][2 * i + 1])));
        WAIT_BAR(0);
    }
}

__global__ __launch_bounds__(768, 3) void fused_kernel(const u16* __restrict__ x, const u16* __restrict__ wsr, const u16* __restrict__ wsi,
                                                        const u16* __restrict__ wdr, const u16* __restrict__ wdi, const u16* __restrict__ wqkv,
                                                        const u16* __restrict__ wo, const u16* __restrict__ gw, u16* __restrict__ out, char* ws) {
    __shared__ __attribute__((aligned(1024))) char smem_[163840];
    LAS char* smem = (LAS char*)smem_;
    const int tid = threadIdx.x, lane = tid & 63, wid = __builtin_amdgcn_readfirstlane(tid >> 6);
    const int xcd = blockIdx.x & 7, idx = blockIdx.x >> 3;
    const int hd = xcd * 4 + (idx >> 3), mt = idx & 7;
    const rsrc_t rs = __builtin_amdgcn_make_buffer_rsrc(ws, 0, WS_END, 0x00020000);
    if (wid == 0) STAMP(0, 0);
    if (wid == 4) {
        const unsigned pg = (blockIdx.x >> 3) * 16 + (lane & 15);
        const char* q = pg < 128 ? (const char*)wsr + (size_t)pg * 65536 : pg < 256 ? (const char*)wsi + (size_t)(pg - 128) * 65536
                      : pg < 352 ? (const char*)wqkv + (size_t)(pg - 256) * 65536 : pg < 416 ? (const char*)x + (size_t)(pg - 352) * 65536
                      : pg < 448 ? (const char*)wdr + (size_t)(pg - 416) * 65536 : pg < 480 ? (const char*)wdi + (size_t)(pg - 448) * 65536 : (const char*)wo + (size_t)(pg - 480) * 65536;
        q += (lane >> 4) * TLB_SUB;
        asm volatile("s_mov_b32 m0, %1\n\ts_nop 0\n\tglobal_load_lds_dword %0, off" :: "v"(q), "s"((unsigned)(size_t)smem + 3 * K1_STAGE + 1024) : "memory");
    }
    if (wid < 8) {
        const char* wdb = (const char*)((hd >> 4) ? wdi : wdr) + (size_t)(hd & 15) * 64 * 2048;
        const char* wsb = (const char*)((hd >> 4) ? wsi : wsr) + (size_t)mt * 256 * 4096;
        const char* pf2 = (const char*)wo + (size_t)blockIdx.x * 8192;
        main_waves(smem, wid, lane, mt, hd, ws, rs, out, x, gw, wdb, (const char*)wqkv, wsb, pf2);
    }
    else side_waves(smem, wid - 8, lane, mt, hd, x, wsr, wsi, wdr, wdi, wqkv, wo, gw, ws, rs);
}

extern "C" void kernel_launch(void* const* d_in, const int* in_sizes, int n_in, void* d_out, int out_size, void* d_ws, size_t ws_size, hipStream_t stream) {
    const u16* x = (const u16*)d_in[0]; const u16* wsr = (const u16*)d_in[1]; const u16* wsi = (const u16*)d_in[2];
    const u16* wdr = (const u16*)d_in[3]; const u16* wdi = (const u16*)d_in[4]; const u16* wqkv = (const u16*)d_in[6];
    const u16* wo = (const u16*)d_in[7]; const u16* gw = (const u16*)d_in[8];
    fused_kernel<<<256, 768, 0, stream>>>(x, wsr, wsi, wdr, wdi, wqkv, wo, gw, (u16*)d_out, (char*)d_ws);
}
```

```cpp
#define DIAG_A 2
#define DIAG_B 4
#include <hip/hip_runtime.h>
#include <hip/hip_bf16.h>

typedef unsigned short u16;
typedef short bf16x8 __attribute__((ext_vector_type(8)));
typedef short s16x4 __attribute__((ext_vector_type(4)));
typedef float f32x2 __attribute__((ext_vector_type(2)));
typedef float f32x4 __attribute__((ext_vector_type(4)));
typedef float f32x16 __attribute__((ext_vector_type(16)));
typedef unsigned u32x4 __attribute__((ext_vector_type(4)));
typedef unsigned u32x2 __attribute__((ext_vector_type(2)));
typedef __bf16 bf16x2_t __attribute__((ext_vector_type(2)));
#define LAS __attribute__((address_space(3)))
#define GAS __attribute__((address_space(1)))
#define DI __device__ __forceinline__
#define SBAR() __builtin_amdgcn_sched_barrier(0)
#define PIN(x) asm volatile("" : "+v"(x))
#define MFMA16(a, b, c) __builtin_amdgcn_mfma_f32_16x16x32_bf16(a, b, c, 0, 0, 0)
#define MFMA32(a, b, c) __builtin_amdgcn_mfma_f32_32x32x16_bf16(a, b, c, 0, 0, 0)
#define WAIT_BAR(N) asm volatile("s_waitcnt vmcnt(" #N ") lgkmcnt(0)\n\ts_barrier" ::: "memory")
#define LGKM_BAR() asm volatile("s_waitcnt lgkmcnt(0)\n\ts_barrier" ::: "memory")
#define RLX_AGENT __ATOMIC_RELAXED, __HIP_MEMORY_SCOPE_AGENT
#define BC(x) __builtin_bit_cast(bf16x8, x)

constexpr int SEQ = 2048, DM = 1024;
constexpr unsigned MB = 1u << 20;
constexpr unsigned WS_F1 = 4096, WS_F2 = 4096 + 16384, WS_GATE = 65536, WS_KV = 1 * MB, WS_AB = 9 * MB, WS_O = 17 * MB, WS_Y = 21 * MB, WS_END = 29 * MB;
typedef unsigned long long u64;

#define TICK(k, i)
#define STAMP(kid, i) do { constexpr int id_ = (kid) * 4 + (i); if (id_ == DIAG_A) { dg0 = __builtin_amdgcn_s_memrealtime(); __builtin_amdgcn_s_waitcnt(0xC07F); } if (id_ == DIAG_B) { dg1 = __builtin_amdgcn_s_memrealtime(); __builtin_amdgcn_s_waitcnt(0xC07F); } } while (0)

DI unsigned cvtpk(float lo, float hi) { f32x2 v = {lo, hi}; bf16x2_t b = __builtin_convertvector(v, bf16x2_t); return __builtin_bit_cast(unsigned, b); }
DI void dma(unsigned voff, const char* sbase, unsigned l) {
    asm volatile("s_mov_b32 m0, %2\n\ts_nop 0\n\tglobal_load_lds_dwordx4 %0, %1" :: "v"(voff), "s"(sbase), "s"(l) : "memory"); }
DI void dma_dev(unsigned voff, const char* sbase, unsigned l) {
    asm volatile("s_mov_b32 m0, %2\n\ts_nop 0\n\tglobal_load_lds_dwordx4 %0, %1 sc1" :: "v"(voff), "s"(sbase), "s"(l) : "memory"); }
DI void ldsw128(LAS char* p, u32x4 v) { *(LAS u32x4*)p = v; }
DI bf16x8 ldsr128(const LAS char* p) { return *(const LAS bf16x8*)p; }
DI bf16x8 ldstr(const LAS char* p, int off2) {
    s16x4 a = __builtin_amdgcn_ds_read_tr16_b64_v4i16((LAS s16x4*)p);
    s16x4 b = __builtin_amdgcn_ds_read_tr16_b64_v4i16((LAS s16x4*)(p + off2));
    return (bf16x8){a[0], a[1], a[2], a[3], b[0], b[1], b[2], b[3]};
}
DI int lane_now() { int l; asm volatile("v_mbcnt_lo_u32_b32 %0, -1, 0\n\tv_mbcnt_hi_u32_b32 %0, -1, %0" : "=v"(l)); return l; }
DI float bflo(unsigned u) { return __uint_as_float(u << 16); }
DI float bfhi(unsigned u) { return __uint_as_float(u & 0xffff0000u); }
DI u32x4 swap16(u32x2 a, u32x2 b) {
    auto r0 = __builtin_amdgcn_permlane16_swap(a[0], b[0], false, false);
    auto r1 = __builtin_amdgcn_permlane16_swap(a[1], b[1], false, false);
    return (u32x4){r0[0], r1[0], r0[1], r1[1]};
}
DI u32x2 pack4(f32x4 v) { return (u32x2){cvtpk(v[0], v[1]), cvtpk(v[2], v[3])}; }
typedef __amdgpu_buffer_rsrc_t rsrc_t;
DI void st_wt(rsrc_t r, unsigned off, u32x4 v) { __builtin_amdgcn_raw_buffer_store_b128(v, r, off, 0, 16); }
DI u32x4 ld_dev(rsrc_t r, unsigned off) { return __builtin_amdgcn_raw_buffer_load_b128(r, off, 0, 16); }
DI void wait_flags(const char* first, int n, u64 lo, u64 span, int lane) {
    const GAS u64* p = (const GAS u64*)(first + (lane < n ? lane : 0) * 64);
    for (unsigned spins = 0; spins < (1u << 16); ++spins) {
        const u64 v = __hip_atomic_load(p, RLX_AGENT);
        if (__all(v - lo <= span)) break;
        __builtin_amdgcn_s_sleep(1);
    }
}
DI void set_flag(char* p, u64 v, int lane) { if (lane == 0) __hip_atomic_store((GAS u64*)p, v, RLX_AGENT); }

#define ABL_FDMA 0
#define ABL_FMFMA 0
#define ABL_ATT 0
#define PADN 0
#define FN_PAD(g) do { _Pragma("unroll") for (int z_ = 0; z_ < PADN; ++z_) asm volatile("s_nop 3"); SBAR(); } while (0)
#define FN_PRIO 1
#define PF_MODE 0
#define TLB_WARM 0
#define TLB_SUB 16384
#define PF_N0 3
#define PF_LEAD 8
#define PF_SLEEP 16
constexpr int K1_STAGE = 53248, K1_A = 0, K1_WD = 32768, K1_WQ = 40960;
constexpr int AT_STAGE = 8192, FB_BASE = 32768, FW_STAGE = 32768, FW_TOP = 131072;
struct K1Frag { bf16x8 x[4], wd[2], wqk[2], wv; };
struct FnFrag { bf16x8 w[4], a[4]; };

DI void attn_store(rsrc_t rs, unsigned off, const f32x16& o, float l) {
    const float rl = 1.0f / l; u32x4 w0, w1;
#pragma unroll
    for (int i = 0; i < 4; ++i) { w0[i] = cvtpk(o[2 * i] * rl, o[2 * i + 1] * rl); w1[i] = cvtpk(o[8 + 2 * i] * rl, o[9 + 2 * i] * rl); }
    st_wt(rs, off, w0); st_wt(rs, off + 256, w1);
}
DI bf16x8 attn_sel(int lane) {
    const short v = (((lane >> 2) & 1) == ((lane >> 4) & 1)) ? (short)0x3f80 : (short)0;
    return (bf16x8){v, v, v, v, v, v, v, v};
}
DI float max16(const f32x16& s) {
    float a = fmaxf(fmaxf(s[0], s[1]), s[2]), b = fmaxf(fmaxf(s[3], s[4]), s[5]);
#pragma unroll
    for (int r = 6; r < 16; r += 4) { a = fmaxf(fmaxf(a, s[r]), s[r + 1]); if (r + 3 < 16) b = fmaxf(fmaxf(b, s[r + 2]), s[r + 3]); }
    return fmaxf(a, b);
}
DI void attn_slow(rsrc_t rs, const char* kv, bf16x8 q0, bf16x8 q1, int lane, unsigned ooff) {
    __builtin_amdgcn_fence(__ATOMIC_ACQUIRE, "agent");
    const bf16x8 sel = attn_sel(lane);
    kv += lane * 16;
    float mx = -INFINITY;
    const f32x16 z = f32x16{};
#pragma unroll 1
    for (int j = 0; j < 64; ++j) {
        const char* kp = kv + (j >> 1) * 8192 + (j & 1) * 2048;
        f32x16 s = MFMA32(*(const bf16x8*)kp, q0, z); s = MFMA32(*(const bf16x8*)(kp + 1024), q1, s);
        mx = fmaxf(mx, max16(s));
    }
    mx = fmaxf(mx, __shfl_xor(mx, 32));
    f32x16 negm, o = f32x16{}; f32x4 l = f32x4{};
#pragma unroll
    for (int r = 0; r < 16; ++r) negm[r] = -mx;
#pragma unroll 1
    for (int j = 0; j < 64; ++j) {
        const char* kp = kv + (j >> 1) * 8192 + (j & 1) * 2048;
        const char* vp = kv + ((j & 1) ? (((j & 7) == 7 ? (j >> 3) * 4 : (j + 1) >> 1) * 8192 + 4096) : ((j >> 1) * 8192 + 6144));
        const bf16x8 v0 = *(const bf16x8*)vp, v1 = *(const bf16x8*)(vp + 1024);
        f32x16 s = MFMA32(*(const bf16x8*)kp, q0, negm); s = MFMA32(*(const bf16x8*)(kp + 1024), q1, s);
        u32x4 p0, p1;
#pragma unroll
        for (int r = 0; r < 16; ++r) s[r] = __builtin_amdgcn_exp2f(s[r]);
#pragma unroll
        for (int r = 0; r < 4; ++r) { p0[r] = cvtpk(s[2 * r], s[2 * r + 1]); p1[r] = cvtpk(s[8 + 2 * r], s[9 + 2 * r]); }
        o = MFMA32(v0, BC(p0), o); l = MFMA16(sel, BC(p0), l);
        o = MFMA32(v1, BC(p1), o); l = MFMA16(sel, BC(p1), l);
    }
    attn_store(rs, ooff, o, l[0]);
}

DI void touch8k(const char* p, unsigned voff, unsigned dump) {
    asm volatile("s_mov_b32 m0, %2\n\ts_nop 0\n\tglobal_load_lds_dword %0, %1" :: "v"(voff), "s"(p), "s"(dump) : "memory"); }
DI void touch_pair(int p, int l, int mt, int hd, const char* x, const char* wdb, const char* wqkv, unsigned dump) {
    const int n = l >= 56 ? l - 8 : l, i = (mt * 2 + (hd & 1)) * 24 + (n - 32);
    const char* a = n < 16 ? x + (size_t)(mt * 256 + hd * 8 + (n >> 1)) * 2048 + p * 256 + (n & 1) * 128
                  : n < 32 ? wdb + (size_t)(mt * 8 + ((n - 16) >> 1)) * 2048 + p * 256 + (n & 1) * 128
                           : wqkv + (size_t)(p * 128 + i / 3) * 6144 + (i % 3) * 2048 + (hd >> 1) * 128;
    asm volatile("s_mov_b32 m0, %1\n\ts_nop 0\n\tglobal_load_lds_dword %0, off" :: "v"(a), "s"(dump) : "memory");
}
DI void main_waves(unsigned long long dg0, LAS char* smem, int wid, int lane, int mt, int hd, char* ws, rsrc_t rs, u16* __restrict__ out,
                   const u16* __restrict__ x, const u16* __restrict__ gw, const char* wdb, const char* wqkv, const char* wsb, const char* pf2) {
    unsigned long long dg1 = 0;
    const unsigned dump = (unsigned)(size_t)smem + 3 * K1_STAGE + wid * 256;
    if (wid >= 1 && wid <= 3) { for (int z = 0; z < wid; ++z) __builtin_amdgcn_s_sleep(PF_SLEEP); touch_pair(wid, lane, mt, hd, (const char*)x, wdb, wqkv, dump); }
    const int t_act = wid < 4 ? 7 + 2 * wid : 2 * wid - PF_LEAD;
#define TW(l_) (unsigned)(((hd & 15) * 16 + ((l_) >> 2)) * 4096 + ((l_) & 3) * 128)
    int fr = lane & 15, fq = lane >> 4;
    u32x2 ep; u64 base; u32x4 gx0, gx1, gw0, gw1;
    {
        const int wm = wid >> 1, wn = wid & 1;
        const int cpart = ((fq ^ (fr >> 1)) & 7) * 16;
        const int aA = K1_A + (64 * wm + fr) * 128 + cpart, aD = K1_WD + (32 * wn + fr) * 128 + cpart;
        const int aT0 = (8 * fq + (fr >> 2)) * 64 + ((((lane & 3) >> 1) ^ ((fq & 1) << 1)) * 16) + (lane & 1) * 8;
        const int aQ = K1_WQ + wn * 4096 + aT0, aV = K1_WQ + 8192 + (aT0 ^ (wn * 32));
        f32x4 accQ[2][4], accV[4], accD[2][4];
#pragma unroll
        for (int m = 0; m < 4; ++m) { accQ[0][m] = f32x4{}; accQ[1][m] = f32x4{}; accV[m] = f32x4{}; accD[0][m] = f32x4{}; accD[1][m] = f32x4{}; }
        K1Frag X, Y;
#define K1_RD(F, i, slot, ks) do { const LAS char* sb_ = smem + (slot); \
        if ((i) == 0) F.wqk[0] = ldstr(sb_ + aQ + (ks) * 2048, 256); \
        else if ((i) == 1) { F.x[0] = ldsr128(sb_ + (aA ^ ((ks) * 64))); } \
        else if ((i) == 2) F.wqk[1] = ldstr(sb_ + (aQ ^ 32) + (ks) * 2048, 256); \
        else if ((i) == 3) F.wv = ldstr(sb_ + aV + (ks) * 2048, 256); \
        else if ((i) == 4) { F.wd[0] = ldsr128(sb_ + (aD ^ ((ks) * 64))); F.wd[1] = ldsr128(sb_ + (aD ^ ((ks) * 64)) + 2048); } \
        else if ((i) < 8) F.x[(i) - 4] = ldsr128(sb_ + (aA ^ ((ks) * 64)) + ((i) - 4) * 2048); } while (0)
#define K1_MF(F, j) do { constexpr int m_ = (j) / 5, r_ = (j) % 5; \
        if (r_ == 0) accQ[0][m_] = MFMA16(F.wqk[0], F.x[m_], accQ[0][m_]); \
        else if (r_ == 1) accQ[1][m_] = MFMA16(F.wqk[1], F.x[m_], accQ[1][m_]); \
        else if (r_ == 2) accV[m_] = MFMA16(F.x[m_], F.wv, accV[m_]); \
        else if (r_ == 3) accD[0][m_] = MFMA16(F.x[m_], F.wd[0], accD[0][m_]); \
        else accD[1][m_] = MFMA16(F.x[m_], F.wd[1], accD[1][m_]); } while (0)
#define K1_G(g, C, R, rslot, ks) do { K1_MF(C, 2 * (g)); K1_RD(R, g, rslot, ks); K1_MF(C, 2 * (g) + 1); SBAR(); } while (0)
#define K1_PHASE(C, R, rslot, ks) do { K1_G(0, C, R, rslot, ks); K1_G(1, C, R, rslot, ks); K1_G(2, C, R, rslot, ks); K1_G(3, C, R, rslot, ks); K1_G(4, C, R, rslot, ks); \
        K1_G(5, C, R, rslot, ks); K1_G(6, C, R, rslot, ks); K1_G(7, C, R, rslot, ks); K1_G(8, C, R, rslot, ks); K1_G(9, C, R, rslot, ks); } while (0)
        LGKM_BAR();
        if (wid == 0) STAMP(0, 1);
#pragma unroll
        for (int i = 0; i < 8; ++i) K1_RD(X, i, 0, 0);
        int sl = 0, sn = K1_STAGE;
        SBAR();
        for (int t = 0; t < 15; ++t) {
            K1_PHASE(X, Y, sl, 1);
            LGKM_BAR(); SBAR();
            TICK(0, t);
            if (t == t_act) {
                const int l_ = lane_now();
                if (wid < 4) touch8k(wsb + ((mt + wid) & 7) * 512, TW(l_), dump);
                else touch_pair(wid, l_, mt, hd, (const char*)x, wdb, wqkv, dump);
            }
            SBAR();
            K1_PHASE(Y, X, sn, 0);
            sl = sn; sn = (sn == 2 * K1_STAGE) ? 0 : sn + K1_STAGE;
        }
        K1_PHASE(X, Y, 0, 1);
        WAIT_BAR(0); SBAR();
        lane = lane_now();
        asm volatile("global_load_dwordx2 %4, %7, off sc1\n\tglobal_load_dwordx4 %0, %5, off\n\tglobal_load_dwordx4 %1, %5, off offset:16\n\t"
                     "global_load_dwordx4 %2, %6, off\n\tglobal_load_dwordx4 %3, %6, off offset:16"
                     : "=&v"(gx0), "=&v"(gx1), "=&v"(gw0), "=&v"(gw1), "=&v"(ep)
                     : "v"((const char*)x + (size_t)(256 * mt + 8 * hd + wid) * 2048 + lane * 32), "v"((const char*)gw + lane * 32), "v"(ws) : "memory");
        SBAR();
        K1_MF(Y, 0); K1_MF(Y, 1); K1_MF(Y, 2); K1_MF(Y, 3); K1_MF(Y, 4); K1_MF(Y, 5); K1_MF(Y, 6); K1_MF(Y, 7); K1_MF(Y, 8); K1_MF(Y, 9);
        K1_MF(Y, 10); K1_MF(Y, 11); K1_MF(Y, 12); K1_MF(Y, 13); K1_MF(Y, 14); K1_MF(Y, 15); K1_MF(Y, 16); K1_MF(Y, 17); K1_MF(Y, 18); K1_MF(Y, 19);
        if (wid == 0) STAMP(0, 2);
        fr = lane & 15; fq = lane >> 4;
        const float qs = wn ? 1.0f : 0.17677669529663687f * 1.4426950408889634f;
#pragma unroll
        for (int m = 0; m < 4; ++m) {
            const u32x4 w = swap16(pack4(accQ[0][m] * qs), pack4(accQ[1][m] * qs));
            const int r = (16 * m + fr) & 31, rho = (r & 19) | ((r & 4) << 1) | ((r & 8) >> 1), j = 2 * wm + (m >> 1);
            const int in = (fq & 1) * 1024 + ((fq >> 1) * 32 + rho) * 16;
            ldsw128(smem + (wn ? wm * AT_STAGE + (m >> 1) * 2048 : FW_TOP - 2 * FW_STAGE + j * 2048) + in, w);
        }
        const int rv = 8 * (fr >> 2) + 4 * wn + (fr & 3);
#pragma unroll
        for (int i = 0; i < 2; ++i) {
            const int in = (fq & 1) * 1024 + ((fq >> 1) * 32 + rv) * 16;
            const u32x4 w = swap16(pack4(accV[2 * i]), pack4(accV[2 * i + 1]));
            ldsw128(smem + ((wm + i) & 3) * AT_STAGE + 6144 - i * 2048 + in, w);
#pragma unroll
            for (int n = 0; n < 2; ++n) {
                const u32x4 d = swap16(pack4(accD[n][2 * i]), pack4(accD[n][2 * i + 1]));
                const int el = 32 * wn + 16 * n + fr, ch = 4 * i + 2 * (fq & 1) + (fq >> 1);
                ldsw128(smem + FB_BASE + wm * 8192 + el * 128 + ((ch ^ (fr >> 1)) * 16), d);
            }
        }
        LGKM_BAR();
        if (wid == 0) STAMP(0, 3);
    }
    {
        const int qt = mt * 8 + wid;
        char* f1 = ws + WS_F1 + hd * 512;
        const unsigned rec0 = (hd * 32 + 4 * mt) * 8192 + wid * 1024 + lane * 16;
#define PUBLISH(q) do { const u32x4 a_ = *(const LAS u32x4*)(smem + (q) * 8192 + wid * 1024 + lane * 16), b_ = *(const LAS u32x4*)(smem + FB_BASE + (q) * 8192 + wid * 1024 + lane * 16); \
        st_wt(rs, WS_KV + rec0 + (q) * 8192, a_); st_wt(rs, WS_AB + rec0 + (q) * 8192, b_); } while (0)
        PUBLISH(0); PUBLISH(1);
        const bf16x8 sel = attn_sel(lane);
        const LAS char* fb = smem + lane * 16;
        const bf16x8 q0 = ldsr128(fb + FW_TOP - 2 * FW_STAGE + wid * 2048), q1 = ldsr128(fb + FW_TOP - 2 * FW_STAGE + wid * 2048 + 1024);
        bf16x8 kA0, kA1, kB0, kB1, vA0, vA1, vB0, vB1 = bf16x8{};
        f32x16 negm, o = f32x16{}, sA, sB; f32x4 l = f32x4{};
        u32x4 p0, p1 = u32x4{};
        unsigned sink = 0; u64 fv = 0;
        kA0 = ldsr128(fb); kA1 = ldsr128(fb + 1024); kB0 = ldsr128(fb + 2048); kB1 = ldsr128(fb + 3072); vA0 = ldsr128(fb + 6144); vA1 = ldsr128(fb + 7168);
        LGKM_BAR();
        {
            sA = MFMA32(kA0, q0, o); sA = MFMA32(kA1, q1, sA);
            float mx = max16(sA); mx = fmaxf(mx, __shfl_xor(mx, 32)) + 40.f;
#pragma unroll
            for (int r = 0; r < 16; ++r) { negm[r] = -mx; sA[r] -= mx; }
        }
        asm volatile("s_waitcnt vmcnt(4)" : "+v"(ep), "+v"(gx0), "+v"(gx1), "+v"(gw0), "+v"(gw1) :: "memory");
        {
            float s = 0.f;
#pragma unroll
            for (int i = 0; i < 4; ++i) {
                s = fmaf(bflo(gx0[i]), bflo(gw0[i]), s); s = fmaf(bfhi(gx0[i]), bfhi(gw0[i]), s);
                s = fmaf(bflo(gx1[i]), bflo(gw1[i]), s); s = fmaf(bfhi(gx1[i]), bfhi(gw1[i]), s);
            }
#pragma unroll
            for (int o = 32; o >= 1; o >>= 1) s += __shfl_xor(s, o);
            if (lane == 0) __builtin_amdgcn_raw_buffer_store_b32(__float_as_uint(1.f / (1.f + __expf(-s))), rs, WS_GATE + (256 * mt + 8 * hd + wid) * 4, 0, 16);
        }
        base = (((u64)(unsigned)__builtin_amdgcn_readfirstlane(ep[1]) << 32) | (unsigned)__builtin_amdgcn_readfirstlane(ep[0])) + 8;
        if (wid == 0) STAMP(1, 0);
#define EX(S_, i) S_[i] = __builtin_amdgcn_exp2f(S_[i])
#define AT_STEP(SC, SN, K0, K1, VC0, VP1, RD0, RD1, RD2, RD3) do { \
        SN = MFMA32(K0, q0, negm); SBAR(); RD0; EX(SC, 0); EX(SC, 1); EX(SC, 2); EX(SC, 3); SBAR(); \
        o = MFMA32(VP1, BC(p1), o); PIN(o); SBAR(); RD1; EX(SC, 4); EX(SC, 5); EX(SC, 6); EX(SC, 7); p0[0] = cvtpk(SC[0], SC[1]); p0[1] = cvtpk(SC[2], SC[3]); SBAR(); \
        l = MFMA16(sel, BC(p1), l); PIN(l); SBAR(); p0[2] = cvtpk(SC[4], SC[5]); p0[3] = cvtpk(SC[6], SC[7]); EX(SC, 8); SBAR(); \
        SN = MFMA32(K1, q1, SN); SBAR(); RD2; EX(SC, 9); EX(SC, 10); EX(SC, 11); EX(SC, 12); SBAR(); \
        o = MFMA32(VC0, BC(p0), o); PIN(o); SBAR(); RD3; EX(SC, 13); EX(SC, 14); EX(SC, 15); p1[0] = cvtpk(SC[8], SC[9]); SBAR(); \
        l = MFMA16(sel, BC(p0), l); PIN(l); SBAR(); p1[1] = cvtpk(SC[10], SC[11]); p1[2] = cvtpk(SC[12], SC[13]); p1[3] = cvtpk(SC[14], SC[15]); SBAR(); } while (0)
        int sl = AT_STAGE;
        for (int T = 1; T <= 32; ++T) {
            if (T <= 4) {
                asm volatile("s_waitcnt vmcnt(0)" : "+v"(fv) :: "memory");
                if (wid == 1 && (T == 2 || (T == 4 && !__all(fv == base + 2)))) wait_flags(f1, 8, base + (T >> 1), 2 - (T >> 1), lane);
            }
            LGKM_BAR(); SBAR();
            TICK(1, T);
            if (T <= 3) {
                if (T <= 2 && wid == 0) set_flag(f1 + mt * 64, base + T, lane);
                if (T == 1) { PUBLISH(2); PUBLISH(3); }
                if (T == 3 && wid == 1) asm volatile("global_load_dwordx2 %0, %1, off sc1" : "+v"(fv) : "v"(f1 + (lane & 7) * 64) : "memory");
            }
            if (T == 4 * wid - 8 && T > 5)
                { asm volatile("global_load_dword %0, %1, %2" : "+v"(sink) : "v"(TW(lane)), "s"(wsb + ((mt + wid) & 7) * 512) : "memory");
                  if (wid == 7) asm volatile("global_load_dword %0, %1, %2" : "+v"(sink) : "v"(lane * 128), "s"(pf2) : "memory"); }
            SBAR();
            if (!ABL_ATT) {
            AT_STEP(sA, sB, kB0, kB1, vA0, vB1, kA0 = ldsr128(fb + sl), kA1 = ldsr128(fb + sl + 1024), vB0 = ldsr128(fb + sl + 4096), vB1 = ldsr128(fb + sl + 5120));
            AT_STEP(sB, sA, kA0, kA1, vB0, vA1, kB0 = ldsr128(fb + sl + 2048), kB1 = ldsr128(fb + sl + 3072), vA0 = ldsr128(fb + sl + 6144), vA1 = ldsr128(fb + sl + 7168)); }
            sl = (sl + AT_STAGE) & (4 * AT_STAGE - 1);
        }
        o = MFMA32(vB1, BC(p1), o); l = MFMA16(sel, BC(p1), l);
        if (wid == 0) STAMP(1, 1);
        bool bad = !(l[0] >= 7.8886e-31f && l[0] <= 3.0e38f);
#pragma unroll
        for (int r = 0; r < 16; ++r) bad |= !(fabsf(o[r]) <= 3.0e38f);
        const int r = lane & 31, qrow = qt * 32 + ((r & 19) | ((r & 4) << 1) | ((r & 8) >> 1));
        const unsigned ooff = WS_O + ((qrow >> 4) * 32 + hd) * 1024 + ((lane >> 5) * 32 + (qrow & 15)) * 16;
        if (!ABL_ATT && !ABL_FDMA && __builtin_expect(__any(bad), 0)) attn_slow(rs, ws + WS_KV + hd * 262144, q0, q1, lane, ooff);
        else attn_store(rs, ooff, o, l[0]);
        WAIT_BAR(0);
        asm volatile("" :: "v"(sink));
        if (wid == 0) STAMP(1, 2);
    }
    {
        const int rt = 2 * (blockIdx.x & 7) + (blockIdx.x >> 7), nb = ((blockIdx.x >> 3) & 15) * 64;
        const int kq = fr >> 2, swz = ((kq >> 1) & 1) | ((fq & 1) << 1);
        const int aW = (8 * fq + kq) * 128 + ((((lane & 3) >> 1) ^ (swz << 1)) * 16) + (lane & 1) * 8;
#define P3_RD(W_, ks_) do { const int kt_ = (ks_) >> 1; \
            const int wb_ = (kt_ < 4 ? FW_TOP - 2 * FW_STAGE + kt_ * 8192 : kt_ == 4 ? FB_BASE : kt_ < 9 ? FW_TOP + (kt_ - 5) * 8192 : kt_ == 9 ? FB_BASE + 8192 \
                              : kt_ < 14 ? FW_TOP - FW_STAGE + (kt_ - 10) * 8192 : FB_BASE + (kt_ - 12) * 8192) + ((ks_) & 1) * 4096; \
            _Pragma("unroll") for (int n_ = 0; n_ < 4; ++n_) W_[n_] = ldstr(smem + wb_ + (aW ^ (n_ * 32)), 512); } while (0)
        bf16x8 wf[2][4];
        P3_RD(wf[0], 0);
        if (wid == 0) {
            set_flag(ws + WS_F2 + (mt * 32 + hd) * 64, base + 5, lane);
            wait_flags(ws + WS_F2 + (rt >> 1) * 2048, 32, base + 5, 0, lane);
            __builtin_amdgcn_fence(__ATOMIC_ACQUIRE, "wavefront");
            if (blockIdx.x == 0) set_flag(ws, base, lane);
            STAMP(1, 3);
        }
        LGKM_BAR();
        const int R0 = rt * 128 + 16 * wid;
        const int t = R0 + fr, col = nb + 16 * (fq & 1) + 8 * (fq >> 1);
        u32x4 of[24], y0[2], y1[2]; float g;
#pragma unroll
        for (int ks = 0; ks < 24; ++ks) of[ks] = ld_dev(rs, WS_O + ((R0 >> 4) * 32 + ks) * 1024 + lane * 16);
        SBAR();
        f32x4 acc[4];
#pragma unroll
        for (int n = 0; n < 4; ++n) acc[n] = f32x4{};
#pragma unroll
        for (int ks = 0; ks < 32; ++ks) {
            if (ks + 1 < 32) P3_RD(wf[(ks + 1) & 1], ks + 1);
            SBAR();
#pragma unroll
            for (int n = 0; n < 4; ++n) acc[n] = MFMA16(wf[ks & 1][n], BC(of[ks % 24]), acc[n]);
            if (ks < 8) of[ks] = ld_dev(rs, WS_O + ((R0 >> 4) * 32 + 24 + ks) * 1024 + lane * 16);
            if (ks == 12) {
                g = __uint_as_float(__builtin_amdgcn_raw_buffer_load_b32(rs, WS_GATE + t * 4, 0, 16));
#pragma unroll
                for (int i = 0; i < 2; ++i) { y0[i] = ld_dev(rs, WS_Y + (t * DM + col + 32 * i) * 2); y1[i] = ld_dev(rs, WS_Y + 4 * MB + (t * DM + col + 32 * i) * 2); }
            }
            SBAR();
        }
        const float g1 = 1.f - g;
#pragma unroll
        for (int i = 0; i < 2; ++i) {
            float v[8];
#pragma unroll
            for (int j = 0; j < 4; ++j) {
                auto r = __builtin_amdgcn_permlane16_swap(__float_as_uint(acc[2 * i][j]), __float_as_uint(acc[2 * i + 1][j]), false, false);
                v[j] = __uint_as_float(r[0]); v[4 + j] = __uint_as_float(r[1]);
            }
            u32x4 w;
#pragma unroll
            for (int j = 0; j < 4; ++j)
                w[j] = cvtpk(g * (bflo(y0[i][j]) - bflo(y1[i][j])) + g1 * v[2 * j], g * (bfhi(y0[i][j]) - bfhi(y1[i][j])) + g1 * v[2 * j + 1]);
            *(u32x4*)(out + (size_t)t * DM + col + 32 * i) = w;
        }
        if (wid == 0) STAMP(1, 4);
        if (wid == 0 && DIAG_A >= 0) { const unsigned long long e0 = __builtin_amdgcn_s_memrealtime(); while (__builtin_amdgcn_s_memrealtime() - e0 < (dg1 - dg0)) __builtin_amdgcn_s_sleep(1); }
    }
}

DI void side_waves(LAS char* smem, int lw, int lane, int mt, int hd, const u16* __restrict__ x, const u16* __restrict__ wsr, const u16* __restrict__ wsi,
                   const u16* __restrict__ wdr, const u16* __restrict__ wdi, const u16* __restrict__ wqkv, const u16* __restrict__ wo,
                   const u16* __restrict__ gw, char* ws, rsrc_t rs) {
    const unsigned lds0 = (unsigned)(size_t)smem + lw * 1024;
    const unsigned rowl = 8 * lw + (lane >> 3), chk = ((lane & 7) ^ ((4 * lw + (lane >> 4)) & 7)) * 16;
    const int c = hd >> 4;
    const char* wb = (const char*)(c ? wsi : wsr) + (size_t)mt * 256 * 4096;
    unsigned vo[10];
    {
        const char* xb = (const char*)x + (size_t)mt * 256 * 2048;
        const char* wdb = (const char*)(c ? wdi : wdr) + (size_t)(hd & 15) * 64 * 2048;
        const char* wqb = (const char*)wqkv + hd * 64 + lw * 16 * 6144;
#pragma unroll
        for (int i = 0; i < 8; ++i) vo[i] = (rowl + 32 * i) * 2048 + chk;
        const unsigned voffW = (lane >> 2) * 6144 + (((lane & 3) ^ ((lane >> 5) << 1)) * 16);
#define K1_DMA(t, slot) do { const char* xa_ = xb + (size_t)(t) * 128; const char* wd_ = wdb + (size_t)(t) * 128; const char* wq_ = wqb + (size_t)(t) * 64 * 6144; \
        _Pragma("unroll") for (int u_ = 0; u_ < 3; ++u_) dma(voffW, wq_ + u_ * 2048, lds0 + (slot) + K1_WQ + u_ * 4096); \
        _Pragma("unroll") for (int i_ = 0; i_ < 2; ++i_) dma(vo[i_], wd_, lds0 + (slot) + K1_WD + i_ * 4096); \
        _Pragma("unroll") for (int i_ = 0; i_ < 8; ++i_) dma(vo[i_], xa_, lds0 + (slot) + K1_A + i_ * 4096); } while (0)
        K1_DMA(0, 0); K1_DMA(1, K1_STAGE); K1_DMA(2, 2 * K1_STAGE);
        WAIT_BAR(26);
        int sl = 0;
        for (int t = 0; t < 13; ++t) {
            WAIT_BAR(13);
            K1_DMA(t + 3, sl);
            sl = (sl == 2 * K1_STAGE) ? 0 : sl + K1_STAGE;
        }
        WAIT_BAR(13);
        WAIT_BAR(0);
#pragma unroll
        for (int i = 0; i < 8; ++i) vo[i] = (rowl + 32 * i) * 4096 + chk;
        vo[8] = lane * 16 + lw * 1024; vo[9] = vo[8] + 4096;
#pragma unroll
        for (int i = 0; i < 7; ++i) dma(vo[i], wb + (size_t)((4 * mt) & 31) * 128, lds0 + FW_TOP + i * 4096);
#pragma unroll
        for (int i = 0; i < 8; ++i) dma(vo[i], wb + (size_t)((4 * mt + 1) & 31) * 128, lds0 + FW_TOP - FW_STAGE + i * 4096);
        LGKM_BAR();
        dma(vo[7], wb + (size_t)((4 * mt) & 31) * 128, lds0 + FW_TOP + 7 * 4096);
        WAIT_BAR(0);
    }
    {
        const char* kvb = ws + WS_KV + hd * 262144;
        const unsigned vl = lane * 16 + lw * 1024;
        const char *bW = wb, *bA = ws + WS_AB + hd * 262144; int kmask = 31, kstep = 128, kstepA = 8192, koff = 4 * mt;
#define FN_DMA_P(p, s, fslot, aslot) do { \
        if ((p) < 8) dma(vo[p], bW + (size_t)((koff + (s)) & kmask) * kstep, lds0 + (fslot) + (p) * 4096); \
        else if ((p) < 10) dma_dev(vo[p], bA + (size_t)((koff + (s)) & kmask) * kstepA, lds0 + FB_BASE + (aslot) + ((p) - 8) * 4096); \
        else if ((p) == 10) dma_dev(vl, kvb + ((4 * mt + (s)) & 31) * 8192, lds0 + (aslot)); \
        else dma_dev(vl, kvb + ((4 * mt + (s) - ((((s) & 3) == 0 && lw < 2) ? 4 : 0)) & 31) * 8192 + 4096, lds0 + (aslot) + 4096); } while (0)
        const int fr = lane & 15, fq = lane >> 4;
        const int cpart = ((fq ^ (fr >> 1)) & 7) * 16;
        const int aW = (64 * lw + fr) * 128 + cpart, aA = FB_BASE + fr * 128 + cpart;
        f32x4 acc[4][4];
#pragma unroll
        for (int m = 0; m < 4; ++m)
#pragma unroll
            for (int n = 0; n < 4; ++n) acc[m][n] = f32x4{};
        FnFrag X, Yf;
#define FN_RD(F, i, slot, bslot, ks) do { \
        if ((i) == 0) F.w[0] = ldsr128(smem + (slot) + (aW ^ ((ks) * 64))); \
        else if ((i) < 5) F.a[(i) - 1] = ldsr128(smem + (bslot) + (aA ^ ((ks) * 64)) + ((i) - 1) * 2048); \
        else F.w[(i) - 4] = ldsr128(smem + (slot) + (aW ^ ((ks) * 64)) + ((i) - 4) * 2048); } while (0)
#define FN_MF(F, j) do { if (!ABL_FMFMA) acc[(j) >> 2][(j) & 3] = MFMA16(F.a[(j) & 3], F.w[(j) >> 2], acc[(j) >> 2][(j) & 3]); else asm volatile("" :: "v"(F.a[(j) & 3]), "v"(F.w[(j) >> 2])); } while (0)
#define FN_G(g, C, R, rslot, rb, ks, P0, s, fslot, aslot) do { FN_MF(C, 2 * (g)); FN_RD(R, g, rslot, rb, ks); FN_MF(C, 2 * (g) + 1); \
        if (!ABL_FDMA && ((g) & 3) != 0 && (P0) + (g) - 1 - ((g) >> 2) < PMAX) FN_DMA_P((P0) + (g) - 1 - ((g) >> 2), s, fslot, aslot); SBAR(); FN_PAD(g); } while (0)
#define FN_PHASE(C, R, rslot, rb, ks, P0, s, fslot, aslot) do { FN_G(0, C, R, rslot, rb, ks, P0, s, fslot, aslot); FN_G(1, C, R, rslot, rb, ks, P0, s, fslot, aslot); \
        FN_G(2, C, R, rslot, rb, ks, P0, s, fslot, aslot); FN_G(3, C, R, rslot, rb, ks, P0, s, fslot, aslot); FN_G(4, C, R, rslot, rb, ks, P0, s, fslot, aslot); \
        FN_G(5, C, R, rslot, rb, ks, P0, s, fslot, aslot); FN_G(6, C, R, rslot, rb, ks, P0, s, fslot, aslot); FN_G(7, C, R, rslot, rb, ks, P0, s, fslot, aslot); } while (0)
#define PMAX 12
        __builtin_amdgcn_s_setprio(FN_PRIO);
#pragma unroll
        for (int i = 0; i < 8; ++i) FN_RD(X, i, FW_TOP, 0, 0);
        LGKM_BAR();
        int f0 = FW_TOP, f1 = FW_TOP - FW_STAGE, f2 = FW_TOP - 2 * FW_STAGE;
        FN_DMA_P(0, 2, f2, 0); FN_DMA_P(1, 2, f2, 0); FN_DMA_P(2, 2, f2, 0); FN_DMA_P(3, 2, f2, 0); FN_DMA_P(4, 2, f2, 0); FN_DMA_P(5, 2, f2, 0);
        SBAR();
#define FN_ITER(t, WAITN) do { \
            FN_PHASE(X, Yf, f0, ((t) & 3) * 8192, 1, 6, (t) + 2, f2, (((t) + 2) & 3) * 8192);       \
            WAIT_BAR(WAITN); SBAR();                                                                \
            FN_SWITCH(t); \
            FN_PHASE(Yf, X, f1, (((t) + 1) & 3) * 8192, 0, 0, (t) + 3, f0, 0);                      \
            const int ft = f0; f0 = f1; f1 = f2; f2 = ft; } while (0)
#define FN_SWITCH(t)
#undef PMAX
#define PMAX 8
        FN_ITER(0, 8); FN_ITER(1, 8);
#undef PMAX
#define PMAX 12
#undef FN_SWITCH
#define FN_SWITCH(t) do { if ((t) == 29) { bW = bA = (const char*)wo + ((blockIdx.x >> 3) & 15) * 128; kmask = 3; kstep = kstepA = 320 * 2048; koff = 0; \
                const unsigned cw = ((lane & 7) ^ ((((lane >> 4) & 1) | ((lw & 1) << 1)) << 1)) * 16; \
                _Pragma("unroll") for (int i = 0; i < 10; ++i) vo[i] = (rowl + 32 * i) * 2048 + cw; } SBAR(); } while (0)
        for (int t = 2; t < 31; ++t) FN_ITER(t, 12);
#undef PMAX
#define PMAX 10
        FN_PHASE(X, Yf, f0, 3 * 8192, 1, 6, 33, f2, 8192);
        WAIT_BAR(10); SBAR();
#pragma unroll
        for (int i = 0; i < 8; ++i) dma(vo[i], bW + (size_t)640 * 2048, lds0 + FW_TOP - FW_STAGE + i * 4096);
#pragma unroll
        for (int i = 0; i < 4; ++i) dma(vo[i], bW + (size_t)896 * 2048, lds0 + FB_BASE + 2 * 8192 + i * 4096);
#pragma unroll
        for (int j = 0; j < 16; ++j) FN_MF(Yf, j);
        const unsigned yb = WS_Y + c * 4 * MB + ((mt * 256 + 64 * lw + fr) * DM + (hd & 15) * 64 + 16 * (fq & 1) + 8 * (fq >> 1)) * 2;
#pragma unroll
        for (int m = 0; m < 4; ++m)
#pragma unroll
            for (int i = 0; i < 2; ++i) st_wt(rs, yb + (m * 16 * DM + 32 * i) * 2, swap16(pack4(acc[m][2 * i]), pack4(acc[m][2 * i + 1])));
        WAIT_BAR(0);
    }
}

__global__ __launch_bounds__(768, 3) void fused_kernel(const u16* __restrict__ x, const u16* __restrict__ wsr, const u16* __restrict__ wsi,
                                                        const u16* __restrict__ wdr, const u16* __restrict__ wdi, const u16* __restrict__ wqkv,
                                                        const u16* __restrict__ wo, const u16* __restrict__ gw, u16* __restrict__ out, char* ws) {
    __shared__ __attribute__((aligned(1024))) char smem_[163840];
    LAS char* smem = (LAS char*)smem_;
    const int tid = threadIdx.x, lane = tid & 63, wid = __builtin_amdgcn_readfirstlane(tid >> 6);
    const int xcd = blockIdx.x & 7, idx = blockIdx.x >> 3;
    const int hd = xcd * 4 + (idx >> 3), mt = idx & 7;
    const rsrc_t rs = __builtin_amdgcn_make_buffer_rsrc(ws, 0, WS_END, 0x00020000);
    unsigned long long dg0 = 0, dg1 = 0;
    if (wid == 0) STAMP(0, 0);
    if (wid < 8) {
        const char* wdb = (const char*)((hd >> 4) ? wdi : wdr) + (size_t)(hd & 15) * 64 * 2048;
        const char* wsb = (const char*)((hd >> 4) ? wsi : wsr) + (size_t)mt * 256 * 4096;
        const char* pf2 = (const char*)wo + (size_t)blockIdx.x * 8192;
        main_waves(dg0, smem, wid, lane, mt, hd, ws, rs, out, x, gw, wdb, (const char*)wqkv, wsb, pf2);
    }
    else side_waves(smem, wid - 8, lane, mt, hd, x, wsr, wsi, wdr, wdi, wqkv, wo, gw, ws, rs);
}

extern "C" void kernel_launch(void* const* d_in, const int* in_sizes, int n_in, void* d_out, int out_size, void* d_ws, size_t ws_size, hipStream_t stream) {
    const u16* x = (const u16*)d_in[0]; const u16* wsr = (const u16*)d_in[1]; const u16* wsi = (const u16*)d_in[2];
    const u16* wdr = (const u16*)d_in[3]; const u16* wdi = (const u16*)d_in[4]; const u16* wqkv = (const u16*)d_in[6];
    const u16* wo = (const u16*)d_in[7]; const u16* gw = (const u16*)d_in[8];
    fused_kernel<<<256, 768, 0, stream>>>(x, wsr, wsi, wdr, wdi, wqkv, wo, gw, (u16*)d_out, (char*)d_ws);
}
```

```cpp
#include <hip/hip_runtime.h>
#include <hip/hip_bf16.h>

typedef unsigned short u16;
typedef short bf16x8 __attribute__((ext_vector_type(8)));
typedef short s16x4 __attribute__((ext_vector_type(4)));
typedef float f32x2 __attribute__((ext_vector_type(2)));
typedef float f32x4 __attribute__((ext_vector_type(4)));
typedef float f32x16 __attribute__((ext_vector_type(16)));
typedef unsigned u32x4 __attribute__((ext_vector_type(4)));
typedef unsigned u32x2 __attribute__((ext_vector_type(2)));
typedef __bf16 bf16x2_t __attribute__((ext_vector_type(2)));
#define LAS __attribute__((address_space(3)))
#define GAS __attribute__((address_space(1)))
#define DI __device__ __forceinline__
#define SBAR() __builtin_amdgcn_sched_barrier(0)
#define PIN(x) asm volatile("" : "+v"(x))
#define MFMA16(a, b, c) __builtin_amdgcn_mfma_f32_16x16x32_bf16(a, b, c, 0, 0, 0)
#define MFMA32(a, b, c) __builtin_amdgcn_mfma_f32_32x32x16_bf16(a, b, c, 0, 0, 0)
#define WAIT_BAR(N) asm volatile("s_waitcnt vmcnt(" #N ") lgkmcnt(0)\n\ts_barrier" ::: "memory")
#define LGKM_BAR() asm volatile("s_waitcnt lgkmcnt(0)\n\ts_barrier" ::: "memory")
#define RLX_AGENT __ATOMIC_RELAXED, __HIP_MEMORY_SCOPE_AGENT
#define BC(x) __builtin_bit_cast(bf16x8, x)

constexpr int SEQ = 2048, DM = 1024;
constexpr unsigned MB = 1u << 20;
constexpr unsigned WS_F1 = 4096, WS_F2 = 4096 + 16384, WS_GATE = 65536, WS_KV = 1 * MB, WS_AB = 9 * MB, WS_O = 17 * MB, WS_Y = 21 * MB, WS_END = 29 * MB;
typedef unsigned long long u64;

#define STAMP(kid, i)
#define TICK(k, i)

DI unsigned cvtpk(float lo, float hi) { f32x2 v = {lo, hi}; bf16x2_t b = __builtin_convertvector(v, bf16x2_t); return __builtin_bit_cast(unsigned, b); }
DI void dma(unsigned voff, const char* sbase, unsigned l) {
    asm volatile("s_mov_b32 m0, %2\n\ts_nop 0\n\tglobal_load_lds_dwordx4 %0, %1" :: "v"(voff), "s"(sbase), "s"(l) : "memory"); }
DI void dma_dev(unsigned voff, const char* sbase, unsigned l) {
    asm volatile("s_mov_b32 m0, %2\n\ts_nop 0\n\tglobal_load_lds_dwordx4 %0, %1 sc1" :: "v"(voff), "s"(sbase), "s"(l) : "memory"); }
DI void ldsw128(LAS char* p, u32x4 v) { *(LAS u32x4*)p = v; }
DI bf16x8 ldsr128(const LAS char* p) { return *(const LAS bf16x8*)p; }
DI bf16x8 ldstr(const LAS char* p, int off2) {
    s16x4 a = __builtin_amdgcn_ds_read_tr16_b64_v4i16((LAS s16x4*)p);
    s16x4 b = __builtin_amdgcn_ds_read_tr16_b64_v4i16((LAS s16x4*)(p + off2));
    return (bf16x8){a[0], a[1], a[2], a[3], b[0], b[1], b[2], b[3]};
}
DI int lane_now() { int l; asm volatile("v_mbcnt_lo_u32_b32 %0, -1, 0\n\tv_mbcnt_hi_u32_b32 %0, -1, %0" : "=v"(l)); return l; }
DI float bflo(unsigned u) { return __uint_as_float(u << 16); }
DI float bfhi(unsigned u) { return __uint_as_float(u & 0xffff0000u); }
DI u32x4 swap16(u32x2 a, u32x2 b) {
    auto r0 = __builtin_amdgcn_permlane16_swap(a[0], b[0], false, false);
    auto r1 = __builtin_amdgcn_permlane16_swap(a[1], b[1], false, false);
    return (u32x4){r0[0], r1[0], r0[1], r1[1]};
}
DI u32x2 pack4(f32x4 v) { return (u32x2){cvtpk(v[0], v[1]), cvtpk(v[2], v[3])}; }
typedef __amdgpu_buffer_rsrc_t rsrc_t;
DI void st_wt(rsrc_t r, unsigned off, u32x4 v) { __builtin_amdgcn_raw_buffer_store_b128(v, r, off, 0, 16); }
DI u32x4 ld_dev(rsrc_t r, unsigned off) { return __builtin_amdgcn_raw_buffer_load_b128(r, off, 0, 16); }
DI void wait_flags(const char* first, int n, u64 lo, u64 span, int lane) {
    const GAS u64* p = (const GAS u64*)(first + (lane < n ? lane : 0) * 64);
    for (unsigned spins = 0; spins < (1u << 16); ++spins) {
        const u64 v = __hip_atomic_load(p, RLX_AGENT);
        if (__all(v - lo <= span)) break;
        __builtin_amdgcn_s_sleep(1);
    }
}
DI void set_flag(char* p, u64 v, int lane) { if (lane == 0) __hip_atomic_store((GAS u64*)p, v, RLX_AGENT); }

#define ABL_FDMA 0
#define ABL_FMFMA 0
#define ABL_ATT 0
#define PADN 0
#define FN_PAD(g) do { _Pragma("unroll") for (int z_ = 0; z_ < PADN; ++z_) asm volatile("s_nop 3"); SBAR(); } while (0)
#define FN_PRIO 1
#define PF_MODE 0
#define TLB_WARM 0
#define TLB_SUB 16384
#define PF_N0 3
#define PF_LEAD 8
#define PF_SLEEP 16
constexpr int K1_STAGE = 53248, K1_A = 0, K1_WD = 32768, K1_WQ = 40960;
constexpr int AT_STAGE = 8192, FB_BASE = 32768, FW_STAGE = 32768, FW_TOP = 131072;
struct K1Frag { bf16x8 x[4], wd[2], wqk[2], wv; };
struct FnFrag { bf16x8 w[4], a[4]; };

DI void attn_store(rsrc_t rs, unsigned off, const f32x16& o, float l) {
    const float rl = 1.0f / l; u32x4 w0, w1;
#pragma unroll
    for (int i = 0; i < 4; ++i) { w0[i] = cvtpk(o[2 * i] * rl, o[2 * i + 1] * rl); w1[i] = cvtpk(o[8 + 2 * i] * rl, o[9 + 2 * i] * rl); }
    st_wt(rs, off, w0); st_wt(rs, off + 256, w1);
}
DI bf16x8 attn_sel(int lane) {
    const short v = (((lane >> 2) & 1) == ((lane >> 4) & 1)) ? (short)0x3f80 : (short)0;
    return (bf16x8){v, v, v, v, v, v, v, v};
}
DI float max16(const f32x16& s) {
    float a = fmaxf(fmaxf(s[0], s[1]), s[2]), b = fmaxf(fmaxf(s[3], s[4]), s[5]);
#pragma unroll
    for (int r = 6; r < 16; r += 4) { a = fmaxf(fmaxf(a, s[r]), s[r + 1]); if (r + 3 < 16) b = fmaxf(fmaxf(b, s[r + 2]), s[r + 3]); }
    return fmaxf(a, b);
}
DI void attn_slow(rsrc_t rs, const char* kv, bf16x8 q0, bf16x8 q1, int lane, unsigned ooff) {
    __builtin_amdgcn_fence(__ATOMIC_ACQUIRE, "agent");
    const bf16x8 sel = attn_sel(lane);
    kv += lane * 16;
    float mx = -INFINITY;
    const f32x16 z = f32x16{};
#pragma unroll 1
    for (int j = 0; j < 64; ++j) {
        const char* kp = kv + (j >> 1) * 8192 + (j & 1) * 2048;
        f32x16 s = MFMA32(*(const bf16x8*)kp, q0, z); s = MFMA32(*(const bf16x8*)(kp + 1024), q1, s);
        mx = fmaxf(mx, max16(s));
    }
    mx = fmaxf(mx, __shfl_xor(mx, 32));
    f32x16 negm, o = f32x16{}; f32x4 l = f32x4{};
#pragma unroll
    for (int r = 0; r < 16; ++r) negm[r] = -mx;
#pragma unroll 1
    for (int j = 0; j < 64; ++j) {
        const char* kp = kv + (j >> 1) * 8192 + (j & 1) * 2048;
        const char* vp = kv + ((j & 1) ? (((j & 7) == 7 ? (j >> 3) * 4 : (j + 1) >> 1) * 8192 + 4096) : ((j >> 1) * 8192 + 6144));
        const bf16x8 v0 = *(const bf16x8*)vp, v1 = *(const bf16x8*)(vp + 1024);
        f32x16 s = MFMA32(*(const bf16x8*)kp, q0, negm); s = MFMA32(*(const bf16x8*)(kp + 1024), q1, s);
        u32x4 p0, p1;
#pragma unroll
        for (int r = 0; r < 16; ++r) s[r] = __builtin_amdgcn_exp2f(s[r]);
#pragma unroll
        for (int r = 0; r < 4; ++r) { p0[r] = cvtpk(s[2 * r], s[2 * r + 1]); p1[r] = cvtpk(s[8 + 2 * r], s[9 + 2 * r]); }
        o = MFMA32(v0, BC(p0), o); l = MFMA16(sel, BC(p0), l);
        o = MFMA32(v1, BC(p1), o); l = MFMA16(sel, BC(p1), l);
    }
    attn_store(rs, ooff, o, l[0]);
}

DI void touch8k(const char* p, unsigned voff, unsigned dump) {
    asm volatile("s_mov_b32 m0, %2\n\ts_nop 0\n\tglobal_load_lds_dword %0, %1" :: "v"(voff), "s"(p), "s"(dump) : "memory"); }
DI void touch_pair(int p, int l, int mt, int hd, const char* x, const char* wdb, const char* wqkv, unsigned dump) {
    const int n = l >= 56 ? l - 8 : l, i = (mt * 2 + (hd & 1)) * 24 + (n - 32);
    const char* a = n < 16 ? x + (size_t)(mt * 256 + hd * 8 + (n >> 1)) * 2048 + p * 256 + (n & 1) * 128
                  : n < 32 ? wdb + (size_t)(mt * 8 + ((n - 16) >> 1)) * 2048 + p * 256 + (n & 1) * 128
                           : wqkv + (size_t)(p * 128 + i / 3) * 6144 + (i % 3) * 2048 + (hd >> 1) * 128;
    asm volatile("s_mov_b32 m0, %1\n\ts_nop 0\n\tglobal_load_lds_dword %0, off" :: "v"(a), "s"(dump) : "memory");
}
DI void main_waves(LAS char* smem, int wid, int lane, int mt, int hd, char* ws, rsrc_t rs, u16* __restrict__ out,
                   const u16* __restrict__ x, const u16* __restrict__ gw, const char* wdb, const char* wqkv, const char* wsb, const char* pf2) {
    const unsigned dump = (unsigned)(size_t)smem + 3 * K1_STAGE + wid * 256;
    if (wid >= 1 && wid <= 3) { for (int z = 0; z < wid; ++z) __builtin_amdgcn_s_sleep(PF_SLEEP); touch_pair(wid, lane, mt, hd, (const char*)x, wdb, wqkv, dump); }
    const int t_act = wid < 4 ? 7 + 2 * wid : 2 * wid - PF_LEAD;
#define TW(l_) (unsigned)(((hd & 15) * 16 + ((l_) >> 2)) * 4096 + ((l_) & 3) * 128)
    int fr = lane & 15, fq = lane >> 4;
    u32x2 ep; u64 base; u32x4 gx0, gx1, gw0, gw1;
    {
        const int wm = wid >> 1, wn = wid & 1;
        const int cpart = ((fq ^ (fr >> 1)) & 7) * 16;
        const int aA = K1_A + (64 * wm + fr) * 128 + cpart, aD = K1_WD + (32 * wn + fr) * 128 + cpart;
        const int aT0 = (8 * fq + (fr >> 2)) * 64 + ((((lane & 3) >> 1) ^ ((fq & 1) << 1)) * 16) + (lane & 1) * 8;
        const int aQ = K1_WQ + wn * 4096 + aT0, aV = K1_WQ + 8192 + (aT0 ^ (wn * 32));
        f32x4 accQ[2][4], accV[4], accD[2][4];
#pragma unroll
        for (int m = 0; m < 4; ++m) { accQ[0][m] = f32x4{}; accQ[1][m] = f32x4{}; accV[m] = f32x4{}; accD[0][m] = f32x4{}; accD[1][m] = f32x4{}; }
        K1Frag X, Y;
#define K1_RD(F, i, slot, ks) do { const LAS char* sb_ = smem + (slot); \
        if ((i) == 0) F.wqk[0] = ldstr(sb_ + aQ + (ks) * 2048, 256); \
        else if ((i) == 1) { F.x[0] = ldsr128(sb_ + (aA ^ ((ks) * 64))); } \
        else if ((i) == 2) F.wqk[1] = ldstr(sb_ + (aQ ^ 32) + (ks) * 2048, 256); \
        else if ((i) == 3) F.wv = ldstr(sb_ + aV + (ks) * 2048, 256); \
        else if ((i) == 4) { F.wd[0] = ldsr128(sb_ + (aD ^ ((ks) * 64))); F.wd[1] = ldsr128(sb_ + (aD ^ ((ks) * 64)) + 2048); } \
        else if ((i) < 8) F.x[(i) - 4] = ldsr128(sb_ + (aA ^ ((ks) * 64)) + ((i) - 4) * 2048); } while (0)
#define K1_MF(F, j) do { constexpr int m_ = (j) / 5, r_ = (j) % 5; \
        if (r_ == 0) accQ[0][m_] = MFMA16(F.wqk[0], F.x[m_], accQ[0][m_]); \
        else if (r_ == 1) accQ[1][m_] = MFMA16(F.wqk[1], F.x[m_], accQ[1][m_]); \
        else if (r_ == 2) accV[m_] = MFMA16(F.x[m_], F.wv, accV[m_]); \
        else if (r_ == 3) accD[0][m_] = MFMA16(F.x[m_], F.wd[0], accD[0][m_]); \
        else accD[1][m_] = MFMA16(F.x[m_], F.wd[1], accD[1][m_]); } while (0)
#define K1_G(g, C, R, rslot, ks) do { K1_MF(C, 2 * (g)); K1_RD(R, g, rslot, ks); K1_MF(C, 2 * (g) + 1); SBAR(); } while (0)
#define K1_PHASE(C, R, rslot, ks) do { K1_G(0, C, R, rslot, ks); K1_G(1, C, R, rslot, ks); K1_G(2, C, R, rslot, ks); K1_G(3, C, R, rslot, ks); K1_G(4, C, R, rslot, ks); \
        K1_G(5, C, R, rslot, ks); K1_G(6, C, R, rslot, ks); K1_G(7, C, R, rslot, ks); K1_G(8, C, R, rslot, ks); K1_G(9, C, R, rslot, ks); } while (0)
        LGKM_BAR();
        if (wid == 0) STAMP(0, 1);
#pragma unroll
        for (int i = 0; i < 8; ++i) K1_RD(X, i, 0, 0);
        int sl = 0, sn = K1_STAGE;
        SBAR();
        for (int t = 0; t < 15; ++t) {
            K1_PHASE(X, Y, sl, 1);
            LGKM_BAR(); SBAR();
            TICK(0, t);
            if (t == t_act) {
                const int l_ = lane_now();
                if (wid < 4) touch8k(wsb + ((mt + wid) & 7) * 512, TW(l_), dump);
                else touch_pair(wid, l_, mt, hd, (const char*)x, wdb, wqkv, dump);
            }
            SBAR();
            K1_PHASE(Y, X, sn, 0);
            sl = sn; sn = (sn == 2 * K1_STAGE) ? 0 : sn + K1_STAGE;
        }
        K1_PHASE(X, Y, 0, 1);
        WAIT_BAR(0); SBAR();
        lane = lane_now();
        asm volatile("global_load_dwordx2 %4, %7, off sc1\n\tglobal_load_dwordx4 %0, %5, off\n\tglobal_load_dwordx4 %1, %5, off offset:16\n\t"
                     "global_load_dwordx4 %2, %6, off\n\tglobal_load_dwordx4 %3, %6, off offset:16"
                     : "=&v"(gx0), "=&v"(gx1), "=&v"(gw0), "=&v"(gw1), "=&v"(ep)
                     : "v"((const char*)x + (size_t)(256 * mt + 8 * hd + wid) * 2048 + lane * 32), "v"((const char*)gw + lane * 32), "v"(ws) : "memory");
        SBAR();
        K1_MF(Y, 0); K1_MF(Y, 1); K1_MF(Y, 2); K1_MF(Y, 3); K1_MF(Y, 4); K1_MF(Y, 5); K1_MF(Y, 6); K1_MF(Y, 7); K1_MF(Y, 8); K1_MF(Y, 9);
        K1_MF(Y, 10); K1_MF(Y, 11); K1_MF(Y, 12); K1_MF(Y, 13); K1_MF(Y, 14); K1_MF(Y, 15); K1_MF(Y, 16); K1_MF(Y, 17); K1_MF(Y, 18); K1_MF(Y, 19);
        if (wid == 0) STAMP(0, 2);
        fr = lane & 15; fq = lane >> 4;
        const float qs = wn ? 1.0f : 0.17677669529663687f * 1.4426950408889634f;
#pragma unroll
        for (int m = 0; m < 4; ++m) {
            const u32x4 w = swap16(pack4(accQ[0][m] * qs), pack4(accQ[1][m] * qs));
            const int r = (16 * m + fr) & 31, rho = (r & 19) | ((r & 4) << 1) | ((r & 8) >> 1), j = 2 * wm + (m >> 1);
            const int in = (fq & 1) * 1024 + ((fq >> 1) * 32 + rho) * 16;
            ldsw128(smem + (wn ? wm * AT_STAGE + (m >> 1) * 2048 : FW_TOP - 2 * FW_STAGE + j * 2048) + in, w);
        }
        const int rv = 8 * (fr >> 2) + 4 * wn + (fr & 3);
#pragma unroll
        for (int i = 0; i < 2; ++i) {
            const int in = (fq & 1) * 1024 + ((fq >> 1) * 32 + rv) * 16;
            const u32x4 w = swap16(pack4(accV[2 * i]), pack4(accV[2 * i + 1]));
            ldsw128(smem + ((wm + i) & 3) * AT_STAGE + 6144 - i * 2048 + in, w);
#pragma unroll
            for (int n = 0; n < 2; ++n) {
                const u32x4 d = swap16(pack4(accD[n][2 * i]), pack4(accD[n][2 * i + 1]));
                const int el = 32 * wn + 16 * n + fr, ch = 4 * i + 2 * (fq & 1) + (fq >> 1);
                ldsw128(smem + FB_BASE + wm * 8192 + el * 128 + ((ch ^ (fr >> 1)) * 16), d);
            }
        }
        LGKM_BAR();
        if (wid == 0) STAMP(0, 3);
    }
    {
        const int qt = mt * 8 + wid;
        char* f1 = ws + WS_F1 + hd * 512;
        const unsigned rec0 = (hd * 32 + 4 * mt) * 8192 + wid * 1024 + lane * 16;
#define PUBLISH(q) do { const u32x4 a_ = *(const LAS u32x4*)(smem + (q) * 8192 + wid * 1024 + lane * 16), b_ = *(const LAS u32x4*)(smem + FB_BASE + (q) * 8192 + wid * 1024 + lane * 16); \
        st_wt(rs, WS_KV + rec0 + (q) * 8192, a_); st_wt(rs, WS_AB + rec0 + (q) * 8192, b_); } while (0)
        PUBLISH(0); PUBLISH(1);
        const bf16x8 sel = attn_sel(lane);
        const LAS char* fb = smem + lane * 16;
        const bf16x8 q0 = ldsr128(fb + FW_TOP - 2 * FW_STAGE + wid * 2048), q1 = ldsr128(fb + FW_TOP - 2 * FW_STAGE + wid * 2048 + 1024);
        bf16x8 kA0, kA1, kB0, kB1, vA0, vA1, vB0, vB1 = bf16x8{};
        f32x16 negm, o = f32x16{}, sA, sB; f32x4 l = f32x4{};
        u32x4 p0, p1 = u32x4{};
        unsigned sink = 0; u64 fv = 0;
        kA0 = ldsr128(fb); kA1 = ldsr128(fb + 1024); kB0 = ldsr128(fb + 2048); kB1 = ldsr128(fb + 3072); vA0 = ldsr128(fb + 6144); vA1 = ldsr128(fb + 7168);
        LGKM_BAR();
        {
            sA = MFMA32(kA0, q0, o); sA = MFMA32(kA1, q1, sA);
            float mx = max16(sA); mx = fmaxf(mx, __shfl_xor(mx, 32)) + 40.f;
#pragma unroll
            for (int r = 0; r < 16; ++r) { negm[r] = -mx; sA[r] -= mx; }
        }
        asm volatile("s_waitcnt vmcnt(4)" : "+v"(ep), "+v"(gx0), "+v"(gx1), "+v"(gw0), "+v"(gw1) :: "memory");
        {
            float s = 0.f;
#pragma unroll
            for (int i = 0; i < 4; ++i) {
                s = fmaf(bflo(gx0[i]), bflo(gw0[i]), s); s = fmaf(bfhi(gx0[i]), bfhi(gw0[i]), s);
                s = fmaf(bflo(gx1[i]), bflo(gw1[i]), s); s = fmaf(bfhi(gx1[i]), bfhi(gw1[i]), s);
            }
#pragma unroll
            for (int o = 32; o >= 1; o >>= 1) s += __shfl_xor(s, o);
            if (lane == 0) __builtin_amdgcn_raw_buffer_store_b32(__float_as_uint(1.f / (1.f + __expf(-s))), rs, WS_GATE + (256 * mt + 8 * hd + wid) * 4, 0, 16);
        }
        base = (((u64)(unsigned)__builtin_amdgcn_readfirstlane(ep[1]) << 32) | (unsigned)__builtin_amdgcn_readfirstlane(ep[0])) + 8;
        if (wid == 0) STAMP(1, 0);
#define EX(S_, i) S_[i] = __builtin_amdgcn_exp2f(S_[i])
#define AT_STEP(SC, SN, K0, K1, VC0, VP1, RD0, RD1, RD2, RD3) do { \
        SN = MFMA32(K0, q0, negm); SBAR(); RD0; EX(SC, 0); EX(SC, 1); EX(SC, 2); EX(SC, 3); SBAR(); \
        o = MFMA32(VP1, BC(p1), o); PIN(o); SBAR(); RD1; EX(SC, 4); EX(SC, 5); EX(SC, 6); EX(SC, 7); p0[0] = cvtpk(SC[0], SC[1]); p0[1] = cvtpk(SC[2], SC[3]); SBAR(); \
        l = MFMA16(sel, BC(p1), l); PIN(l); SBAR(); p0[2] = cvtpk(SC[4], SC[5]); p0[3] = cvtpk(SC[6], SC[7]); EX(SC, 8); SBAR(); \
        SN = MFMA32(K1, q1, SN); SBAR(); RD2; EX(SC, 9); EX(SC, 10); EX(SC, 11); EX(SC, 12); SBAR(); \
        o = MFMA32(VC0, BC(p0), o); PIN(o); SBAR(); RD3; EX(SC, 13); EX(SC, 14); EX(SC, 15); p1[0] = cvtpk(SC[8], SC[9]); SBAR(); \
        l = MFMA16(sel, BC(p0), l); PIN(l); SBAR(); p1[1] = cvtpk(SC[10], SC[11]); p1[2] = cvtpk(SC[12], SC[13]); p1[3] = cvtpk(SC[14], SC[15]); SBAR(); } while (0)
        int sl = AT_STAGE;
        for (int T = 1; T <= 32; ++T) {
            if (T <= 4) {
                asm volatile("s_waitcnt vmcnt(0)" : "+v"(fv) :: "memory");
                if (wid == 1 && (T == 2 || (T == 4 && !__all(fv == base + 2)))) wait_flags(f1, 8, base + (T >> 1), 2 - (T >> 1), lane);
            }
            LGKM_BAR(); SBAR();
            TICK(1, T);
            if (T <= 3) {
                if (T <= 2 && wid == 0) set_flag(f1 + mt * 64, base + T, lane);
                if (T == 1) { PUBLISH(2); PUBLISH(3); }
                if (T == 3 && wid == 1) asm volatile("global_load_dwordx2 %0, %1, off sc1" : "+v"(fv) : "v"(f1 + (lane & 7) * 64) : "memory");
            }
            if (T == 4 * wid - 8 && T > 5)
                { asm volatile("global_load_dword %0, %1, %2" : "+v"(sink) : "v"(TW(lane)), "s"(wsb + ((mt + wid) & 7) * 512) : "memory");
                  if (wid == 7) asm volatile("global_load_dword %0, %1, %2" : "+v"(sink) : "v"(lane * 128), "s"(pf2) : "memory"); }
            SBAR();
            if (!ABL_ATT) {
            AT_STEP(sA, sB, kB0, kB1, vA0, vB1, kA0 = ldsr128(fb + sl), kA1 = ldsr128(fb + sl + 1024), vB0 = ldsr128(fb + sl + 4096), vB1 = ldsr128(fb + sl + 5120));
            AT_STEP(sB, sA, kA0, kA1, vB0, vA1, kB0 = ldsr128(fb + sl + 2048), kB1 = ldsr128(fb + sl + 3072), vA0 = ldsr128(fb + sl + 6144), vA1 = ldsr128(fb + sl + 7168)); }
            sl = (sl + AT_STAGE) & (4 * AT_STAGE - 1);
        }
        o = MFMA32(vB1, BC(p1), o); l = MFMA16(sel, BC(p1), l);
        if (wid == 0) STAMP(1, 1);
        bool bad = !(l[0] >= 7.8886e-31f && l[0] <= 3.0e38f);
#pragma unroll
        for (int r = 0; r < 16; ++r) bad |= !(fabsf(o[r]) <= 3.0e38f);
        const int r = lane & 31, qrow = qt * 32 + ((r & 19) | ((r & 4) << 1) | ((r & 8) >> 1));
        const unsigned ooff = WS_O + ((qrow >> 4) * 32 + hd) * 1024 + ((lane >> 5) * 32 + (qrow & 15)) * 16;
        if (!ABL_ATT && !ABL_FDMA && __builtin_expect(__any(bad), 0)) attn_slow(rs, ws + WS_KV + hd * 262144, q0, q1, lane, ooff);
        else attn_store(rs, ooff, o, l[0]);
        WAIT_BAR(0);
        asm volatile("" :: "v"(sink));
        if (wid == 0) STAMP(1, 2);
    }
    {
        const int rt = 2 * (blockIdx.x & 7) + (blockIdx.x >> 7), nb = ((blockIdx.x >> 3) & 15) * 64;
        const int kq = fr >> 2, swz = ((kq >> 1) & 1) | ((fq & 1) << 1);
        const int aW = (8 * fq + kq) * 128 + ((((lane & 3) >> 1) ^ (swz << 1)) * 16) + (lane & 1) * 8;
#define P3_RD(W_, ks_) do { const int kt_ = (ks_) >> 1; \
            const int wb_ = (kt_ < 4 ? FW_TOP - 2 * FW_STAGE + kt_ * 8192 : kt_ == 4 ? FB_BASE : kt_ < 9 ? FW_TOP + (kt_ - 5) * 8192 : kt_ == 9 ? FB_BASE + 8192 \
                              : kt_ < 14 ? FW_TOP - FW_STAGE + (kt_ - 10) * 8192 : FB_BASE + (kt_ - 12) * 8192) + ((ks_) & 1) * 4096; \
            _Pragma("unroll") for (int n_ = 0; n_ < 4; ++n_) W_[n_] = ldstr(smem + wb_ + (aW ^ (n_ * 32)), 512); } while (0)
        bf16x8 wf[2][4];
        P3_RD(wf[0], 0);
        if (wid == 0) {
            set_flag(ws + WS_F2 + (mt * 32 + hd) * 64, base + 5, lane);
            wait_flags(ws + WS_F2 + (rt >> 1) * 2048, 32, base + 5, 0, lane);
            __builtin_amdgcn_fence(__ATOMIC_ACQUIRE, "wavefront");
            if (blockIdx.x == 0) set_flag(ws, base, lane);
            STAMP(1, 3);
        }
        LGKM_BAR();
        const int R0 = rt * 128 + 16 * wid;
        const int t = R0 + fr, col = nb + 16 * (fq & 1) + 8 * (fq >> 1);
        u32x4 of[24], y0[2], y1[2]; float g;
#pragma unroll
        for (int ks = 0; ks < 24; ++ks) of[ks] = ld_dev(rs, WS_O + ((R0 >> 4) * 32 + ks) * 1024 + lane * 16);
        SBAR();
        f32x4 acc[4];
#pragma unroll
        for (int n = 0; n < 4; ++n) acc[n] = f32x4{};
#pragma unroll
        for (int ks = 0; ks < 32; ++ks) {
            if (ks + 1 < 32) P3_RD(wf[(ks + 1) & 1], ks + 1);
            SBAR();
#pragma unroll
            for (int n = 0; n < 4; ++n) acc[n] = MFMA16(wf[ks & 1][n], BC(of[ks % 24]), acc[n]);
            if (ks < 8) of[ks] = ld_dev(rs, WS_O + ((R0 >> 4) * 32 + 24 + ks) * 1024 + lane * 16);
            if (ks == 12) {
                g = __uint_as_float(__builtin_amdgcn_raw_buffer_load_b32(rs, WS_GATE + t * 4, 0, 16));
#pragma unroll
                for (int i = 0; i < 2; ++i) { y0[i] = ld_dev(rs, WS_Y + (t * DM + col + 32 * i) * 2); y1[i] = ld_dev(rs, WS_Y + 4 * MB + (t * DM + col + 32 * i) * 2); }
            }
            SBAR();
        }
        const float g1 = 1.f - g;
#pragma unroll
        for (int i = 0; i < 2; ++i) {
            float v[8];
#pragma unroll
            for (int j = 0; j < 4; ++j) {
                auto r = __builtin_amdgcn_permlane16_swap(__float_as_uint(acc[2 * i][j]), __float_as_uint(acc[2 * i + 1][j]), false, false);
                v[j] = __uint_as_float(r[0]); v[4 + j] = __uint_as_float(r[1]);
            }
            u32x4 w;
#pragma unroll
            for (int j = 0; j < 4; ++j)
                w[j] = cvtpk(g * (bflo(y0[i][j]) - bflo(y1[i][j])) + g1 * v[2 * j], g * (bfhi(y0[i][j]) - bfhi(y1[i][j])) + g1 * v[2 * j + 1]);
            *(u32x4*)(out + (size_t)t * DM + col + 32 * i) = w;
        }
        if (wid == 0) STAMP(1, 4);
    }
}

DI void side_waves(LAS char* smem, int lw, int lane, int mt, int hd, const u16* __restrict__ x, const u16* __restrict__ wsr, const u16* __restrict__ wsi,
                   const u16* __restrict__ wdr, const u16* __restrict__ wdi, const u16* __restrict__ wqkv, const u16* __restrict__ wo,
                   const u16* __restrict__ gw, char* ws, rsrc_t rs) {
    const unsigned lds0 = (unsigned)(size_t)smem + lw * 1024;
    const unsigned rowl = 8 * lw + (lane >> 3), chk = ((lane & 7) ^ ((4 * lw + (lane >> 4)) & 7)) * 16;
    const int c = hd >> 4;
    const char* wb = (const char*)(c ? wsi : wsr) + (size_t)mt * 256 * 4096;
    unsigned vo[10];
    {
        const char* xb = (const char*)x + (size_t)mt * 256 * 2048;
        const char* wdb = (const char*)(c ? wdi : wdr) + (size_t)(hd & 15) * 64 * 2048;
        const char* wqb = (const char*)wqkv + hd * 64 + lw * 16 * 6144;
#pragma unroll
        for (int i = 0; i < 8; ++i) vo[i] = (rowl + 32 * i) * 2048 + chk;
        const unsigned voffW = (lane >> 2) * 6144 + (((lane & 3) ^ ((lane >> 5) << 1)) * 16);
#define K1_DMA(t, slot) do { const char* xa_ = xb + (size_t)(t) * 128; const char* wd_ = wdb + (size_t)(t) * 128; const char* wq_ = wqb + (size_t)(t) * 64 * 6144; \
        _Pragma("unroll") for (int u_ = 0; u_ < 3; ++u_) dma(voffW, wq_ + u_ * 2048, lds0 + (slot) + K1_WQ + u_ * 4096); \
        _Pragma("unroll") for (int i_ = 0; i_ < 2; ++i_) dma(vo[i_], wd_, lds0 + (slot) + K1_WD + i_ * 4096); \
        _Pragma("unroll") for (int i_ = 0; i_ < 8; ++i_) dma(vo[i_], xa_, lds0 + (slot) + K1_A + i_ * 4096); } while (0)
        K1_DMA(0, 0); K1_DMA(1, K1_STAGE); K1_DMA(2, 2 * K1_STAGE);
        WAIT_BAR(26);
        int sl = 0;
        for (int t = 0; t < 13; ++t) {
            WAIT_BAR(13);
            K1_DMA(t + 3, sl);
            sl = (sl == 2 * K1_STAGE) ? 0 : sl + K1_STAGE;
        }
        WAIT_BAR(13);
        WAIT_BAR(0);
#pragma unroll
        for (int i = 0; i < 8; ++i) vo[i] = (rowl + 32 * i) * 4096 + chk;
        vo[8] = lane * 16 + lw * 1024; vo[9] = vo[8] + 4096;
#pragma unroll
        for (int i = 0; i < 7; ++i) dma(vo[i], wb + (size_t)((4 * mt) & 31) * 128, lds0 + FW_TOP + i * 4096);
#pragma unroll
        for (int i = 0; i < 8; ++i) dma(vo[i], wb + (size_t)((4 * mt + 1) & 31) * 128, lds0 + FW_TOP - FW_STAGE + i * 4096);
        LGKM_BAR();
        dma(vo[7], wb + (size_t)((4 * mt) & 31) * 128, lds0 + FW_TOP + 7 * 4096);
        WAIT_BAR(0);
    }
    {
        const char* kvb = ws + WS_KV + hd * 262144;
        const unsigned vl = lane * 16 + lw * 1024;
        const char *bW = wb, *bA = ws + WS_AB + hd * 262144; int kmask = 31, kstep = 128, kstepA = 8192, koff = 4 * mt;
#define FN_DMA_P(p, s, fslot, aslot) do { \
        if ((p) < 8) dma(vo[p], bW + (size_t)((koff + (s)) & kmask) * kstep, lds0 + (fslot) + (p) * 4096); \
        else if ((p) < 10) dma_dev(vo[p], bA + (size_t)((koff + (s)) & kmask) * kstepA, lds0 + FB_BASE + (aslot) + ((p) - 8) * 4096); \
        else if ((p) == 10) dma_dev(vl, kvb + ((4 * mt + (s)) & 31) * 8192, lds0 + (aslot)); \
        else dma_dev(vl, kvb + ((4 * mt + (s) - ((((s) & 3) == 0 && lw < 2) ? 4 : 0)) & 31) * 8192 + 4096, lds0 + (aslot) + 4096); } while (0)
        const int fr = lane & 15, fq = lane >> 4;
        const int cpart = ((fq ^ (fr >> 1)) & 7) * 16;
        const int aW = (64 * lw + fr) * 128 + cpart, aA = FB_BASE + fr * 128 + cpart;
        f32x4 acc[4][4];
#pragma unroll
        for (int m = 0; m < 4; ++m)
#pragma unroll
            for (int n = 0; n < 4; ++n) acc[m][n] = f32x4{};
        FnFrag X, Yf;
#define FN_RD(F, i, slot, bslot, ks) do { \
        if ((i) == 0) F.w[0] = ldsr128(smem + (slot) + (aW ^ ((ks) * 64))); \
        else if ((i) < 5) F.a[(i) - 1] = ldsr128(smem + (bslot) + (aA ^ ((ks) * 64)) + ((i) - 1) * 2048); \
        else F.w[(i) - 4] = ldsr128(smem + (slot) + (aW ^ ((ks) * 64)) + ((i) - 4) * 2048); } while (0)
#define FN_MF(F, j) do { if (!ABL_FMFMA) acc[(j) >> 2][(j) & 3] = MFMA16(F.a[(j) & 3], F.w[(j) >> 2], acc[(j) >> 2][(j) & 3]); else asm volatile("" :: "v"(F.a[(j) & 3]), "v"(F.w[(j) >> 2])); } while (0)
#define FN_G(g, C, R, rslot, rb, ks, P0, s, fslot, aslot) do { FN_MF(C, 2 * (g)); FN_RD(R, g, rslot, rb, ks); FN_MF(C, 2 * (g) + 1); \
        if (!ABL_FDMA && ((g) & 3) != 0 && (P0) + (g) - 1 - ((g) >> 2) < PMAX) FN_DMA_P((P0) + (g) - 1 - ((g) >> 2), s, fslot, aslot); SBAR(); FN_PAD(g); } while (0)
#define FN_PHASE(C, R, rslot, rb, ks, P0, s, fslot, aslot) do { FN_G(0, C, R, rslot, rb, ks, P0, s, fslot, aslot); FN_G(1, C, R, rslot, rb, ks, P0, s, fslot, aslot); \
        FN_G(2, C, R, rslot, rb, ks, P0, s, fslot, aslot); FN_G(3, C, R, rslot, rb, ks, P0, s, fslot, aslot); FN_G(4, C, R, rslot, rb, ks, P0, s, fslot, aslot); \
        FN_G(5, C, R, rslot, rb, ks, P0, s, fslot, aslot); FN_G(6, C, R, rslot, rb, ks, P0, s, fslot, aslot); FN_G(7, C, R, rslot, rb, ks, P0, s, fslot, aslot); } while (0)
#define PMAX 12
        __builtin_amdgcn_s_setprio(FN_PRIO);
#pragma unroll
        for (int i = 0; i < 8; ++i) FN_RD(X, i, FW_TOP, 0, 0);
        LGKM_BAR();
        int f0 = FW_TOP, f1 = FW_TOP - FW_STAGE, f2 = FW_TOP - 2 * FW_STAGE;
        FN_DMA_P(0, 2, f2, 0); FN_DMA_P(1, 2, f2, 0); FN_DMA_P(2, 2, f2, 0); FN_DMA_P(3, 2, f2, 0); FN_DMA_P(4, 2, f2, 0); FN_DMA_P(5, 2, f2, 0);
        SBAR();
#define FN_ITER(t, WAITN) do { \
            FN_PHASE(X, Yf, f0, ((t) & 3) * 8192, 1, 6, (t) + 2, f2, (((t) + 2) & 3) * 8192);       \
            WAIT_BAR(WAITN); SBAR();                                                                \
            FN_SWITCH(t); \
            FN_PHASE(Yf, X, f1, (((t) + 1) & 3) * 8192, 0, 0, (t) + 3, f0, 0);                      \
            const int ft = f0; f0 = f1; f1 = f2; f2 = ft; } while (0)
#define FN_SWITCH(t)
#undef PMAX
#define PMAX 8
        FN_ITER(0, 8); FN_ITER(1, 8);
#undef PMAX
#define PMAX 12
#undef FN_SWITCH
#define FN_SWITCH(t) do { if ((t) == 29) { bW = bA = (const char*)wo + ((blockIdx.x >> 3) & 15) * 128; kmask = 3; kstep = kstepA = 320 * 2048; koff = 0; \
                const unsigned cw = ((lane & 7) ^ ((((lane >> 4) & 1) | ((lw & 1) << 1)) << 1)) * 16; \
                _Pragma("unroll") for (int i = 0; i < 10; ++i) vo[i] = (rowl + 32 * i) * 2048 + cw; } SBAR(); } while (0)
        for (int t = 2; t < 31; ++t) FN_ITER(t, 12);
#undef PMAX
#define PMAX 10
        FN_PHASE(X, Yf, f0, 3 * 8192, 1, 6, 33, f2, 8192);
        WAIT_BAR(10); SBAR();
#pragma unroll
        for (int i = 0; i < 8; ++i) dma(vo[i], bW + (size_t)640 * 2048, lds0 + FW_TOP - FW_STAGE + i * 4096);
#pragma unroll
        for (int i = 0; i < 4; ++i) dma(vo[i], bW + (size_t)896 * 2048, lds0 + FB_BASE + 2 * 8192 + i * 4096);
#pragma unroll
        for (int j = 0; j < 16; ++j) FN_MF(Yf, j);
        const unsigned yb = WS_Y + c * 4 * MB + ((mt * 256 + 64 * lw + fr) * DM + (hd & 15) * 64 + 16 * (fq & 1) + 8 * (fq >> 1)) * 2;
#pragma unroll
        for (int m = 0; m < 4; ++m)
#pragma unroll
            for (int i = 0; i < 2; ++i) st_wt(rs, yb + (m * 16 * DM + 32 * i) * 2, swap16(pack4(acc[m][2 * i]), pack4(acc[m][2 * i + 1])));
        WAIT_BAR(0);
    }
}

__global__ __launch_bounds__(768, 3) void fused_kernel(const u16* __restrict__ x, const u16* __restrict__ wsr, const u16* __restrict__ wsi,
                                                        const u16* __restrict__ wdr, const u16* __restrict__ wdi, const u16* __restrict__ wqkv,
                                                        const u16* __restrict__ wo, const u16* __restrict__ gw, u16* __restrict__ out, char* ws) {
    __shared__ __attribute__((aligned(1024))) char smem_[163840];
    LAS char* smem = (LAS char*)smem_;
    const int tid = threadIdx.x, lane = tid & 63, wid = __builtin_amdgcn_readfirstlane(tid >> 6);
    const int xcd = blockIdx.x & 7, idx = blockIdx.x >> 3;
    const int hd = xcd * 4 + (idx >> 3), mt = idx & 7;
    const rsrc_t rs = __builtin_amdgcn_make_buffer_rsrc(ws, 0, WS_END, 0x00020000);
    if (wid == 0) STAMP(0, 0);
    if (wid < 8) {
        const char* wdb = (const char*)((hd >> 4) ? wdi : wdr) + (size_t)(hd & 15) * 64 * 2048;
        const char* wsb = (const char*)((hd >> 4) ? wsi : wsr) + (size_t)mt * 256 * 4096;
        const char* pf2 = (const char*)wo + (size_t)blockIdx.x * 8192;
        main_waves(smem, wid, lane, mt, hd, ws, rs, out, x, gw, wdb, (const char*)wqkv, wsb, pf2);
    }
    else side_waves(smem, wid - 8, lane, mt, hd, x, wsr, wsi, wdr, wdi, wqkv, wo, gw, ws, rs);
}

extern "C" void kernel_launch(void* const* d_in, const int* in_sizes, int n_in, void* d_out, int out_size, void* d_ws, size_t ws_size, hipStream_t stream) {
    const u16* x = (const u16*)d_in[0]; const u16* wsr = (const u16*)d_in[1]; const u16* wsi = (const u16*)d_in[2];
    const u16* wdr = (const u16*)d_in[3]; const u16* wdi = (const u16*)d_in[4]; const u16* wqkv = (const u16*)d_in[6];
    const u16* wo = (const u16*)d_in[7]; const u16* gw = (const u16*)d_in[8];
    fused_kernel<<<256, 768, 0, stream>>>(x, wsr, wsi, wdr, wdi, wqkv, wo, gw, (u16*)d_out, (char*)d_ws);
}
```

```cpp
#define SPIN_TO 6000ull
#include <hip/hip_runtime.h>
#include <hip/hip_bf16.h>

#define O_AUX 16
#define O_ACQ 0
typedef unsigned short u16;
typedef short bf16x8 __attribute__((ext_vector_type(8)));
typedef short s16x4 __attribute__((ext_vector_type(4)));
typedef float f32x2 __attribute__((ext_vector_type(2)));
typedef float f32x4 __attribute__((ext_vector_type(4)));
typedef float f32x16 __attribute__((ext_vector_type(16)));
typedef unsigned u32x4 __attribute__((ext_vector_type(4)));
typedef unsigned u32x2 __attribute__((ext_vector_type(2)));
typedef __bf16 bf16x2_t __attribute__((ext_vector_type(2)));
#define LAS __attribute__((address_space(3)))
#define GAS __attribute__((address_space(1)))
#define DI __device__ __forceinline__
#define SBAR() __builtin_amdgcn_sched_barrier(0)
#define PIN(x) asm volatile("" : "+v"(x))
#define MFMA16(a, b, c) __builtin_amdgcn_mfma_f32_16x16x32_bf16(a, b, c, 0, 0, 0)
#define MFMA32(a, b, c) __builtin_amdgcn_mfma_f32_32x32x16_bf16(a, b, c, 0, 0, 0)
#define WAIT_BAR(N) asm volatile("s_waitcnt vmcnt(" #N ") lgkmcnt(0)\n\ts_barrier" ::: "memory")
#define LGKM_BAR() asm volatile("s_waitcnt lgkmcnt(0)\n\ts_barrier" ::: "memory")
#define RLX_AGENT __ATOMIC_RELAXED, __HIP_MEMORY_SCOPE_AGENT
#define BC(x) __builtin_bit_cast(bf16x8, x)

constexpr int SEQ = 2048, DM = 1024;
constexpr unsigned MB = 1u << 20;
constexpr unsigned WS_F1 = 4096, WS_F2 = 4096 + 16384, WS_GATE = 65536, WS_KV = 1 * MB, WS_AB = 9 * MB, WS_O = 17 * MB, WS_Y = 21 * MB, WS_END = 29 * MB;
typedef unsigned long long u64;

#define STAMP(kid, i)
#define TICK(k, i)

DI unsigned cvtpk(float lo, float hi) { f32x2 v = {lo, hi}; bf16x2_t b = __builtin_convertvector(v, bf16x2_t); return __builtin_bit_cast(unsigned, b); }
DI void dma(unsigned voff, const char* sbase, unsigned l) {
    asm volatile("s_mov_b32 m0, %2\n\ts_nop 0\n\tglobal_load_lds_dwordx4 %0, %1" :: "v"(voff), "s"(sbase), "s"(l) : "memory"); }
DI void dma_dev(unsigned voff, const char* sbase, unsigned l) {
    asm volatile("s_mov_b32 m0, %2\n\ts_nop 0\n\tglobal_load_lds_dwordx4 %0, %1 sc1" :: "v"(voff), "s"(sbase), "s"(l) : "memory"); }
DI void ldsw128(LAS char* p, u32x4 v) { *(LAS u32x4*)p = v; }
DI bf16x8 ldsr128(const LAS char* p) { return *(const LAS bf16x8*)p; }
DI bf16x8 ldstr(const LAS char* p, int off2) {
    s16x4 a = __builtin_amdgcn_ds_read_tr16_b64_v4i16((LAS s16x4*)p);
    s16x4 b = __builtin_amdgcn_ds_read_tr16_b64_v4i16((LAS s16x4*)(p + off2));
    return (bf16x8){a[0], a[1], a[2], a[3], b[0], b[1], b[2], b[3]};
}
DI int lane_now() { int l; asm volatile("v_mbcnt_lo_u32_b32 %0, -1, 0\n\tv_mbcnt_hi_u32_b32 %0, -1, %0" : "=v"(l)); return l; }
DI float bflo(unsigned u) { return __uint_as_float(u << 16); }
DI float bfhi(unsigned u) { return __uint_as_float(u & 0xffff0000u); }
DI u32x4 swap16(u32x2 a, u32x2 b) {
    auto r0 = __builtin_amdgcn_permlane16_swap(a[0], b[0], false, false);
    auto r1 = __builtin_amdgcn_permlane16_swap(a[1], b[1], false, false);
    return (u32x4){r0[0], r1[0], r0[1], r1[1]};
}
DI u32x2 pack4(f32x4 v) { return (u32x2){cvtpk(v[0], v[1]), cvtpk(v[2], v[3])}; }
typedef __amdgpu_buffer_rsrc_t rsrc_t;
DI void st_wt(rsrc_t r, unsigned off, u32x4 v) { __builtin_amdgcn_raw_buffer_store_b128(v, r, off, 0, 16); }
DI u32x4 ld_dev(rsrc_t r, unsigned off) { return __builtin_amdgcn_raw_buffer_load_b128(r, off, 0, 16); }
DI u32x4 ld_o(rsrc_t r, unsigned off, unsigned soff) { return __builtin_amdgcn_raw_buffer_load_b128(r, off, soff, 16); }
DI void wait_flags(const char* first, int n, u64 lo, u64 span, int lane) {
    const GAS u64* p = (const GAS u64*)(first + (lane < n ? lane : 0) * 64);
#pragma unroll 1
    for (unsigned spins = 0; spins < (1u << 16); ++spins) {
        const u64 v = __hip_atomic_load(p, RLX_AGENT);
        if (__all(v - lo <= span)) break;
        __builtin_amdgcn_s_sleep(1);
    }
}
DI void set_flag(char* p, u64 v, int lane) { if (lane == 0) __hip_atomic_store((GAS u64*)p, v, RLX_AGENT); }

#define ABL_FDMA 0
#define ABL_FMFMA 0
#define ABL_ATT 0
#define PADN 0
#define FN_PAD(g) do { _Pragma("unroll") for (int z_ = 0; z_ < PADN; ++z_) asm volatile("s_nop 3"); SBAR(); } while (0)
#define FN_PRIO 1
#define PF_MODE 0
#define TLB_WARM 0
#define TLB_SUB 16384
#define PF_PRE 1
#define PF_N0 3
#define PF_LEAD 8
#define PF_SLEEP 12
constexpr int K1_STAGE = 53248, K1_A = 0, K1_WD = 32768, K1_WQ = 40960;
constexpr int AT_STAGE = 8192, FB_BASE = 32768, FW_STAGE = 32768, FW_TOP = 131072;
struct K1Frag { bf16x8 x[4], wd[2], wqk[2], wv; };
struct FnFrag { bf16x8 w[4], a[4]; };

DI void attn_store(rsrc_t rs, unsigned off, const f32x16& o, float l) {
    const float rl = 1.0f / l; u32x4 w0, w1;
#pragma unroll
    for (int i = 0; i < 4; ++i) { w0[i] = cvtpk(o[2 * i] * rl, o[2 * i + 1] * rl); w1[i] = cvtpk(o[8 + 2 * i] * rl, o[9 + 2 * i] * rl); }
    st_wt(rs, off, w0); st_wt(rs, off + 256, w1);
}
DI bf16x8 attn_sel(int lane) {
    const short v = (((lane >> 2) & 1) == ((lane >> 4) & 1)) ? (short)0x3f80 : (short)0;
    return (bf16x8){v, v, v, v, v, v, v, v};
}
DI float max16(const f32x16& s) {
    float a = fmaxf(fmaxf(s[0], s[1]), s[2]), b = fmaxf(fmaxf(s[3], s[4]), s[5]);
#pragma unroll
    for (int r = 6; r < 16; r += 4) { a = fmaxf(fmaxf(a, s[r]), s[r + 1]); if (r + 3 < 16) b = fmaxf(fmaxf(b, s[r + 2]), s[r + 3]); }
    return fmaxf(a, b);
}
DI void attn_slow(rsrc_t rs, const char* kv, bf16x8 q0, bf16x8 q1, int lane, unsigned ooff) {
    __builtin_amdgcn_fence(__ATOMIC_ACQUIRE, "agent");
    const bf16x8 sel = attn_sel(lane);
    kv += lane * 16;
    float mx = -INFINITY;
    const f32x16 z = f32x16{};
#pragma unroll 1
    for (int j = 0; j < 64; ++j) {
        const char* kp = kv + (j >> 1) * 8192 + (j & 1) * 2048;
        f32x16 s = MFMA32(*(const bf16x8*)kp, q0, z); s = MFMA32(*(const bf16x8*)(kp + 1024), q1, s);
        mx = fmaxf(mx, max16(s));
    }
    mx = fmaxf(mx, __shfl_xor(mx, 32));
    f32x16 negm, o = f32x16{}; f32x4 l = f32x4{};
#pragma unroll
    for (int r = 0; r < 16; ++r) negm[r] = -mx;
#pragma unroll 1
    for (int j = 0; j < 64; ++j) {
        const char* kp = kv + (j >> 1) * 8192 + (j & 1) * 2048;
        const char* vp = kv + ((j & 1) ? (((j & 7) == 7 ? (j >> 3) * 4 : (j + 1) >> 1) * 8192 + 4096) : ((j >> 1) * 8192 + 6144));
        const bf16x8 v0 = *(const bf16x8*)vp, v1 = *(const bf16x8*)(vp + 1024);
        f32x16 s = MFMA32(*(const bf16x8*)kp, q0, negm); s = MFMA32(*(const bf16x8*)(kp + 1024), q1, s);
        u32x4 p0, p1;
#pragma unroll
        for (int r = 0; r < 16; ++r) s[r] = __builtin_amdgcn_exp2f(s[r]);
#pragma unroll
        for (int r = 0; r < 4; ++r) { p0[r] = cvtpk(s[2 * r], s[2 * r + 1]); p1[r] = cvtpk(s[8 + 2 * r], s[9 + 2 * r]); }
        o = MFMA32(v0, BC(p0), o); l = MFMA16(sel, BC(p0), l);
        o = MFMA32(v1, BC(p1), o); l = MFMA16(sel, BC(p1), l);
    }
    attn_store(rs, ooff, o, l[0]);
}

DI void touch8k(const char* p, unsigned voff, unsigned dump) {
    asm volatile("s_mov_b32 m0, %2\n\ts_nop 0\n\tglobal_load_lds_dword %0, %1" :: "v"(voff), "s"(p), "s"(dump) : "memory"); }
DI void touch_pair(int p, int l, int mt, int hd, const char* x, const char* wdb, const char* wqkv, unsigned dump) {
    const int n = l >= 56 ? l - 8 : l, i = (mt * 2 + (hd & 1)) * 24 + (n - 32);
    const char* a = n < 16 ? x + (size_t)(mt * 256 + hd * 8 + (n >> 1)) * 2048 + p * 256 + (n & 1) * 128
                  : n < 32 ? wdb + (size_t)(mt * 8 + ((n - 16) >> 1)) * 2048 + p * 256 + (n & 1) * 128
                           : wqkv + (size_t)(p * 128 + i / 3) * 6144 + (i % 3) * 2048 + (hd >> 1) * 128;
    asm volatile("s_mov_b32 m0, %1\n\ts_nop 0\n\tglobal_load_lds_dword %0, off" :: "v"(a), "s"(dump) : "memory");
}
DI void main_waves(unsigned long long dg0, LAS char* smem, int wid, int lane, int mt, int hd, char* ws, rsrc_t rs, u16* __restrict__ out,
                   const u16* __restrict__ x, const u16* __restrict__ gw, const char* wdb, const char* wqkv, const char* wsb, const char* pf2) {
    const unsigned dump = (unsigned)(size_t)smem + 3 * K1_STAGE + wid * 256;
    if (wid >= 1 && wid <= PF_PRE) { __builtin_amdgcn_s_sleep(PF_SLEEP); touch_pair(wid, lane, mt, hd, (const char*)x, wdb, wqkv, dump); }
    const int t_act = wid < 4 ? 7 + 2 * wid : 2 * wid - PF_LEAD;
#define TW(l_) (unsigned)(((hd & 15) * 16 + ((l_) >> 2)) * 4096 + ((l_) & 3) * 128)
    int fr = lane & 15, fq = lane >> 4;
    u64 ep, base; u32x4 gx0, gx1, gw0, gw1;
    {
        const int wm = wid >> 1, wn = wid & 1;
        const int cpart = ((fq ^ (fr >> 1)) & 7) * 16;
        const int aA = K1_A + (64 * wm + fr) * 128 + cpart, aD = K1_WD + (32 * wn + fr) * 128 + cpart;
        const int aT0 = (8 * fq + (fr >> 2)) * 64 + ((((lane & 3) >> 1) ^ ((fq & 1) << 1)) * 16) + (lane & 1) * 8;
        const int aQ = K1_WQ + wn * 4096 + aT0, aV = K1_WQ + 8192 + (aT0 ^ (wn * 32));
        f32x4 accQ[2][4], accV[4], accD[2][4];
#pragma unroll
        for (int m = 0; m < 4; ++m) { accQ[0][m] = f32x4{}; accQ[1][m] = f32x4{}; accV[m] = f32x4{}; accD[0][m] = f32x4{}; accD[1][m] = f32x4{}; }
        K1Frag X, Y;
#define K1_RD(F, i, slot, ks) do { const LAS char* sb_ = smem + (slot); \
        if ((i) == 0) F.wqk[0] = ldstr(sb_ + aQ + (ks) * 2048, 256); \
        else if ((i) == 1) { F.x[0] = ldsr128(sb_ + (aA ^ ((ks) * 64))); } \
        else if ((i) == 2) F.wqk[1] = ldstr(sb_ + (aQ ^ 32) + (ks) * 2048, 256); \
        else if ((i) == 3) F.wv = ldstr(sb_ + aV + (ks) * 2048, 256); \
        else if ((i) == 4) { F.wd[0] = ldsr128(sb_ + (aD ^ ((ks) * 64))); F.wd[1] = ldsr128(sb_ + (aD ^ ((ks) * 64)) + 2048); } \
        else if ((i) < 8) F.x[(i) - 4] = ldsr128(sb_ + (aA ^ ((ks) * 64)) + ((i) - 4) * 2048); } while (0)
#define K1_MF(F, j) do { constexpr int m_ = (j) / 5, r_ = (j) % 5; \
        if (r_ == 0) accQ[0][m_] = MFMA16(F.wqk[0], F.x[m_], accQ[0][m_]); \
        else if (r_ == 1) accQ[1][m_] = MFMA16(F.wqk[1], F.x[m_], accQ[1][m_]); \
        else if (r_ == 2) accV[m_] = MFMA16(F.x[m_], F.wv, accV[m_]); \
        else if (r_ == 3) accD[0][m_] = MFMA16(F.x[m_], F.wd[0], accD[0][m_]); \
        else accD[1][m_] = MFMA16(F.x[m_], F.wd[1], accD[1][m_]); } while (0)
#define K1_G(g, C, R, rslot, ks) do { K1_MF(C, 2 * (g)); if ((g) == 0) SBAR(); K1_RD(R, g, rslot, ks); K1_MF(C, 2 * (g) + 1); SBAR(); } while (0)
#define K1_PHASE(C, R, rslot, ks) do { K1_G(0, C, R, rslot, ks); K1_G(1, C, R, rslot, ks); K1_G(2, C, R, rslot, ks); K1_G(3, C, R, rslot, ks); K1_G(4, C, R, rslot, ks); \
        K1_G(5, C, R, rslot, ks); K1_G(6, C, R, rslot, ks); K1_G(7, C, R, rslot, ks); K1_G(8, C, R, rslot, ks); K1_G(9, C, R, rslot, ks); } while (0)
        asm volatile("" :: "s"(gw), "s"(out));
        __builtin_amdgcn_s_waitcnt(0xC07F);
        LGKM_BAR();
        if (wid == 0) STAMP(0, 1);
        if (wid > PF_PRE && wid <= 3) touch_pair(wid, lane, mt, hd, (const char*)x, wdb, wqkv, dump);
#pragma unroll
        for (int i = 0; i < 8; ++i) K1_RD(X, i, 0, 0);
        int sl = 0, sn = K1_STAGE;
        SBAR();
        for (int t = 0; t < 15; ++t) {
            K1_PHASE(X, Y, sl, 1);
            LGKM_BAR(); SBAR();
            TICK(0, t);
            if (__builtin_expect(t == t_act, 0)) {
                const int l_ = lane_now();
                if (wid < 4) touch8k(wsb + ((mt + wid) & 7) * 512, TW(l_), dump);
                else touch_pair(wid, l_, mt, hd, (const char*)x, wdb, wqkv, dump);
            }
            SBAR();
            K1_PHASE(Y, X, sn, 0);
            sl = sn; sn = (sn == 2 * K1_STAGE) ? 0 : sn + K1_STAGE;
        }
        K1_PHASE(X, Y, 0, 1);
        WAIT_BAR(0); SBAR();
        lane = lane_now();
        ep = __hip_atomic_load((const GAS u64*)ws, RLX_AGENT);
        { const u32x4* px = (const u32x4*)(x + (size_t)(256 * mt + 8 * hd + wid) * DM + lane * 16); const u32x4* pw = (const u32x4*)(gw + lane * 16);
          gx0 = px[0]; gx1 = px[1]; gw0 = pw[0]; gw1 = pw[1]; }
        SBAR();
        K1_MF(Y, 0); K1_MF(Y, 1); K1_MF(Y, 2); K1_MF(Y, 3); K1_MF(Y, 4); K1_MF(Y, 5); K1_MF(Y, 6); K1_MF(Y, 7); K1_MF(Y, 8); K1_MF(Y, 9);
        K1_MF(Y, 10); K1_MF(Y, 11); K1_MF(Y, 12); K1_MF(Y, 13); K1_MF(Y, 14); K1_MF(Y, 15); K1_MF(Y, 16); K1_MF(Y, 17); K1_MF(Y, 18); K1_MF(Y, 19);
        if (wid == 0) STAMP(0, 2);
        fr = lane & 15; fq = lane >> 4;
        const float qs = wn ? 1.0f : 0.17677669529663687f * 1.4426950408889634f;
#pragma unroll
        for (int m = 0; m < 4; ++m) {
            const u32x4 w = swap16(pack4(accQ[0][m] * qs), pack4(accQ[1][m] * qs));
            const int r = (16 * m + fr) & 31, rho = (r & 19) | ((r & 4) << 1) | ((r & 8) >> 1), j = 2 * wm + (m >> 1);
            const int in = (fq & 1) * 1024 + ((fq >> 1) * 32 + rho) * 16;
            ldsw128(smem + (wn ? wm * AT_STAGE + (m >> 1) * 2048 : FW_TOP - 2 * FW_STAGE + j * 2048) + in, w);
        }
        const int rv = 8 * (fr >> 2) + 4 * wn + (fr & 3);
#pragma unroll
        for (int i = 0; i < 2; ++i) {
            const int in = (fq & 1) * 1024 + ((fq >> 1) * 32 + rv) * 16;
            const u32x4 w = swap16(pack4(accV[2 * i]), pack4(accV[2 * i + 1]));
            ldsw128(smem + ((wm + i) & 3) * AT_STAGE + 6144 - i * 2048 + in, w);
#pragma unroll
            for (int n = 0; n < 2; ++n) {
                const u32x4 d = swap16(pack4(accD[n][2 * i]), pack4(accD[n][2 * i + 1]));
                const int el = 32 * wn + 16 * n + fr, ch = 4 * i + 2 * (fq & 1) + (fq >> 1);
                ldsw128(smem + FB_BASE + wm * 8192 + el * 128 + ((ch ^ (fr >> 1)) * 16), d);
            }
        }
        LGKM_BAR();
        if (wid == 0) STAMP(0, 3);
    }
    {
        const int qt = mt * 8 + wid;
        char* f1 = ws + WS_F1 + hd * 512;
        const unsigned rec0 = (hd * 32 + 4 * mt) * 8192 + wid * 1024 + lane * 16;
#define PUBLISH(q) do { const u32x4 a_ = *(const LAS u32x4*)(smem + (q) * 8192 + wid * 1024 + lane * 16), b_ = *(const LAS u32x4*)(smem + FB_BASE + (q) * 8192 + wid * 1024 + lane * 16); \
        st_wt(rs, WS_KV + rec0 + (q) * 8192, a_); st_wt(rs, WS_AB + rec0 + (q) * 8192, b_); } while (0)
        PUBLISH(0); PUBLISH(1);
        const bf16x8 sel = attn_sel(lane);
        const LAS char* fb = smem + lane * 16;
        const bf16x8 q0 = ldsr128(fb + FW_TOP - 2 * FW_STAGE + wid * 2048), q1 = ldsr128(fb + FW_TOP - 2 * FW_STAGE + wid * 2048 + 1024);
        bf16x8 kA0, kA1, kB0, kB1, vA0, vA1, vB0, vB1 = bf16x8{};
        f32x16 negm, o = f32x16{}, sA, sB; f32x4 l = f32x4{};
        u32x4 p0, p1 = u32x4{};
        kA0 = ldsr128(fb); kA1 = ldsr128(fb + 1024); kB0 = ldsr128(fb + 2048); kB1 = ldsr128(fb + 3072); vA0 = ldsr128(fb + 6144); vA1 = ldsr128(fb + 7168);
        SBAR(); LGKM_BAR(); SBAR();
        {
            sA = MFMA32(kA0, q0, o); sA = MFMA32(kA1, q1, sA);
            float mx = max16(sA); mx = fmaxf(mx, __shfl_xor(mx, 32)) + 40.f;
#pragma unroll
            for (int r = 0; r < 16; ++r) { negm[r] = -mx; sA[r] -= mx; }
        }
        {
            float s = 0.f;
#pragma unroll
            for (int i = 0; i < 4; ++i) {
                s = fmaf(bflo(gx0[i]), bflo(gw0[i]), s); s = fmaf(bfhi(gx0[i]), bfhi(gw0[i]), s);
                s = fmaf(bflo(gx1[i]), bflo(gw1[i]), s); s = fmaf(bfhi(gx1[i]), bfhi(gw1[i]), s);
            }
#pragma unroll
            for (int o = 32; o >= 1; o >>= 1) s += __shfl_xor(s, o);
            if (lane == 0) __builtin_amdgcn_raw_buffer_store_b32(__float_as_uint(1.f / (1.f + __expf(-s))), rs, WS_GATE + (256 * mt + 8 * hd + wid) * 4, 0, 16);
        }
        base = (((u64)(unsigned)__builtin_amdgcn_readfirstlane((unsigned)(ep >> 32)) << 32) | (unsigned)__builtin_amdgcn_readfirstlane((unsigned)ep)) + 8;
        if (wid == 0) STAMP(1, 0);
#define EX(S_, i) S_[i] = __builtin_amdgcn_exp2f(S_[i])
#define AT_STEP(SC, SN, K0, K1, VC0, VP1, RD0, RD1, RD2, RD3) do { \
        SN = MFMA32(K0, q0, negm); SBAR(); RD0; EX(SC, 0); EX(SC, 1); EX(SC, 2); EX(SC, 3); SBAR(); \
        o = MFMA32(VP1, BC(p1), o); PIN(o); SBAR(); RD1; EX(SC, 4); EX(SC, 5); EX(SC, 6); EX(SC, 7); p0[0] = cvtpk(SC[0], SC[1]); p0[1] = cvtpk(SC[2], SC[3]); SBAR(); \
        l = MFMA16(sel, BC(p1), l); PIN(l); SBAR(); p0[2] = cvtpk(SC[4], SC[5]); p0[3] = cvtpk(SC[6], SC[7]); EX(SC, 8); SBAR(); \
        SN = MFMA32(K1, q1, SN); SBAR(); RD2; EX(SC, 9); EX(SC, 10); EX(SC, 11); EX(SC, 12); SBAR(); \
        o = MFMA32(VC0, BC(p0), o); PIN(o); SBAR(); RD3; EX(SC, 13); EX(SC, 14); EX(SC, 15); p1[0] = cvtpk(SC[8], SC[9]); SBAR(); \
        l = MFMA16(sel, BC(p0), l); PIN(l); SBAR(); p1[1] = cvtpk(SC[10], SC[11]); p1[2] = cvtpk(SC[12], SC[13]); p1[3] = cvtpk(SC[14], SC[15]); SBAR(); } while (0)
        int sl = AT_STAGE;
#define AT_TICK() do { \
            AT_STEP(sA, sB, kB0, kB1, vA0, vB1, kA0 = ldsr128(fb + sl), kA1 = ldsr128(fb + sl + 1024), vB0 = ldsr128(fb + sl + 4096), vB1 = ldsr128(fb + sl + 5120)); \
            AT_STEP(sB, sA, kA0, kA1, vB0, vA1, kB0 = ldsr128(fb + sl + 2048), kB1 = ldsr128(fb + sl + 3072), vA0 = ldsr128(fb + sl + 6144), vA1 = ldsr128(fb + sl + 7168)); \
            sl = (sl + AT_STAGE) & (4 * AT_STAGE - 1); } while (0)
#pragma unroll 1
        for (int T = 1; T <= 2; ++T) {
            asm volatile("s_waitcnt vmcnt(0)" ::: "memory");
            if (wid == 1 && T == 2) wait_flags(f1, 8, base + 1, 1, lane);
            LGKM_BAR(); SBAR();
            TICK(1, T);
            if (wid == 0) set_flag(f1 + mt * 64, base + T, lane);
            if (T == 1) { PUBLISH(2); PUBLISH(3); }
            SBAR();
            AT_TICK();
        }
        LGKM_BAR(); SBAR();
        {
            const u64 fv = __hip_atomic_load((const GAS u64*)(f1 + (lane & 7) * 64), RLX_AGENT);
            SBAR();
            AT_TICK();
            if (wid == 1 && !__all(fv == base + 2)) wait_flags(f1, 8, base + 2, 0, lane);
        }
        const int t_touch = wid >= 4 ? 4 * wid - 8 : 0;
#pragma unroll 1
        for (int T = 4; T <= 32; ++T) {
            LGKM_BAR(); SBAR();
            TICK(1, T);
            if (__builtin_expect(T == t_touch, 0)) {
                asm volatile("global_load_dword v167, %0, %1" :: "v"(TW(lane)), "s"(wsb + ((mt + wid) & 7) * 512) : "memory", "v167");
                if (wid == 7) asm volatile("global_load_dword v167, %0, %1" :: "v"(lane * 128), "s"(pf2) : "memory", "v167");
            }
            SBAR();
            AT_TICK();
        }
        o = MFMA32(vB1, BC(p1), o); l = MFMA16(sel, BC(p1), l);
        if (wid == 0) STAMP(1, 1);
        bool bad = !(l[0] >= 7.8886e-31f && l[0] <= 3.0e38f);
#pragma unroll
        for (int r = 0; r < 16; ++r) bad |= !(fabsf(o[r]) <= 3.0e38f);
        const int r = lane & 31, qrow = qt * 32 + ((r & 19) | ((r & 4) << 1) | ((r & 8) >> 1));
        const unsigned ooff = WS_O + ((qrow >> 4) * 32 + hd) * 1024 + ((lane >> 5) * 32 + (qrow & 15)) * 16;
        if (!ABL_ATT && !ABL_FDMA && __builtin_expect(__any(bad), 0)) attn_slow(rs, ws + WS_KV + hd * 262144, q0, q1, lane, ooff);
        else attn_store(rs, ooff, o, l[0]);
        WAIT_BAR(0);
        if (wid == 0) STAMP(1, 2);
    }
    {
        const int rt = 2 * (blockIdx.x & 7) + (blockIdx.x >> 7), nb = ((blockIdx.x >> 3) & 15) * 64;
        const int kq = fr >> 2, swz = ((kq >> 1) & 1) | ((fq & 1) << 1);
        const int aW = (8 * fq + kq) * 128 + ((((lane & 3) >> 1) ^ (swz << 1)) * 16) + (lane & 1) * 8;
        const int kh = wid & 1;
        const int rot = (blockIdx.x >> 3) & 15;
#define P3_KS(ks_) (((ks_) + rot) & 15)
#define P3_RD(W_, ks_) do { const int wb_ = FB_BASE + (16 * kh + P3_KS(ks_)) * 4096; \
            _Pragma("unroll") for (int n_ = 0; n_ < 4; ++n_) W_[n_] = ldstr(smem + wb_ + (aW ^ (n_ * 32)), 512); } while (0)
        bf16x8 wf[2][4];
        P3_RD(wf[0], 0);
        if (wid == 0) {
            set_flag(ws + WS_F2 + (mt * 32 + hd) * 64, base + 5, lane);
            wait_flags(ws + WS_F2 + (rt >> 1) * 2048, 32, base + 5, 0, lane);
            __builtin_amdgcn_fence(__ATOMIC_ACQUIRE, "wavefront");
            if (blockIdx.x == 0) set_flag(ws, base, lane);
            STAMP(1, 3);
        }
        LGKM_BAR();
        const int R0 = rt * 128 + 16 * wid;
        const int t = R0 + fr, col = nb + 16 * (fq & 1) + 8 * (fq >> 1);
        const unsigned ob0 = WS_O + ((R0 >> 4) * 32 + 16 * kh) * 1024 + lane * 16, ob1 = WS_O + (((R0 >> 4) ^ 1) * 32 + 16 * kh) * 1024 + lane * 16;
        u32x4 of[10][2], y0[2], y1[2]; float g;
#pragma unroll
        for (int ks = 0; ks < 10; ++ks) { of[ks][0] = ld_o(rs, ob0, P3_KS(ks) * 1024); of[ks][1] = ld_o(rs, ob1, P3_KS(ks) * 1024); }
        SBAR();
        f32x4 acc[4], acp[4];
#pragma unroll
        for (int n = 0; n < 4; ++n) { acc[n] = f32x4{}; acp[n] = f32x4{}; }
#pragma unroll
        for (int ks = 0; ks < 16; ++ks) {
            if (ks + 1 < 16) P3_RD(wf[(ks + 1) & 1], ks + 1);
            SBAR();
#pragma unroll
            for (int n = 0; n < 4; ++n) acc[n] = MFMA16(wf[ks & 1][n], BC(of[ks % 10][0]), acc[n]);
#pragma unroll
            for (int n = 0; n < 4; ++n) acp[n] = MFMA16(wf[ks & 1][n], BC(of[ks % 10][1]), acp[n]);
            if (ks < 6) { of[ks][0] = ld_o(rs, ob0, P3_KS(10 + ks) * 1024); of[ks][1] = ld_o(rs, ob1, P3_KS(10 + ks) * 1024); }
            if (ks == 8) {
                g = __uint_as_float(__builtin_amdgcn_raw_buffer_load_b32(rs, WS_GATE + t * 4, 0, 16));
#pragma unroll
                for (int i = 0; i < 2; ++i) { y0[i] = ld_dev(rs, WS_Y + (t * DM + col + 32 * i) * 2); y1[i] = ld_dev(rs, WS_Y + 4 * MB + (t * DM + col + 32 * i) * 2); }
            }
            SBAR();
        }
#pragma unroll
        for (int n = 0; n < 4; ++n) *(LAS f32x4*)(smem + wid * 4096 + n * 1024 + lane * 16) = acp[n];
        LGKM_BAR();
#pragma unroll
        for (int n = 0; n < 4; ++n) acc[n] += *(const LAS f32x4*)(smem + (wid ^ 1) * 4096 + n * 1024 + lane * 16);
        const float g1 = 1.f - g;
#pragma unroll
        for (int i = 0; i < 2; ++i) {
            float v[8];
#pragma unroll
            for (int j = 0; j < 4; ++j) {
                auto r = __builtin_amdgcn_permlane16_swap(__float_as_uint(acc[2 * i][j]), __float_as_uint(acc[2 * i + 1][j]), false, false);
                v[j] = __uint_as_float(r[0]); v[4 + j] = __uint_as_float(r[1]);
            }
            u32x4 w;
#pragma unroll
            for (int j = 0; j < 4; ++j)
                w[j] = cvtpk(g * (bflo(y0[i][j]) - bflo(y1[i][j])) + g1 * v[2 * j], g * (bfhi(y0[i][j]) - bfhi(y1[i][j])) + g1 * v[2 * j + 1]);
            *(u32x4*)(out + (size_t)t * DM + col + 32 * i) = w;
        }
        if (wid == 0) { while (__builtin_amdgcn_s_memrealtime() - dg0 < SPIN_TO) __builtin_amdgcn_s_sleep(1); }
        if (wid == 7) STAMP(1, 5);
    }
}

DI void side_waves(LAS char* smem, int lw, int lane, int mt, int hd, const u16* __restrict__ x, const u16* __restrict__ wsr, const u16* __restrict__ wsi,
                   const u16* __restrict__ wdr, const u16* __restrict__ wdi, const u16* __restrict__ wqkv, const u16* __restrict__ wo,
                   const u16* __restrict__ gw, char* ws, rsrc_t rs) {
    const unsigned lds0 = (unsigned)(size_t)smem + lw * 1024;
    const unsigned rowl = 8 * lw + (lane >> 3), chk = ((lane & 7) ^ ((4 * lw + (lane >> 4)) & 7)) * 16;
    const int c = hd >> 4;
    const char* wb = (const char*)(c ? wsi : wsr) + (size_t)mt * 256 * 4096;
    unsigned vo[10];
    {
        const char* xb = (const char*)x + (size_t)mt * 256 * 2048;
        const char* wdb = (const char*)(c ? wdi : wdr) + (size_t)(hd & 15) * 64 * 2048;
        const char* wqb = (const char*)wqkv + hd * 64 + lw * 16 * 6144;
#pragma unroll
        for (int i = 0; i < 8; ++i) vo[i] = (rowl + 32 * i) * 2048 + chk;
        const unsigned voffW = (lane >> 2) * 6144 + (((lane & 3) ^ ((lane >> 5) << 1)) * 16);
#define K1_DMA(t, slot) do { const char* xa_ = xb + (size_t)(t) * 128; const char* wd_ = wdb + (size_t)(t) * 128; const char* wq_ = wqb + (size_t)(t) * 64 * 6144; \
        _Pragma("unroll") for (int u_ = 0; u_ < 3; ++u_) dma(voffW, wq_ + u_ * 2048, lds0 + (slot) + K1_WQ + u_ * 4096); \
        _Pragma("unroll") for (int i_ = 0; i_ < 2; ++i_) dma(vo[i_], wd_, lds0 + (slot) + K1_WD + i_ * 4096); \
        _Pragma("unroll") for (int i_ = 0; i_ < 8; ++i_) dma(vo[i_], xa_, lds0 + (slot) + K1_A + i_ * 4096); } while (0)
        K1_DMA(0, 0); K1_DMA(1, K1_STAGE);
        WAIT_BAR(13);
        K1_DMA(2, 2 * K1_STAGE);
        int sl = 0;
        for (int t = 0; t < 13; ++t) {
            WAIT_BAR(13);
            K1_DMA(t + 3, sl);
            sl = (sl == 2 * K1_STAGE) ? 0 : sl + K1_STAGE;
        }
        WAIT_BAR(13);
        WAIT_BAR(0);
#pragma unroll
        for (int i = 0; i < 8; ++i) vo[i] = (rowl + 32 * i) * 4096 + chk;
        vo[8] = lane * 16 + lw * 1024; vo[9] = vo[8] + 4096;
#pragma unroll
        for (int i = 0; i < 7; ++i) dma(vo[i], wb + (size_t)((4 * mt) & 31) * 128, lds0 + FW_TOP + i * 4096);
#pragma unroll
        for (int i = 0; i < 8; ++i) dma(vo[i], wb + (size_t)((4 * mt + 1) & 31) * 128, lds0 + FW_TOP - FW_STAGE + i * 4096);
        LGKM_BAR();
        dma(vo[7], wb + (size_t)((4 * mt) & 31) * 128, lds0 + FW_TOP + 7 * 4096);
        WAIT_BAR(0);
    }
    {
        const char* kvb = ws + WS_KV + hd * 262144;
        const unsigned vl = lane * 16 + lw * 1024;
        const char *bW = wb, *bA = ws + WS_AB + hd * 262144; int kmask = 31, kstep = 128, kstepA = 8192, koff = 4 * mt;
#define FN_DMA_P(p, s, fslot, aslot) do { \
        if ((p) < 8) dma(vo[p], bW + (size_t)((koff + (s)) & kmask) * kstep, lds0 + (fslot) + (p) * 4096); \
        else if ((p) < 10) dma_dev(vo[p], bA + (size_t)((koff + (s)) & kmask) * kstepA, lds0 + FB_BASE + (aslot) + ((p) - 8) * 4096); \
        else if ((p) == 10) dma_dev(vl, kvb + ((4 * mt + (s)) & 31) * 8192, lds0 + (aslot)); \
        else dma_dev(vl, kvb + ((4 * mt + (s) - ((((s) & 3) == 0 && lw < 2) ? 4 : 0)) & 31) * 8192 + 4096, lds0 + (aslot) + 4096); } while (0)
        const int fr = lane & 15, fq = lane >> 4;
        const int cpart = ((fq ^ (fr >> 1)) & 7) * 16;
        const int aW = (64 * lw + fr) * 128 + cpart, aA = FB_BASE + fr * 128 + cpart;
        f32x4 acc[4][4];
#pragma unroll
        for (int m = 0; m < 4; ++m)
#pragma unroll
            for (int n = 0; n < 4; ++n) acc[m][n] = f32x4{};
        FnFrag X, Yf;
#define FN_RD(F, i, slot, bslot, ks) do { \
        if ((i) == 0) F.w[0] = ldsr128(smem + (slot) + (aW ^ ((ks) * 64))); \
        else if ((i) < 5) F.a[(i) - 1] = ldsr128(smem + (bslot) + (aA ^ ((ks) * 64)) + ((i) - 1) * 2048); \
        else F.w[(i) - 4] = ldsr128(smem + (slot) + (aW ^ ((ks) * 64)) + ((i) - 4) * 2048); } while (0)
#define FN_MF(F, j) do { if (!ABL_FMFMA) acc[(j) >> 2][(j) & 3] = MFMA16(F.a[(j) & 3], F.w[(j) >> 2], acc[(j) >> 2][(j) & 3]); else asm volatile("" :: "v"(F.a[(j) & 3]), "v"(F.w[(j) >> 2])); } while (0)
#define FN_G(g, C, R, rslot, rb, ks, P0, s, fslot, aslot) do { FN_MF(C, 2 * (g)); FN_RD(R, g, rslot, rb, ks); FN_MF(C, 2 * (g) + 1); \
        if (!ABL_FDMA && ((g) & 3) != 0 && (P0) + (g) - 1 - ((g) >> 2) < PMAX) FN_DMA_P((P0) + (g) - 1 - ((g) >> 2), s, fslot, aslot); SBAR(); FN_PAD(g); } while (0)
#define FN_PHASE(C, R, rslot, rb, ks, P0, s, fslot, aslot) do { FN_G(0, C, R, rslot, rb, ks, P0, s, fslot, aslot); FN_G(1, C, R, rslot, rb, ks, P0, s, fslot, aslot); \
        FN_G(2, C, R, rslot, rb, ks, P0, s, fslot, aslot); FN_G(3, C, R, rslot, rb, ks, P0, s, fslot, aslot); FN_G(4, C, R, rslot, rb, ks, P0, s, fslot, aslot); \
        FN_G(5, C, R, rslot, rb, ks, P0, s, fslot, aslot); FN_G(6, C, R, rslot, rb, ks, P0, s, fslot, aslot); FN_G(7, C, R, rslot, rb, ks, P0, s, fslot, aslot); } while (0)
#define PMAX 12
        __builtin_amdgcn_s_setprio(FN_PRIO);
#pragma unroll
        for (int i = 0; i < 8; ++i) FN_RD(X, i, FW_TOP, 0, 0);
        LGKM_BAR();
        int f0 = FW_TOP, f1 = FW_TOP - FW_STAGE, f2 = FW_TOP - 2 * FW_STAGE;
        FN_DMA_P(0, 2, f2, 0); FN_DMA_P(1, 2, f2, 0); FN_DMA_P(2, 2, f2, 0); FN_DMA_P(3, 2, f2, 0); FN_DMA_P(4, 2, f2, 0); FN_DMA_P(5, 2, f2, 0);
        SBAR();
#define FN_ITER(t, WAITN) do { \
            FN_PHASE(X, Yf, f0, ((t) & 3) * 8192, 1, 6, (t) + 2, f2, (((t) + 2) & 3) * 8192);       \
            WAIT_BAR(WAITN); SBAR();                                                                \
            FN_SWITCH(t); \
            FN_PHASE(Yf, X, f1, (((t) + 1) & 3) * 8192, 0, 0, (t) + 3, f0, 0);                      \
            const int ft = f0; f0 = f1; f1 = f2; f2 = ft; } while (0)
#define FN_SWITCH(t)
#undef PMAX
#define PMAX 8
        FN_ITER(0, 8); FN_ITER(1, 8);
#undef PMAX
#define PMAX 12
#undef FN_SWITCH
#define FN_SWITCH(t) do { if (__builtin_expect((t) == 29, 0)) { bA = (const char*)wo + ((blockIdx.x >> 3) & 15) * 128; bW = bA + 256 * 2048; kmask = 3; kstep = 512 * 2048; kstepA = 64 * 2048; koff = 0; \
                const unsigned cw = ((lane & 7) ^ ((((lane >> 4) & 1) | ((lw & 1) << 1)) << 1)) * 16; \
                _Pragma("unroll") for (int i = 0; i < 10; ++i) vo[i] = (rowl + 32 * (i & 7)) * 2048 + cw; } SBAR(); } while (0)
        for (int t = 2; t < 31; ++t) FN_ITER(t, 12);
#undef PMAX
#define PMAX 10
        FN_PHASE(X, Yf, f0, 3 * 8192, 1, 6, 33, f2, 8192);
        WAIT_BAR(10); SBAR();
#pragma unroll
        for (int i = 0; i < 8; ++i) dma(vo[i], bA + (size_t)512 * 2048, lds0 + FW_TOP - FW_STAGE + i * 4096);
#pragma unroll
        for (int i = 0; i < 4; ++i) dma(vo[i], bA + (size_t)128 * 2048, lds0 + FB_BASE + 2 * 8192 + i * 4096);
#pragma unroll
        for (int j = 0; j < 16; ++j) FN_MF(Yf, j);
        const unsigned yb = WS_Y + c * 4 * MB + ((mt * 256 + 64 * lw + fr) * DM + (hd & 15) * 64 + 16 * (fq & 1) + 8 * (fq >> 1)) * 2;
#pragma unroll
        for (int m = 0; m < 4; ++m)
#pragma unroll
            for (int i = 0; i < 2; ++i) st_wt(rs, yb + (m * 16 * DM + 32 * i) * 2, swap16(pack4(acc[m][2 * i]), pack4(acc[m][2 * i + 1])));
        WAIT_BAR(0);
    }
}

__global__ __launch_bounds__(768, 3) void fused_kernel(const u16* __restrict__ x, const u16* __restrict__ wsr, const u16* __restrict__ wsi,
                                                        const u16* __restrict__ wdr, const u16* __restrict__ wdi, const u16* __restrict__ wqkv,
                                                        const u16* __restrict__ wo, const u16* __restrict__ gw, u16* __restrict__ out, char* ws) {
    __shared__ __attribute__((aligned(1024))) char smem_[163840];
    LAS char* smem = (LAS char*)smem_;
    const int tid = threadIdx.x, lane = tid & 63, wid = __builtin_amdgcn_readfirstlane(tid >> 6);
    const int xcd = blockIdx.x & 7, idx = blockIdx.x >> 3;
    const int hd = xcd * 4 + (idx >> 3), mt = idx & 7;
    const rsrc_t rs = __builtin_amdgcn_make_buffer_rsrc(ws, 0, WS_END, 0x00020000);
    unsigned long long dg0 = __builtin_amdgcn_s_memrealtime();
    if (wid < 8) {
        const char* wdb = (const char*)((hd >> 4) ? wdi : wdr) + (size_t)(hd & 15) * 64 * 2048;
        const char* wsb = (const char*)((hd >> 4) ? wsi : wsr) + (size_t)mt * 256 * 4096;
        const char* pf2 = (const char*)wo + (size_t)blockIdx.x * 8192;
        main_waves(dg0, smem, wid, lane, mt, hd, ws, rs, out, x, gw, wdb, (const char*)wqkv, wsb, pf2);
    }
    else side_waves(smem, wid - 8, lane, mt, hd, x, wsr, wsi, wdr, wdi, wqkv, wo, gw, ws, rs);
}

extern "C" void kernel_launch(void* const* d_in, const int* in_sizes, int n_in, void* d_out, int out_size, void* d_ws, size_t ws_size, hipStream_t stream) {
    const u16* x = (const u16*)d_in[0]; const u16* wsr = (const u16*)d_in[1]; const u16* wsi = (const u16*)d_in[2];
    const u16* wdr = (const u16*)d_in[3]; const u16* wdi = (const u16*)d_in[4]; const u16* wqkv = (const u16*)d_in[6];
    const u16* wo = (const u16*)d_in[7]; const u16* gw = (const u16*)d_in[8];
    fused_kernel<<<256, 768, 0, stream>>>(x, wsr, wsi, wdr, wdi, wqkv, wo, gw, (u16*)d_out, (char*)d_ws);
}
```

```cpp
#include <hip/hip_runtime.h>
#include <hip/hip_bf16.h>

#define O_AUX 16
#define O_ACQ 0
typedef unsigned short u16;
typedef short bf16x8 __attribute__((ext_vector_type(8)));
typedef short s16x4 __attribute__((ext_vector_type(4)));
typedef float f32x2 __attribute__((ext_vector_type(2)));
typedef float f32x4 __attribute__((ext_vector_type(4)));
typedef float f32x16 __attribute__((ext_vector_type(16)));
typedef unsigned u32x4 __attribute__((ext_vector_type(4)));
typedef unsigned u32x2 __attribute__((ext_vector_type(2)));
typedef __bf16 bf16x2_t __attribute__((ext_vector_type(2)));
#define LAS __attribute__((address_space(3)))
#define GAS __attribute__((address_space(1)))
#define DI __device__ __forceinline__
#define SBAR() __builtin_amdgcn_sched_barrier(0)
#define PIN(x) asm volatile("" : "+v"(x))
#define MFMA16(a, b, c) __builtin_amdgcn_mfma_f32_16x16x32_bf16(a, b, c, 0, 0, 0)
#define MFMA32(a, b, c) __builtin_amdgcn_mfma_f32_32x32x16_bf16(a, b, c, 0, 0, 0)
#define WAIT_BAR(N) asm volatile("s_waitcnt vmcnt(" #N ") lgkmcnt(0)\n\ts_barrier" ::: "memory")
#define LGKM_BAR() asm volatile("s_waitcnt lgkmcnt(0)\n\ts_barrier" ::: "memory")
#define RLX_AGENT __ATOMIC_RELAXED, __HIP_MEMORY_SCOPE_AGENT
#define BC(x) __builtin_bit_cast(bf16x8, x)

constexpr int SEQ = 2048, DM = 1024;
constexpr unsigned MB = 1u << 20;
constexpr unsigned WS_F1 = 4096, WS_F2 = 4096 + 16384, WS_GATE = 65536, WS_KV = 1 * MB, WS_AB = 9 * MB, WS_O = 17 * MB, WS_Y = 21 * MB, WS_END = 29 * MB;
typedef unsigned long long u64;

#define STAMP(kid, i)
#define TICK(k, i)

DI unsigned cvtpk(float lo, float hi) { f32x2 v = {lo, hi}; bf16x2_t b = __builtin_convertvector(v, bf16x2_t); return __builtin_bit_cast(unsigned, b); }
DI void dma(unsigned voff, const char* sbase, unsigned l) {
    asm volatile("s_mov_b32 m0, %2\n\ts_nop 0\n\tglobal_load_lds_dwordx4 %0, %1" :: "v"(voff), "s"(sbase), "s"(l) : "memory"); }
DI void dma_dev(unsigned voff, const char* sbase, unsigned l) {
    asm volatile("s_mov_b32 m0, %2\n\ts_nop 0\n\tglobal_load_lds_dwordx4 %0, %1 sc1" :: "v"(voff), "s"(sbase), "s"(l) : "memory"); }
DI void ldsw128(LAS char* p, u32x4 v) { *(LAS u32x4*)p = v; }
DI bf16x8 ldsr128(const LAS char* p) { return *(const LAS bf16x8*)p; }
DI bf16x8 ldstr(const LAS char* p, int off2) {
    s16x4 a = __builtin_amdgcn_ds_read_tr16_b64_v4i16((LAS s16x4*)p);
    s16x4 b = __builtin_amdgcn_ds_read_tr16_b64_v4i16((LAS s16x4*)(p + off2));
    return (bf16x8){a[0], a[1], a[2], a[3], b[0], b[1], b[2], b[3]};
}
DI int lane_now() { int l; asm volatile("v_mbcnt_lo_u32_b32 %0, -1, 0\n\tv_mbcnt_hi_u32_b32 %0, -1, %0" : "=v"(l)); return l; }
DI float bflo(unsigned u) { return __uint_as_float(u << 16); }
DI float bfhi(unsigned u) { return __uint_as_float(u & 0xffff0000u); }
DI u32x4 swap16(u32x2 a, u32x2 b) {
    auto r0 = __builtin_amdgcn_permlane16_swap(a[0], b[0], false, false);
    auto r1 = __builtin_amdgcn_permlane16_swap(a[1], b[1], false, false);
    return (u32x4){r0[0], r1[0], r0[1], r1[1]};
}
DI u32x2 pack4(f32x4 v) { return (u32x2){cvtpk(v[0], v[1]), cvtpk(v[2], v[3])}; }
typedef __amdgpu_buffer_rsrc_t rsrc_t;
DI void st_wt(rsrc_t r, unsigned off, u32x4 v) { __builtin_amdgcn_raw_buffer_store_b128(v, r, off, 0, 16); }
DI u32x4 ld_dev(rsrc_t r, unsigned off) { return __builtin_amdgcn_raw_buffer_load_b128(r, off, 0, 16); }
DI u32x4 ld_o(rsrc_t r, unsigned off, unsigned soff) { return __builtin_amdgcn_raw_buffer_load_b128(r, off, soff, 16); }
DI void wait_flags(const char* first, int n, u64 lo, u64 span, int lane) {
    const GAS u64* p = (const GAS u64*)(first + (lane < n ? lane : 0) * 64);
#pragma unroll 1
    for (unsigned spins = 0; spins < (1u << 16); ++spins) {
        const u64 v = __hip_atomic_load(p, RLX_AGENT);
        if (__all(v - lo <= span)) break;
        __builtin_amdgcn_s_sleep(1);
    }
}
DI void set_flag(char* p, u64 v, int lane) { if (lane == 0) __hip_atomic_store((GAS u64*)p, v, RLX_AGENT); }

#define ABL_FDMA 0
#define ABL_FMFMA 0
#define ABL_ATT 0
#define PADN 0
#define FN_PAD(g) do { _Pragma("unroll") for (int z_ = 0; z_ < PADN; ++z_) asm volatile("s_nop 3"); SBAR(); } while (0)
#define FN_PRIO 1
#define PF_MODE 0
#define TLB_WARM 0
#define TLB_SUB 16384
#define PF_PRE 1
#define PF_N0 3
#define PF_LEAD 8
#define PF_SLEEP 12
constexpr int K1_STAGE = 53248, K1_A = 0, K1_WD = 32768, K1_WQ = 40960;
constexpr int AT_STAGE = 8192, FB_BASE = 32768, FW_STAGE = 32768, FW_TOP = 131072;
struct K1Frag { bf16x8 x[4], wd[2], wqk[2], wv; };
struct FnFrag { bf16x8 w[4], a[4]; };

DI void attn_store(rsrc_t rs, unsigned off, const f32x16& o, float l) {
    const float rl = 1.0f / l; u32x4 w0, w1;
#pragma unroll
    for (int i = 0; i < 4; ++i) { w0[i] = cvtpk(o[2 * i] * rl, o[2 * i + 1] * rl); w1[i] = cvtpk(o[8 + 2 * i] * rl, o[9 + 2 * i] * rl); }
    st_wt(rs, off, w0); st_wt(rs, off + 256, w1);
}
DI bf16x8 attn_sel(int lane) {
    const short v = (((lane >> 2) & 1) == ((lane >> 4) & 1)) ? (short)0x3f80 : (short)0;
    return (bf16x8){v, v, v, v, v, v, v, v};
}
DI float max16(const f32x16& s) {
    float a = fmaxf(fmaxf(s[0], s[1]), s[2]), b = fmaxf(fmaxf(s[3], s[4]), s[5]);
#pragma unroll
    for (int r = 6; r < 16; r += 4) { a = fmaxf(fmaxf(a, s[r]), s[r + 1]); if (r + 3 < 16) b = fmaxf(fmaxf(b, s[r + 2]), s[r + 3]); }
    return fmaxf(a, b);
}
DI void attn_slow(rsrc_t rs, const char* kv, bf16x8 q0, bf16x8 q1, int lane, unsigned ooff) {
    __builtin_amdgcn_fence(__ATOMIC_ACQUIRE, "agent");
    const bf16x8 sel = attn_sel(lane);
    kv += lane * 16;
    float mx = -INFINITY;
    const f32x16 z = f32x16{};
#pragma unroll 1
    for (int j = 0; j < 64; ++j) {
        const char* kp = kv + (j >> 1) * 8192 + (j & 1) * 2048;
        f32x16 s = MFMA32(*(const bf16x8*)kp, q0, z); s = MFMA32(*(const bf16x8*)(kp + 1024), q1, s);
        mx = fmaxf(mx, max16(s));
    }
    mx = fmaxf(mx, __shfl_xor(mx, 32));
    f32x16 negm, o = f32x16{}; f32x4 l = f32x4{};
#pragma unroll
    for (int r = 0; r < 16; ++r) negm[r] = -mx;
#pragma unroll 1
    for (int j = 0; j < 64; ++j) {
        const char* kp = kv + (j >> 1) * 8192 + (j & 1) * 2048;
        const char* vp = kv + ((j & 1) ? (((j & 7) == 7 ? (j >> 3) * 4 : (j + 1) >> 1) * 8192 + 4096) : ((j >> 1) * 8192 + 6144));
        const bf16x8 v0 = *(const bf16x8*)vp, v1 = *(const bf16x8*)(vp + 1024);
        f32x16 s = MFMA32(*(const bf16x8*)kp, q0, negm); s = MFMA32(*(const bf16x8*)(kp + 1024), q1, s);
        u32x4 p0, p1;
#pragma unroll
        for (int r = 0; r < 16; ++r) s[r] = __builtin_amdgcn_exp2f(s[r]);
#pragma unroll
        for (int r = 0; r < 4; ++r) { p0[r] = cvtpk(s[2 * r], s[2 * r + 1]); p1[r] = cvtpk(s[8 + 2 * r], s[9 + 2 * r]); }
        o = MFMA32(v0, BC(p0), o); l = MFMA16(sel, BC(p0), l);
        o = MFMA32(v1, BC(p1), o); l = MFMA16(sel, BC(p1), l);
    }
    attn_store(rs, ooff, o, l[0]);
}

DI void touch8k(const char* p, unsigned voff, unsigned dump) {
    asm volatile("s_mov_b32 m0, %2\n\ts_nop 0\n\tglobal_load_lds_dword %0, %1" :: "v"(voff), "s"(p), "s"(dump) : "memory"); }
DI void touch_pair(int p, int l, int mt, int hd, const char* x, const char* wdb, const char* wqkv, unsigned dump) {
    const int n = l >= 56 ? l - 8 : l, i = (mt * 2 + (hd & 1)) * 24 + (n - 32);
    const char* a = n < 16 ? x + (size_t)(mt * 256 + hd * 8 + (n >> 1)) * 2048 + p * 256 + (n & 1) * 128
                  : n < 32 ? wdb + (size_t)(mt * 8 + ((n - 16) >> 1)) * 2048 + p * 256 + (n & 1) * 128
                           : wqkv + (size_t)(p * 128 + i / 3) * 6144 + (i % 3) * 2048 + (hd >> 1) * 128;
    asm volatile("s_mov_b32 m0, %1\n\ts_nop 0\n\tglobal_load_lds_dword %0, off" :: "v"(a), "s"(dump) : "memory");
}
DI void main_waves(LAS char* smem, int wid, int lane, int mt, int hd, char* ws, rsrc_t rs, u16* __restrict__ out,
                   const u16* __restrict__ x, const u16* __restrict__ gw, const char* wdb, const char* wqkv, const char* wsb, const char* pf2) {
    const unsigned dump = (unsigned)(size_t)smem + 3 * K1_STAGE + wid * 256;
    if (wid >= 1 && wid <= PF_PRE) { __builtin_amdgcn_s_sleep(PF_SLEEP); touch_pair(wid, lane, mt, hd, (const char*)x, wdb, wqkv, dump); }
    const int t_act = wid < 4 ? 7 + 2 * wid : 2 * wid - PF_LEAD;
#define TW(l_) (unsigned)(((hd & 15) * 16 + ((l_) >> 2)) * 4096 + ((l_) & 3) * 128)
    int fr = lane & 15, fq = lane >> 4;
    u64 ep, base; u32x4 gx0, gx1, gw0, gw1;
    {
        const int wm = wid >> 1, wn = wid & 1;
        const int cpart = ((fq ^ (fr >> 1)) & 7) * 16;
        const int aA = K1_A + (64 * wm + fr) * 128 + cpart, aD = K1_WD + (32 * wn + fr) * 128 + cpart;
        const int aT0 = (8 * fq + (fr >> 2)) * 64 + ((((lane & 3) >> 1) ^ ((fq & 1) << 1)) * 16) + (lane & 1) * 8;
        const int aQ = K1_WQ + wn * 4096 + aT0, aV = K1_WQ + 8192 + (aT0 ^ (wn * 32));
        f32x4 accQ[2][4], accV[4], accD[2][4];
#pragma unroll
        for (int m = 0; m < 4; ++m) { accQ[0][m] = f32x4{}; accQ[1][m] = f32x4{}; accV[m] = f32x4{}; accD[0][m] = f32x4{}; accD[1][m] = f32x4{}; }
        K1Frag X, Y;
#define K1_RD(F, i, slot, ks) do { const LAS char* sb_ = smem + (slot); \
        if ((i) == 0) F.wqk[0] = ldstr(sb_ + aQ + (ks) * 2048, 256); \
        else if ((i) == 1) { F.x[0] = ldsr128(sb_ + (aA ^ ((ks) * 64))); } \
        else if ((i) == 2) F.wqk[1] = ldstr(sb_ + (aQ ^ 32) + (ks) * 2048, 256); \
        else if ((i) == 3) F.wv = ldstr(sb_ + aV + (ks) * 2048, 256); \
        else if ((i) == 4) { F.wd[0] = ldsr128(sb_ + (aD ^ ((ks) * 64))); F.wd[1] = ldsr128(sb_ + (aD ^ ((ks) * 64)) + 2048); } \
        else if ((i) < 8) F.x[(i) - 4] = ldsr128(sb_ + (aA ^ ((ks) * 64)) + ((i) - 4) * 2048); } while (0)
#define K1_MF(F, j) do { constexpr int m_ = (j) / 5, r_ = (j) % 5; \
        if (r_ == 0) accQ[0][m_] = MFMA16(F.wqk[0], F.x[m_], accQ[0][m_]); \
        else if (r_ == 1) accQ[1][m_] = MFMA16(F.wqk[1], F.x[m_], accQ[1][m_]); \
        else if (r_ == 2) accV[m_] = MFMA16(F.x[m_], F.wv, accV[m_]); \
        else if (r_ == 3) accD[0][m_] = MFMA16(F.x[m_], F.wd[0], accD[0][m_]); \
        else accD[1][m_] = MFMA16(F.x[m_], F.wd[1], accD[1][m_]); } while (0)
#define K1_G(g, C, R, rslot, ks) do { K1_MF(C, 2 * (g)); if ((g) == 0) SBAR(); K1_RD(R, g, rslot, ks); K1_MF(C, 2 * (g) + 1); SBAR(); } while (0)
#define K1_PHASE(C, R, rslot, ks) do { K1_G(0, C, R, rslot, ks); K1_G(1, C, R, rslot, ks); K1_G(2, C, R, rslot, ks); K1_G(3, C, R, rslot, ks); K1_G(4, C, R, rslot, ks); \
        K1_G(5, C, R, rslot, ks); K1_G(6, C, R, rslot, ks); K1_G(7, C, R, rslot, ks); K1_G(8, C, R, rslot, ks); K1_G(9, C, R, rslot, ks); } while (0)
        asm volatile("" :: "s"(gw), "s"(out));
        __builtin_amdgcn_s_waitcnt(0xC07F);
        LGKM_BAR();
        if (wid == 0) STAMP(0, 1);
        if (wid > PF_PRE && wid <= 3) touch_pair(wid, lane, mt, hd, (const char*)x, wdb, wqkv, dump);
#pragma unroll
        for (int i = 0; i < 8; ++i) K1_RD(X, i, 0, 0);
        int sl = 0, sn = K1_STAGE;
        SBAR();
        for (int t = 0; t < 15; ++t) {
            K1_PHASE(X, Y, sl, 1);
            LGKM_BAR(); SBAR();
            TICK(0, t);
            if (__builtin_expect(t == t_act, 0)) {
                const int l_ = lane_now();
                if (wid < 4) touch8k(wsb + ((mt + wid) & 7) * 512, TW(l_), dump);
                else touch_pair(wid, l_, mt, hd, (const char*)x, wdb, wqkv, dump);
            }
            SBAR();
            K1_PHASE(Y, X, sn, 0);
            sl = sn; sn = (sn == 2 * K1_STAGE) ? 0 : sn + K1_STAGE;
        }
        K1_PHASE(X, Y, 0, 1);
        WAIT_BAR(0); SBAR();
        lane = lane_now();
        ep = __hip_atomic_load((const GAS u64*)ws, RLX_AGENT);
        { const u32x4* px = (const u32x4*)(x + (size_t)(256 * mt + 8 * hd + wid) * DM + lane * 16); const u32x4* pw = (const u32x4*)(gw + lane * 16);
          gx0 = px[0]; gx1 = px[1]; gw0 = pw[0]; gw1 = pw[1]; }
        SBAR();
        K1_MF(Y, 0); K1_MF(Y, 1); K1_MF(Y, 2); K1_MF(Y, 3); K1_MF(Y, 4); K1_MF(Y, 5); K1_MF(Y, 6); K1_MF(Y, 7); K1_MF(Y, 8); K1_MF(Y, 9);
        K1_MF(Y, 10); K1_MF(Y, 11); K1_MF(Y, 12); K1_MF(Y, 13); K1_MF(Y, 14); K1_MF(Y, 15); K1_MF(Y, 16); K1_MF(Y, 17); K1_MF(Y, 18); K1_MF(Y, 19);
        if (wid == 0) STAMP(0, 2);
        fr = lane & 15; fq = lane >> 4;
        const float qs = wn ? 1.0f : 0.17677669529663687f * 1.4426950408889634f;
#pragma unroll
        for (int m = 0; m < 4; ++m) {
            const u32x4 w = swap16(pack4(accQ[0][m] * qs), pack4(accQ[1][m] * qs));
            const int r = (16 * m + fr) & 31, rho = (r & 19) | ((r & 4) << 1) | ((r & 8) >> 1), j = 2 * wm + (m >> 1);
            const int in = (fq & 1) * 1024 + ((fq >> 1) * 32 + rho) * 16;
            ldsw128(smem + (wn ? wm * AT_STAGE + (m >> 1) * 2048 : FW_TOP - 2 * FW_STAGE + j * 2048) + in, w);
        }
        const int rv = 8 * (fr >> 2) + 4 * wn + (fr & 3);
#pragma unroll
        for (int i = 0; i < 2; ++i) {
            const int in = (fq & 1) * 1024 + ((fq >> 1) * 32 + rv) * 16;
            const u32x4 w = swap16(pack4(accV[2 * i]), pack4(accV[2 * i + 1]));
            ldsw128(smem + ((wm + i) & 3) * AT_STAGE + 6144 - i * 2048 + in, w);
#pragma unroll
            for (int n = 0; n < 2; ++n) {
                const u32x4 d = swap16(pack4(accD[n][2 * i]), pack4(accD[n][2 * i + 1]));
                const int el = 32 * wn + 16 * n + fr, ch = 4 * i + 2 * (fq & 1) + (fq >> 1);
                ldsw128(smem + FB_BASE + wm * 8192 + el * 128 + ((ch ^ (fr >> 1)) * 16), d);
            }
        }
        LGKM_BAR();
        if (wid == 0) STAMP(0, 3);
    }
    {
        const int qt = mt * 8 + wid;
        char* f1 = ws + WS_F1 + hd * 512;
        const unsigned rec0 = (hd * 32 + 4 * mt) * 8192 + wid * 1024 + lane * 16;
#define PUBLISH(q) do { const u32x4 a_ = *(const LAS u32x4*)(smem + (q) * 8192 + wid * 1024 + lane * 16), b_ = *(const LAS u32x4*)(smem + FB_BASE + (q) * 8192 + wid * 1024 + lane * 16); \
        st_wt(rs, WS_KV + rec0 + (q) * 8192, a_); st_wt(rs, WS_AB + rec0 + (q) * 8192, b_); } while (0)
        PUBLISH(0); PUBLISH(1);
        const bf16x8 sel = attn_sel(lane);
        const LAS char* fb = smem + lane * 16;
        const bf16x8 q0 = ldsr128(fb + FW_TOP - 2 * FW_STAGE + wid * 2048), q1 = ldsr128(fb + FW_TOP - 2 * FW_STAGE + wid * 2048 + 1024);
        bf16x8 kA0, kA1, kB0, kB1, vA0, vA1, vB0, vB1 = bf16x8{};
        f32x16 negm, o = f32x16{}, sA, sB; f32x4 l = f32x4{};
        u32x4 p0, p1 = u32x4{};
        kA0 = ldsr128(fb); kA1 = ldsr128(fb + 1024); kB0 = ldsr128(fb + 2048); kB1 = ldsr128(fb + 3072); vA0 = ldsr128(fb + 6144); vA1 = ldsr128(fb + 7168);
        SBAR(); LGKM_BAR(); SBAR();
        {
            sA = MFMA32(kA0, q0, o); sA = MFMA32(kA1, q1, sA);
            float mx = max16(sA); mx = fmaxf(mx, __shfl_xor(mx, 32)) + 40.f;
#pragma unroll
            for (int r = 0; r < 16; ++r) { negm[r] = -mx; sA[r] -= mx; }
        }
        {
            float s = 0.f;
#pragma unroll
            for (int i = 0; i < 4; ++i) {
                s = fmaf(bflo(gx0[i]), bflo(gw0[i]), s); s = fmaf(bfhi(gx0[i]), bfhi(gw0[i]), s);
                s = fmaf(bflo(gx1[i]), bflo(gw1[i]), s); s = fmaf(bfhi(gx1[i]), bfhi(gw1[i]), s);
            }
#pragma unroll
            for (int o = 32; o >= 1; o >>= 1) s += __shfl_xor(s, o);
            if (lane == 0) __builtin_amdgcn_raw_buffer_store_b32(__float_as_uint(1.f / (1.f + __expf(-s))), rs, WS_GATE + (256 * mt + 8 * hd + wid) * 4, 0, 16);
        }
        base = (((u64)(unsigned)__builtin_amdgcn_readfirstlane((unsigned)(ep >> 32)) << 32) | (unsigned)__builtin_amdgcn_readfirstlane((unsigned)ep)) + 8;
        if (wid == 0) STAMP(1, 0);
#define EX(S_, i) S_[i] = __builtin_amdgcn_exp2f(S_[i])
#define AT_STEP(SC, SN, K0, K1, VC0, VP1, RD0, RD1, RD2, RD3) do { \
        SN = MFMA32(K0, q0, negm); SBAR(); RD0; EX(SC, 0); EX(SC, 1); EX(SC, 2); EX(SC, 3); SBAR(); \
        o = MFMA32(VP1, BC(p1), o); PIN(o); SBAR(); RD1; EX(SC, 4); EX(SC, 5); EX(SC, 6); EX(SC, 7); p0[0] = cvtpk(SC[0], SC[1]); p0[1] = cvtpk(SC[2], SC[3]); SBAR(); \
        l = MFMA16(sel, BC(p1), l); PIN(l); SBAR(); p0[2] = cvtpk(SC[4], SC[5]); p0[3] = cvtpk(SC[6], SC[7]); EX(SC, 8); SBAR(); \
        SN = MFMA32(K1, q1, SN); SBAR(); RD2; EX(SC, 9); EX(SC, 10); EX(SC, 11); EX(SC, 12); SBAR(); \
        o = MFMA32(VC0, BC(p0), o); PIN(o); SBAR(); RD3; EX(SC, 13); EX(SC, 14); EX(SC, 15); p1[0] = cvtpk(SC[8], SC[9]); SBAR(); \
        l = MFMA16(sel, BC(p0), l); PIN(l); SBAR(); p1[1] = cvtpk(SC[10], SC[11]); p1[2] = cvtpk(SC[12], SC[13]); p1[3] = cvtpk(SC[14], SC[15]); SBAR(); } while (0)
        int sl = AT_STAGE;
#define AT_TICK() do { \
            AT_STEP(sA, sB, kB0, kB1, vA0, vB1, kA0 = ldsr128(fb + sl), kA1 = ldsr128(fb + sl + 1024), vB0 = ldsr128(fb + sl + 4096), vB1 = ldsr128(fb + sl + 5120)); \
            AT_STEP(sB, sA, kA0, kA1, vB0, vA1, kB0 = ldsr128(fb + sl + 2048), kB1 = ldsr128(fb + sl + 3072), vA0 = ldsr128(fb + sl + 6144), vA1 = ldsr128(fb + sl + 7168)); \
            sl = (sl + AT_STAGE) & (4 * AT_STAGE - 1); } while (0)
#pragma unroll 1
        for (int T = 1; T <= 2; ++T) {
            asm volatile("s_waitcnt vmcnt(0)" ::: "memory");
            if (wid == 1 && T == 2) wait_flags(f1, 8, base + 1, 1, lane);
            LGKM_BAR(); SBAR();
            TICK(1, T);
            if (wid == 0) set_flag(f1 + mt * 64, base + T, lane);
            if (T == 1) { PUBLISH(2); PUBLISH(3); }
            SBAR();
            AT_TICK();
        }
        LGKM_BAR(); SBAR();
        {
            const u64 fv = __hip_atomic_load((const GAS u64*)(f1 + (lane & 7) * 64), RLX_AGENT);
            SBAR();
            AT_TICK();
            if (wid == 1 && !__all(fv == base + 2)) wait_flags(f1, 8, base + 2, 0, lane);
        }
        const int t_touch = wid >= 4 ? 4 * wid - 8 : 0;
#pragma unroll 1
        for (int T = 4; T <= 32; ++T) {
            LGKM_BAR(); SBAR();
            TICK(1, T);
            if (__builtin_expect(T == t_touch, 0)) {
                asm volatile("global_load_dword v167, %0, %1" :: "v"(TW(lane)), "s"(wsb + ((mt + wid) & 7) * 512) : "memory", "v167");
                if (wid == 7) asm volatile("global_load_dword v167, %0, %1" :: "v"(lane * 128), "s"(pf2) : "memory", "v167");
            }
            SBAR();
            AT_TICK();
        }
        o = MFMA32(vB1, BC(p1), o); l = MFMA16(sel, BC(p1), l);
        if (wid == 0) STAMP(1, 1);
        bool bad = !(l[0] >= 7.8886e-31f && l[0] <= 3.0e38f);
#pragma unroll
        for (int r = 0; r < 16; ++r) bad |= !(fabsf(o[r]) <= 3.0e38f);
        const int r = lane & 31, qrow = qt * 32 + ((r & 19) | ((r & 4) << 1) | ((r & 8) >> 1));
        const unsigned ooff = WS_O + ((qrow >> 4) * 32 + hd) * 1024 + ((lane >> 5) * 32 + (qrow & 15)) * 16;
        if (!ABL_ATT && !ABL_FDMA && __builtin_expect(__any(bad), 0)) attn_slow(rs, ws + WS_KV + hd * 262144, q0, q1, lane, ooff);
        else attn_store(rs, ooff, o, l[0]);
        WAIT_BAR(0);
        if (wid == 0) STAMP(1, 2);
    }
    {
        const int rt = 2 * (blockIdx.x & 7) + (blockIdx.x >> 7), nb = ((blockIdx.x >> 3) & 15) * 64;
        const int kq = fr >> 2, swz = ((kq >> 1) & 1) | ((fq & 1) << 1);
        const int aW = (8 * fq + kq) * 128 + ((((lane & 3) >> 1) ^ (swz << 1)) * 16) + (lane & 1) * 8;
        const int kh = wid & 1;
        const int rot = (blockIdx.x >> 3) & 15;
#define P3_KS(ks_) (((ks_) + rot) & 15)
#define P3_RD(W_, ks_) do { const int wb_ = FB_BASE + (16 * kh + P3_KS(ks_)) * 4096; \
            _Pragma("unroll") for (int n_ = 0; n_ < 4; ++n_) W_[n_] = ldstr(smem + wb_ + (aW ^ (n_ * 32)), 512); } while (0)
        bf16x8 wf[2][4];
        P3_RD(wf[0], 0);
        if (wid == 0) {
            set_flag(ws + WS_F2 + (mt * 32 + hd) * 64, base + 5, lane);
            wait_flags(ws + WS_F2 + (rt >> 1) * 2048, 32, base + 5, 0, lane);
            __builtin_amdgcn_fence(__ATOMIC_ACQUIRE, "wavefront");
            if (blockIdx.x == 0) set_flag(ws, base, lane);
            STAMP(1, 3);
        }
        LGKM_BAR();
        const int R0 = rt * 128 + 16 * wid;
        const int t = R0 + fr, col = nb + 16 * (fq & 1) + 8 * (fq >> 1);
        const unsigned ob0 = WS_O + ((R0 >> 4) * 32 + 16 * kh) * 1024 + lane * 16, ob1 = WS_O + (((R0 >> 4) ^ 1) * 32 + 16 * kh) * 1024 + lane * 16;
        u32x4 of[10][2], y0[2], y1[2]; float g;
#pragma unroll
        for (int ks = 0; ks < 10; ++ks) { of[ks][0] = ld_o(rs, ob0, P3_KS(ks) * 1024); of[ks][1] = ld_o(rs, ob1, P3_KS(ks) * 1024); }
        SBAR();
        f32x4 acc[4], acp[4];
#pragma unroll
        for (int n = 0; n < 4; ++n) { acc[n] = f32x4{}; acp[n] = f32x4{}; }
#pragma unroll
        for (int ks = 0; ks < 16; ++ks) {
            if (ks + 1 < 16) P3_RD(wf[(ks + 1) & 1], ks + 1);
            SBAR();
#pragma unroll
            for (int n = 0; n < 4; ++n) acc[n] = MFMA16(wf[ks & 1][n], BC(of[ks % 10][0]), acc[n]);
#pragma unroll
            for (int n = 0; n < 4; ++n) acp[n] = MFMA16(wf[ks & 1][n], BC(of[ks % 10][1]), acp[n]);
            if (ks < 6) { of[ks][0] = ld_o(rs, ob0, P3_KS(10 + ks) * 1024); of[ks][1] = ld_o(rs, ob1, P3_KS(10 + ks) * 1024); }
            if (ks == 8) {
                g = __uint_as_float(__builtin_amdgcn_raw_buffer_load_b32(rs, WS_GATE + t * 4, 0, 16));
#pragma unroll
                for (int i = 0; i < 2; ++i) { y0[i] = ld_dev(rs, WS_Y + (t * DM + col + 32 * i) * 2); y1[i] = ld_dev(rs, WS_Y + 4 * MB + (t * DM + col + 32 * i) * 2); }
            }
            SBAR();
        }
#pragma unroll
        for (int n = 0; n < 4; ++n) *(LAS f32x4*)(smem + wid * 4096 + n * 1024 + lane * 16) = acp[n];
        LGKM_BAR();
#pragma unroll
        for (int n = 0; n < 4; ++n) acc[n] += *(const LAS f32x4*)(smem + (wid ^ 1) * 4096 + n * 1024 + lane * 16);
        const float g1 = 1.f - g;
#pragma unroll
        for (int i = 0; i < 2; ++i) {
            float v[8];
#pragma unroll
            for (int j = 0; j < 4; ++j) {
                auto r = __builtin_amdgcn_permlane16_swap(__float_as_uint(acc[2 * i][j]), __float_as_uint(acc[2 * i + 1][j]), false, false);
                v[j] = __uint_as_float(r[0]); v[4 + j] = __uint_as_float(r[1]);
            }
            u32x4 w;
#pragma unroll
            for (int j = 0; j < 4; ++j)
                w[j] = cvtpk(g * (bflo(y0[i][j]) - bflo(y1[i][j])) + g1 * v[2 * j], g * (bfhi(y0[i][j]) - bfhi(y1[i][j])) + g1 * v[2 * j + 1]);
            *(u32x4*)(out + (size_t)t * DM + col + 32 * i) = w;
        }
        if (wid == 0) STAMP(1, 4);
        if (wid == 7) STAMP(1, 5);
    }
}

DI void side_waves(LAS char* smem, int lw, int lane, int mt, int hd, const u16* __restrict__ x, const u16* __restrict__ wsr, const u16* __restrict__ wsi,
                   const u16* __restrict__ wdr, const u16* __restrict__ wdi, const u16* __restrict__ wqkv, const u16* __restrict__ wo,
                   const u16* __restrict__ gw, char* ws, rsrc_t rs) {
    const unsigned lds0 = (unsigned)(size_t)smem + lw * 1024;
    const unsigned rowl = 8 * lw + (lane >> 3), chk = ((lane & 7) ^ ((4 * lw + (lane >> 4)) & 7)) * 16;
    const int c = hd >> 4;
    const char* wb = (const char*)(c ? wsi : wsr) + (size_t)mt * 256 * 4096;
    unsigned vo[10];
    {
        const char* xb = (const char*)x + (size_t)mt * 256 * 2048;
        const char* wdb = (const char*)(c ? wdi : wdr) + (size_t)(hd & 15) * 64 * 2048;
        const char* wqb = (const char*)wqkv + hd * 64 + lw * 16 * 6144;
#pragma unroll
        for (int i = 0; i < 8; ++i) vo[i] = (rowl + 32 * i) * 2048 + chk;
        const unsigned voffW = (lane >> 2) * 6144 + (((lane & 3) ^ ((lane >> 5) << 1)) * 16);
#define K1_DMA(t, slot) do { const char* xa_ = xb + (size_t)(t) * 128; const char* wd_ = wdb + (size_t)(t) * 128; const char* wq_ = wqb + (size_t)(t) * 64 * 6144; \
        _Pragma("unroll") for (int u_ = 0; u_ < 3; ++u_) dma(voffW, wq_ + u_ * 2048, lds0 + (slot) + K1_WQ + u_ * 4096); \
        _Pragma("unroll") for (int i_ = 0; i_ < 2; ++i_) dma(vo[i_], wd_, lds0 + (slot) + K1_WD + i_ * 4096); \
        _Pragma("unroll") for (int i_ = 0; i_ < 8; ++i_) dma(vo[i_], xa_, lds0 + (slot) + K1_A + i_ * 4096); } while (0)
        K1_DMA(0, 0); K1_DMA(1, K1_STAGE);
        WAIT_BAR(13);
        K1_DMA(2, 2 * K1_STAGE);
        int sl = 0;
        for (int t = 0; t < 13; ++t) {
            WAIT_BAR(13);
            K1_DMA(t + 3, sl);
            sl = (sl == 2 * K1_STAGE) ? 0 : sl + K1_STAGE;
        }
        WAIT_BAR(13);
        WAIT_BAR(0);
#pragma unroll
        for (int i = 0; i < 8; ++i) vo[i] = (rowl + 32 * i) * 4096 + chk;
        vo[8] = lane * 16 + lw * 1024; vo[9] = vo[8] + 4096;
#pragma unroll
        for (int i = 0; i < 7; ++i) dma(vo[i], wb + (size_t)((4 * mt) & 31) * 128, lds0 + FW_TOP + i * 4096);
#pragma unroll
        for (int i = 0; i < 8; ++i) dma(vo[i], wb + (size_t)((4 * mt + 1) & 31) * 128, lds0 + FW_TOP - FW_STAGE + i * 4096);
        LGKM_BAR();
        dma(vo[7], wb + (size_t)((4 * mt) & 31) * 128, lds0 + FW_TOP + 7 * 4096);
        WAIT_BAR(0);
    }
    {
        const char* kvb = ws + WS_KV + hd * 262144;
        const unsigned vl = lane * 16 + lw * 1024;
        const char *bW = wb, *bA = ws + WS_AB + hd * 262144; int kmask = 31, kstep = 128, kstepA = 8192, koff = 4 * mt;
#define FN_DMA_P(p, s, fslot, aslot) do { \
        if ((p) < 8) dma(vo[p], bW + (size_t)((koff + (s)) & kmask) * kstep, lds0 + (fslot) + (p) * 4096); \
        else if ((p) < 10) dma_dev(vo[p], bA + (size_t)((koff + (s)) & kmask) * kstepA, lds0 + FB_BASE + (aslot) + ((p) - 8) * 4096); \
        else if ((p) == 10) dma_dev(vl, kvb + ((4 * mt + (s)) & 31) * 8192, lds0 + (aslot)); \
        else dma_dev(vl, kvb + ((4 * mt + (s) - ((((s) & 3) == 0 && lw < 2) ? 4 : 0)) & 31) * 8192 + 4096, lds0 + (aslot) + 4096); } while (0)
        const int fr = lane & 15, fq = lane >> 4;
        const int cpart = ((fq ^ (fr >> 1)) & 7) * 16;
        const int aW = (64 * lw + fr) * 128 + cpart, aA = FB_BASE + fr * 128 + cpart;
        f32x4 acc[4][4];
#pragma unroll
        for (int m = 0; m < 4; ++m)
#pragma unroll
            for (int n = 0; n < 4; ++n) acc[m][n] = f32x4{};
        FnFrag X, Yf;
#define FN_RD(F, i, slot, bslot, ks) do { \
        if ((i) == 0) F.w[0] = ldsr128(smem + (slot) + (aW ^ ((ks) * 64))); \
        else if ((i) < 5) F.a[(i) - 1] = ldsr128(smem + (bslot) + (aA ^ ((ks) * 64)) + ((i) - 1) * 2048); \
        else F.w[(i) - 4] = ldsr128(smem + (slot) + (aW ^ ((ks) * 64)) + ((i) - 4) * 2048); } while (0)
#define FN_MF(F, j) do { if (!ABL_FMFMA) acc[(j) >> 2][(j) & 3] = MFMA16(F.a[(j) & 3], F.w[(j) >> 2], acc[(j) >> 2][(j) & 3]); else asm volatile("" :: "v"(F.a[(j) & 3]), "v"(F.w[(j) >> 2])); } while (0)
#define FN_G(g, C, R, rslot, rb, ks, P0, s, fslot, aslot) do { FN_MF(C, 2 * (g)); FN_RD(R, g, rslot, rb, ks); FN_MF(C, 2 * (g) + 1); \
        if (!ABL_FDMA && ((g) & 3) != 0 && (P0) + (g) - 1 - ((g) >> 2) < PMAX) FN_DMA_P((P0) + (g) - 1 - ((g) >> 2), s, fslot, aslot); SBAR(); FN_PAD(g); } while (0)
#define FN_PHASE(C, R, rslot, rb, ks, P0, s, fslot, aslot) do { FN_G(0, C, R, rslot, rb, ks, P0, s, fslot, aslot); FN_G(1, C, R, rslot, rb, ks, P0, s, fslot, aslot); \
        FN_G(2, C, R, rslot, rb, ks, P0, s, fslot, aslot); FN_G(3, C, R, rslot, rb, ks, P0, s, fslot, aslot); FN_G(4, C, R, rslot, rb, ks, P0, s, fslot, aslot); \
        FN_G(5, C, R, rslot, rb, ks, P0, s, fslot, aslot); FN_G(6, C, R, rslot, rb, ks, P0, s, fslot, aslot); FN_G(7, C, R, rslot, rb, ks, P0, s, fslot, aslot); } while (0)
#define PMAX 12
        __builtin_amdgcn_s_setprio(FN_PRIO);
#pragma unroll
        for (int i = 0; i < 8; ++i) FN_RD(X, i, FW_TOP, 0, 0);
        LGKM_BAR();
        int f0 = FW_TOP, f1 = FW_TOP - FW_STAGE, f2 = FW_TOP - 2 * FW_STAGE;
        FN_DMA_P(0, 2, f2, 0); FN_DMA_P(1, 2, f2, 0); FN_DMA_P(2, 2, f2, 0); FN_DMA_P(3, 2, f2, 0); FN_DMA_P(4, 2, f2, 0); FN_DMA_P(5, 2, f2, 0);
        SBAR();
#define FN_ITER(t, WAITN) do { \
            FN_PHASE(X, Yf, f0, ((t) & 3) * 8192, 1, 6, (t) + 2, f2, (((t) + 2) & 3) * 8192);       \
            WAIT_BAR(WAITN); SBAR();                                                                \
            FN_SWITCH(t); \
            FN_PHASE(Yf, X, f1, (((t) + 1) & 3) * 8192, 0, 0, (t) + 3, f0, 0);                      \
            const int ft = f0; f0 = f1; f1 = f2; f2 = ft; } while (0)
#define FN_SWITCH(t)
#undef PMAX
#define PMAX 8
        FN_ITER(0, 8); FN_ITER(1, 8);
#undef PMAX
#define PMAX 12
#undef FN_SWITCH
#define FN_SWITCH(t) do { if (__builtin_expect((t) == 29, 0)) { bA = (const char*)wo + ((blockIdx.x >> 3) & 15) * 128; bW = bA + 256 * 2048; kmask = 3; kstep = 512 * 2048; kstepA = 64 * 2048; koff = 0; \
                const unsigned cw = ((lane & 7) ^ ((((lane >> 4) & 1) | ((lw & 1) << 1)) << 1)) * 16; \
                _Pragma("unroll") for (int i = 0; i < 10; ++i) vo[i] = (rowl + 32 * (i & 7)) * 2048 + cw; } SBAR(); } while (0)
        for (int t = 2; t < 31; ++t) FN_ITER(t, 12);
#undef PMAX
#define PMAX 10
        FN_PHASE(X, Yf, f0, 3 * 8192, 1, 6, 33, f2, 8192);
        WAIT_BAR(10); SBAR();
#pragma unroll
        for (int i = 0; i < 8; ++i) dma(vo[i], bA + (size_t)512 * 2048, lds0 + FW_TOP - FW_STAGE + i * 4096);
#pragma unroll
        for (int i = 0; i < 4; ++i) dma(vo[i], bA + (size_t)128 * 2048, lds0 + FB_BASE + 2 * 8192 + i * 4096);
#pragma unroll
        for (int j = 0; j < 16; ++j) FN_MF(Yf, j);
        const unsigned yb = WS_Y + c * 4 * MB + ((mt * 256 + 64 * lw + fr) * DM + (hd & 15) * 64 + 16 * (fq & 1) + 8 * (fq >> 1)) * 2;
#pragma unroll
        for (int m = 0; m < 4; ++m)
#pragma unroll
            for (int i = 0; i < 2; ++i) st_wt(rs, yb + (m * 16 * DM + 32 * i) * 2, swap16(pack4(acc[m][2 * i]), pack4(acc[m][2 * i + 1])));
        WAIT_BAR(0);
    }
}

__global__ __launch_bounds__(768, 3) void fused_kernel(const u16* __restrict__ x, const u16* __restrict__ wsr, const u16* __restrict__ wsi,
                                                        const u16* __restrict__ wdr, const u16* __restrict__ wdi, const u16* __restrict__ wqkv,
                                                        const u16* __restrict__ wo, const u16* __restrict__ gw, u16* __restrict__ out, char* ws) {
    __shared__ __attribute__((aligned(1024))) char smem_[163840];
    LAS char* smem = (LAS char*)smem_;
    const int tid = threadIdx.x, lane = tid & 63, wid = __builtin_amdgcn_readfirstlane(tid >> 6);
    const int xcd = blockIdx.x & 7, idx = blockIdx.x >> 3;
    const int hd = xcd * 4 + (idx >> 3), mt = idx & 7;
    const rsrc_t rs = __builtin_amdgcn_make_buffer_rsrc(ws, 0, WS_END, 0x00020000);
    if (wid == 0) STAMP(0, 0);
    if (wid < 8) {
        const char* wdb = (const char*)((hd >> 4) ? wdi : wdr) + (size_t)(hd & 15) * 64 * 2048;
        const char* wsb = (const char*)((hd >> 4) ? wsi : wsr) + (size_t)mt * 256 * 4096;
        const char* pf2 = (const char*)wo + (size_t)blockIdx.x * 8192;
        main_waves(smem, wid, lane, mt, hd, ws, rs, out, x, gw, wdb, (const char*)wqkv, wsb, pf2);
    }
    else side_waves(smem, wid - 8, lane, mt, hd, x, wsr, wsi, wdr, wdi, wqkv, wo, gw, ws, rs);
}

extern "C" void kernel_launch(void* const* d_in, const int* in_sizes, int n_in, void* d_out, int out_size, void* d_ws, size_t ws_size, hipStream_t stream) {
    const u16* x = (const u16*)d_in[0]; const u16* wsr = (const u16*)d_in[1]; const u16* wsi = (const u16*)d_in[2];
    const u16* wdr = (const u16*)d_in[3]; const u16* wdi = (const u16*)d_in[4]; const u16* wqkv = (const u16*)d_in[6];
    const u16* wo = (const u16*)d_in[7]; const u16* gw = (const u16*)d_in[8];
    fused_kernel<<<256, 768, 0, stream>>>(x, wsr, wsi, wdr, wdi, wqkv, wo, gw, (u16*)d_out, (char*)d_ws);
}
```

```cpp
#include <hip/hip_runtime.h>
#include <hip/hip_bf16.h>

#define O_AUX 16
#define O_ACQ 0
typedef unsigned short u16;
typedef short bf16x8 __attribute__((ext_vector_type(8)));
typedef short s16x4 __attribute__((ext_vector_type(4)));
typedef float f32x2 __attribute__((ext_vector_type(2)));
typedef float f32x4 __attribute__((ext_vector_type(4)));
typedef float f32x16 __attribute__((ext_vector_type(16)));
typedef unsigned u32x4 __attribute__((ext_vector_type(4)));
typedef unsigned u32x2 __attribute__((ext_vector_type(2)));
typedef __bf16 bf16x2_t __attribute__((ext_vector_type(2)));
#define LAS __attribute__((address_space(3)))
#define GAS __attribute__((address_space(1)))
#define DI __device__ __forceinline__
#define SBAR() __builtin_amdgcn_sched_barrier(0)
#define PIN(x) asm volatile("" : "+v"(x))
#define MFMA16(a, b, c) __builtin_amdgcn_mfma_f32_16x16x32_bf16(a, b, c, 0, 0, 0)
#define MFMA32(a, b, c) __builtin_amdgcn_mfma_f32_32x32x16_bf16(a, b, c, 0, 0, 0)
#define WAIT_BAR(N) asm volatile("s_waitcnt vmcnt(" #N ") lgkmcnt(0)\n\ts_barrier" ::: "memory")
#define LGKM_BAR() asm volatile("s_waitcnt lgkmcnt(0)\n\ts_barrier" ::: "memory")
#define RLX_AGENT __ATOMIC_RELAXED, __HIP_MEMORY_SCOPE_AGENT
#define BC(x) __builtin_bit_cast(bf16x8, x)

constexpr int SEQ = 2048, DM = 1024;
constexpr unsigned MB = 1u << 20;
constexpr unsigned WS_F1 = 4096, WS_F2 = 4096 + 16384, WS_GATE = 65536, WS_KV = 1 * MB, WS_AB = 9 * MB, WS_O = 17 * MB, WS_Y = 21 * MB, WS_END = 29 * MB;
typedef unsigned long long u64;

#define STAMP(kid, i)
#define TICK(k, i)

DI unsigned cvtpk(float lo, float hi) { f32x2 v = {lo, hi}; bf16x2_t b = __builtin_convertvector(v, bf16x2_t); return __builtin_bit_cast(unsigned, b); }
DI void dma(unsigned voff, const char* sbase, unsigned l) {
    asm volatile("s_mov_b32 m0, %2\n\ts_nop 0\n\tglobal_load_lds_dwordx4 %0, %1" :: "v"(voff), "s"(sbase), "s"(l) : "memory"); }
DI void dma_dev(unsigned voff, const char* sbase, unsigned l) {
    asm volatile("s_mov_b32 m0, %2\n\ts_nop 0\n\tglobal_load_lds_dwordx4 %0, %1 sc1" :: "v"(voff), "s"(sbase), "s"(l) : "memory"); }
DI void ldsw128(LAS char* p, u32x4 v) { *(LAS u32x4*)p = v; }
DI bf16x8 ldsr128(const LAS char* p) { return *(const LAS bf16x8*)p; }
DI bf16x8 ldstr(const LAS char* p, int off2) {
    s16x4 a = __builtin_amdgcn_ds_read_tr16_b64_v4i16((LAS s16x4*)p);
    s16x4 b = __builtin_amdgcn_ds_read_tr16_b64_v4i16((LAS s16x4*)(p + off2));
    return (bf16x8){a[0], a[1], a[2], a[3], b[0], b[1], b[2], b[3]};
}
DI int lane_now() { int l; asm volatile("v_mbcnt_lo_u32_b32 %0, -1, 0\n\tv_mbcnt_hi_u32_b32 %0, -1, %0" : "=v"(l)); return l; }
DI float bflo(unsigned u) { return __uint_as_float(u << 16); }
DI float bfhi(unsigned u) { return __uint_as_float(u & 0xffff0000u); }
DI u32x4 swap16(u32x2 a, u32x2 b) {
    auto r0 = __builtin_amdgcn_permlane16_swap(a[0], b[0], false, false);
    auto r1 = __builtin_amdgcn_permlane16_swap(a[1], b[1], false, false);
    return (u32x4){r0[0], r1[0], r0[1], r1[1]};
}
DI u32x2 pack4(f32x4 v) { return (u32x2){cvtpk(v[0], v[1]), cvtpk(v[2], v[3])}; }
typedef __amdgpu_buffer_rsrc_t rsrc_t;
DI void st_wt(rsrc_t r, unsigned off, u32x4 v) { __builtin_amdgcn_raw_buffer_store_b128(v, r, off, 0, 16); }
DI u32x4 ld_dev(rsrc_t r, unsigned off) { return __builtin_amdgcn_raw_buffer_load_b128(r, off, 0, 16); }
DI u32x4 ld_o(rsrc_t r, unsigned off, unsigned soff) { return __builtin_amdgcn_raw_buffer_load_b128(r, off, soff, 16); }
DI void wait_flags(const char* first, int n, u64 lo, u64 span, int lane) {
    const GAS u64* p = (const GAS u64*)(first + (lane < n ? lane : 0) * 64);
#pragma unroll 1
    for (unsigned spins = 0; spins < (1u << 16); ++spins) {
        const u64 v = __hip_atomic_load(p, RLX_AGENT);
        if (__all(v - lo <= span)) break;
        __builtin_amdgcn_s_sleep(1);
    }
}
DI void set_flag(char* p, u64 v, int lane) { if (lane == 0) __hip_atomic_store((GAS u64*)p, v, RLX_AGENT); }

#define ABL_FDMA 0
#define ABL_FMFMA 0
#define ABL_ATT 0
#define PADN 0
#define FN_PAD(g) do { _Pragma("unroll") for (int z_ = 0; z_ < PADN; ++z_) asm volatile("s_nop 3"); SBAR(); } while (0)
#define FN_PRIO 1
#define PF_MODE 0
#define TLB_WARM 0
#define TLB_SUB 16384
#define PF_PRE 1
#define PF_FIRST 0
#define PF_N0 3
#define PF_LEAD 8
#define PF_SLEEP 0
constexpr int K1_STAGE = 53248, K1_A = 0, K1_WD = 32768, K1_WQ = 40960;
constexpr int AT_STAGE = 8192, FB_BASE = 32768, FW_STAGE = 32768, FW_TOP = 131072;
struct K1Frag { bf16x8 x[4], wd[2], wqk[2], wv; };
struct FnFrag { bf16x8 w[4], a[4]; };

DI void attn_store(rsrc_t rs, unsigned off, const f32x16& o, float l) {
    const float rl = 1.0f / l; u32x4 w0, w1;
#pragma unroll
    for (int i = 0; i < 4; ++i) { w0[i] = cvtpk(o[2 * i] * rl, o[2 * i + 1] * rl); w1[i] = cvtpk(o[8 + 2 * i] * rl, o[9 + 2 * i] * rl); }
    st_wt(rs, off, w0); st_wt(rs, off + 256, w1);
}
DI bf16x8 attn_sel(int lane) {
    const short v = (((lane >> 2) & 1) == ((lane >> 4) & 1)) ? (short)0x3f80 : (short)0;
    return (bf16x8){v, v, v, v, v, v, v, v};
}
DI float max16(const f32x16& s) {
    float a = fmaxf(fmaxf(s[0], s[1]), s[2]), b = fmaxf(fmaxf(s[3], s[4]), s[5]);
#pragma unroll
    for (int r = 6; r < 16; r += 4) { a = fmaxf(fmaxf(a, s[r]), s[r + 1]); if (r + 3 < 16) b = fmaxf(fmaxf(b, s[r + 2]), s[r + 3]); }
    return fmaxf(a, b);
}
DI void attn_slow(rsrc_t rs, const char* kv, bf16x8 q0, bf16x8 q1, int lane, unsigned ooff) {
    __builtin_amdgcn_fence(__ATOMIC_ACQUIRE, "agent");
    const bf16x8 sel = attn_sel(lane);
    kv += lane * 16;
    float mx = -INFINITY;
    const f32x16 z = f32x16{};
#pragma unroll 1
    for (int j = 0; j < 64; ++j) {
        const char* kp = kv + (j >> 1) * 8192 + (j & 1) * 2048;
        f32x16 s = MFMA32(*(const bf16x8*)kp, q0, z); s = MFMA32(*(const bf16x8*)(kp + 1024), q1, s);
        mx = fmaxf(mx, max16(s));
    }
    mx = fmaxf(mx, __shfl_xor(mx, 32));
    f32x16 negm, o = f32x16{}; f32x4 l = f32x4{};
#pragma unroll
    for (int r = 0; r < 16; ++r) negm[r] = -mx;
#pragma unroll 1
    for (int j = 0; j < 64; ++j) {
        const char* kp = kv + (j >> 1) * 8192 + (j & 1) * 2048;
        const char* vp = kv + ((j & 1) ? (((j & 7) == 7 ? (j >> 3) * 4 : (j + 1) >> 1) * 8192 + 4096) : ((j >> 1) * 8192 + 6144));
        const bf16x8 v0 = *(const bf16x8*)vp, v1 = *(const bf16x8*)(vp + 1024);
        f32x16 s = MFMA32(*(const bf16x8*)kp, q0, negm); s = MFMA32(*(const bf16x8*)(kp + 1024), q1, s);
        u32x4 p0, p1;
#pragma unroll
        for (int r = 0; r < 16; ++r) s[r] = __builtin_amdgcn_exp2f(s[r]);
#pragma unroll
        for (int r = 0; r < 4; ++r) { p0[r] = cvtpk(s[2 * r], s[2 * r + 1]); p1[r] = cvtpk(s[8 + 2 * r], s[9 + 2 * r]); }
        o = MFMA32(v0, BC(p0), o); l = MFMA16(sel, BC(p0), l);
        o = MFMA32(v1, BC(p1), o); l = MFMA16(sel, BC(p1), l);
    }
    attn_store(rs, ooff, o, l[0]);
}

DI void touch8k(const char* p, unsigned voff, unsigned dump) {
    asm volatile("s_mov_b32 m0, %2\n\ts_nop 0\n\tglobal_load_lds_dword %0, %1" :: "v"(voff), "s"(p), "s"(dump) : "memory"); }
DI void touch_pair(int p, int l, int mt, int hd, const char* x, const char* wdb, const char* wqkv, unsigned dump) {
    const int n = l >= 56 ? l - 8 : l, i = (mt * 2 + (hd & 1)) * 24 + (n - 32);
    const char* a = n < 16 ? x + (size_t)(mt * 256 + hd * 8 + (n >> 1)) * 2048 + p * 256 + (n & 1) * 128
                  : n < 32 ? wdb + (size_t)(mt * 8 + ((n - 16) >> 1)) * 2048 + p * 256 + (n & 1) * 128
                           : wqkv + (size_t)(p * 128 + i / 3) * 6144 + (i % 3) * 2048 + (hd >> 1) * 128;
    asm volatile("s_mov_b32 m0, %1\n\ts_nop 0\n\tglobal_load_lds_dword %0, off" :: "v"(a), "s"(dump) : "memory");
}
DI void main_waves(LAS char* smem, int wid, int lane, int mt, int hd, char* ws, rsrc_t rs, u16* __restrict__ out,
                   const u16* __restrict__ x, const u16* __restrict__ gw, const char* wdb, const char* wqkv, const char* wsb, const char* pf2) {
    const unsigned dump = (unsigned)(size_t)smem + 3 * K1_STAGE + wid * 256;
    if (wid >= PF_FIRST && wid <= PF_PRE) { __builtin_amdgcn_s_sleep(PF_SLEEP); touch_pair(wid, lane, mt, hd, (const char*)x, wdb, wqkv, dump); }
    const int t_act = wid < 4 ? 7 + 2 * wid : 2 * wid - PF_LEAD;
#define TW(l_) (unsigned)(((hd & 15) * 16 + ((l_) >> 2)) * 4096 + ((l_) & 3) * 128)
    int fr = lane & 15, fq = lane >> 4;
    u64 ep, base; u32x4 gx0, gx1, gw0, gw1;
    {
        const int wm = wid >> 1, wn = wid & 1;
        const int cpart = ((fq ^ (fr >> 1)) & 7) * 16;
        const int aA = K1_A + (64 * wm + fr) * 128 + cpart, aD = K1_WD + (32 * wn + fr) * 128 + cpart;
        const int aT0 = (8 * fq + (fr >> 2)) * 64 + ((((lane & 3) >> 1) ^ ((fq & 1) << 1)) * 16) + (lane & 1) * 8;
        const int aQ = K1_WQ + wn * 4096 + aT0, aV = K1_WQ + 8192 + (aT0 ^ (wn * 32));
        f32x4 accQ[2][4], accV[4], accD[2][4];
#pragma unroll
        for (int m = 0; m < 4; ++m) { accQ[0][m] = f32x4{}; accQ[1][m] = f32x4{}; accV[m] = f32x4{}; accD[0][m] = f32x4{}; accD[1][m] = f32x4{}; }
        K1Frag X, Y;
#define K1_RD(F, i, slot, ks) do { const LAS char* sb_ = smem + (slot); \
        if ((i) == 0) F.wqk[0] = ldstr(sb_ + aQ + (ks) * 2048, 256); \
        else if ((i) == 1) { F.x[0] = ldsr128(sb_ + (aA ^ ((ks) * 64))); } \
        else if ((i) == 2) F.wqk[1] = ldstr(sb_ + (aQ ^ 32) + (ks) * 2048, 256); \
        else if ((i) == 3) F.wv = ldstr(sb_ + aV + (ks) * 2048, 256); \
        else if ((i) == 4) { F.wd[0] = ldsr128(sb_ + (aD ^ ((ks) * 64))); F.wd[1] = ldsr128(sb_ + (aD ^ ((ks) * 64)) + 2048); } \
        else if ((i) < 8) F.x[(i) - 4] = ldsr128(sb_ + (aA ^ ((ks) * 64)) + ((i) - 4) * 2048); } while (0)
#define K1_MF(F, j) do { constexpr int m_ = (j) / 5, r_ = (j) % 5; \
        if (r_ == 0) accQ[0][m_] = MFMA16(F.wqk[0], F.x[m_], accQ[0][m_]); \
        else if (r_ == 1) accQ[1][m_] = MFMA16(F.wqk[1], F.x[m_], accQ[1][m_]); \
        else if (r_ == 2) accV[m_] = MFMA16(F.x[m_], F.wv, accV[m_]); \
        else if (r_ == 3) accD[0][m_] = MFMA16(F.x[m_], F.wd[0], accD[0][m_]); \
        else accD[1][m_] = MFMA16(F.x[m_], F.wd[1], accD[1][m_]); } while (0)
#define K1_G(g, C, R, rslot, ks) do { K1_MF(C, 2 * (g)); if ((g) == 0) SBAR(); K1_RD(R, g, rslot, ks); K1_MF(C, 2 * (g) + 1); SBAR(); } while (0)
#define K1_PHASE(C, R, rslot, ks) do { K1_G(0, C, R, rslot, ks); K1_G(1, C, R, rslot, ks); K1_G(2, C, R, rslot, ks); K1_G(3, C, R, rslot, ks); K1_G(4, C, R, rslot, ks); \
        K1_G(5, C, R, rslot, ks); K1_G(6, C, R, rslot, ks); K1_G(7, C, R, rslot, ks); K1_G(8, C, R, rslot, ks); K1_G(9, C, R, rslot, ks); } while (0)
        asm volatile("" :: "s"(gw), "s"(out));
        __builtin_amdgcn_s_waitcnt(0xC07F);
        LGKM_BAR();
        if (wid == 0) STAMP(0, 1);
        if (wid > PF_PRE && wid <= 3) touch_pair(wid, lane, mt, hd, (const char*)x, wdb, wqkv, dump);
#pragma unroll
        for (int i = 0; i < 8; ++i) K1_RD(X, i, 0, 0);
        int sl = 0, sn = K1_STAGE;
        SBAR();
        for (int t = 0; t < 15; ++t) {
            K1_PHASE(X, Y, sl, 1);
            LGKM_BAR(); SBAR();
            TICK(0, t);
            if (__builtin_expect(t == t_act, 0)) {
                const int l_ = lane_now();
                if (wid < 4) touch8k(wsb + ((mt + wid) & 7) * 512, TW(l_), dump);
                else touch_pair(wid, l_, mt, hd, (const char*)x, wdb, wqkv, dump);
            }
            SBAR();
            K1_PHASE(Y, X, sn, 0);
            sl = sn; sn = (sn == 2 * K1_STAGE) ? 0 : sn + K1_STAGE;
        }
        K1_PHASE(X, Y, 0, 1);
        WAIT_BAR(0); SBAR();
        lane = lane_now();
        ep = __hip_atomic_load((const GAS u64*)ws, RLX_AGENT);
        { const u32x4* px = (const u32x4*)(x + (size_t)(256 * mt + 8 * hd + wid) * DM + lane * 16); const u32x4* pw = (const u32x4*)(gw + lane * 16);
          gx0 = px[0]; gx1 = px[1]; gw0 = pw[0]; gw1 = pw[1]; }
        SBAR();
        K1_MF(Y, 0); K1_MF(Y, 1); K1_MF(Y, 2); K1_MF(Y, 3); K1_MF(Y, 4); K1_MF(Y, 5); K1_MF(Y, 6); K1_MF(Y, 7); K1_MF(Y, 8); K1_MF(Y, 9);
        K1_MF(Y, 10); K1_MF(Y, 11); K1_MF(Y, 12); K1_MF(Y, 13); K1_MF(Y, 14); K1_MF(Y, 15); K1_MF(Y, 16); K1_MF(Y, 17); K1_MF(Y, 18); K1_MF(Y, 19);
        if (wid == 0) STAMP(0, 2);
        fr = lane & 15; fq = lane >> 4;
        const float qs = wn ? 1.0f : 0.17677669529663687f * 1.4426950408889634f;
#pragma unroll
        for (int m = 0; m < 4; ++m) {
            const u32x4 w = swap16(pack4(accQ[0][m] * qs), pack4(accQ[1][m] * qs));
            const int r = (16 * m + fr) & 31, rho = (r & 19) | ((r & 4) << 1) | ((r & 8) >> 1), j = 2 * wm + (m >> 1);
            const int in = (fq & 1) * 1024 + ((fq >> 1) * 32 + rho) * 16;
            ldsw128(smem + (wn ? wm * AT_STAGE + (m >> 1) * 2048 : FW_TOP - 2 * FW_STAGE + j * 2048) + in, w);
        }
        const int rv = 8 * (fr >> 2) + 4 * wn + (fr & 3);
#pragma unroll
        for (int i = 0; i < 2; ++i) {
            const int in = (fq & 1) * 1024 + ((fq >> 1) * 32 + rv) * 16;
            const u32x4 w = swap16(pack4(accV[2 * i]), pack4(accV[2 * i + 1]));
            ldsw128(smem + ((wm + i) & 3) * AT_STAGE + 6144 - i * 2048 + in, w);
#pragma unroll
            for (int n = 0; n < 2; ++n) {
                const u32x4 d = swap16(pack4(accD[n][2 * i]), pack4(accD[n][2 * i + 1]));
                const int el = 32 * wn + 16 * n + fr, ch = 4 * i + 2 * (fq & 1) + (fq >> 1);
                ldsw128(smem + FB_BASE + wm * 8192 + el * 128 + ((ch ^ (fr >> 1)) * 16), d);
            }
        }
        LGKM_BAR();
        if (wid == 0) STAMP(0, 3);
    }
    {
        const int qt = mt * 8 + wid;
        char* f1 = ws + WS_F1 + hd * 512;
        const unsigned rec0 = (hd * 32 + 4 * mt) * 8192 + wid * 1024 + lane * 16;
#define PUBLISH(q) do { const u32x4 a_ = *(const LAS u32x4*)(smem + (q) * 8192 + wid * 1024 + lane * 16), b_ = *(const LAS u32x4*)(smem + FB_BASE + (q) * 8192 + wid * 1024 + lane * 16); \
        st_wt(rs, WS_KV + rec0 + (q) * 8192, a_); st_wt(rs, WS_AB + rec0 + (q) * 8192, b_); } while (0)
        PUBLISH(0); PUBLISH(1);
        const bf16x8 sel = attn_sel(lane);
        const LAS char* fb = smem + lane * 16;
        const bf16x8 q0 = ldsr128(fb + FW_TOP - 2 * FW_STAGE + wid * 2048), q1 = ldsr128(fb + FW_TOP - 2 * FW_STAGE + wid * 2048 + 1024);
        bf16x8 kA0, kA1, kB0, kB1, vA0, vA1, vB0, vB1 = bf16x8{};
        f32x16 negm, o = f32x16{}, sA, sB; f32x4 l = f32x4{};
        u32x4 p0, p1 = u32x4{};
        kA0 = ldsr128(fb); kA1 = ldsr128(fb + 1024); kB0 = ldsr128(fb + 2048); kB1 = ldsr128(fb + 3072); vA0 = ldsr128(fb + 6144); vA1 = ldsr128(fb + 7168);
        SBAR(); LGKM_BAR(); SBAR();
        {
            sA = MFMA32(kA0, q0, o); sA = MFMA32(kA1, q1, sA);
            float mx = max16(sA); mx = fmaxf(mx, __shfl_xor(mx, 32)) + 40.f;
#pragma unroll
            for (int r = 0; r < 16; ++r) { negm[r] = -mx; sA[r] -= mx; }
        }
        {
            float s = 0.f;
#pragma unroll
            for (int i = 0; i < 4; ++i) {
                s = fmaf(bflo(gx0[i]), bflo(gw0[i]), s); s = fmaf(bfhi(gx0[i]), bfhi(gw0[i]), s);
                s = fmaf(bflo(gx1[i]), bflo(gw1[i]), s); s = fmaf(bfhi(gx1[i]), bfhi(gw1[i]), s);
            }
#pragma unroll
            for (int o = 32; o >= 1; o >>= 1) s += __shfl_xor(s, o);
            if (lane == 0) __builtin_amdgcn_raw_buffer_store_b32(__float_as_uint(1.f / (1.f + __expf(-s))), rs, WS_GATE + (256 * mt + 8 * hd + wid) * 4, 0, 16);
        }
        base = (((u64)(unsigned)__builtin_amdgcn_readfirstlane((unsigned)(ep >> 32)) << 32) | (unsigned)__builtin_amdgcn_readfirstlane((unsigned)ep)) + 8;
        if (wid == 0) STAMP(1, 0);
#define EX(S_, i) S_[i] = __builtin_amdgcn_exp2f(S_[i])
#define AT_STEP(SC, SN, K0, K1, VC0, VP1, RD0, RD1, RD2, RD3) do { \
        SN = MFMA32(K0, q0, negm); SBAR(); RD0; EX(SC, 0); EX(SC, 1); EX(SC, 2); EX(SC, 3); SBAR(); \
        o = MFMA32(VP1, BC(p1), o); PIN(o); SBAR(); RD1; EX(SC, 4); EX(SC, 5); EX(SC, 6); EX(SC, 7); p0[0] = cvtpk(SC[0], SC[1]); p0[1] = cvtpk(SC[2], SC[3]); SBAR(); \
        l = MFMA16(sel, BC(p1), l); PIN(l); SBAR(); p0[2] = cvtpk(SC[4], SC[5]); p0[3] = cvtpk(SC[6], SC[7]); EX(SC, 8); SBAR(); \
        SN = MFMA32(K1, q1, SN); SBAR(); RD2; EX(SC, 9); EX(SC, 10); EX(SC, 11); EX(SC, 12); SBAR(); \
        o = MFMA32(VC0, BC(p0), o); PIN(o); SBAR(); RD3; EX(SC, 13); EX(SC, 14); EX(SC, 15); p1[0] = cvtpk(SC[8], SC[9]); SBAR(); \
        l = MFMA16(sel, BC(p0), l); PIN(l); SBAR(); p1[1] = cvtpk(SC[10], SC[11]); p1[2] = cvtpk(SC[12], SC[13]); p1[3] = cvtpk(SC[14], SC[15]); SBAR(); } while (0)
        int sl = AT_STAGE;
#define AT_TICK() do { \
            AT_STEP(sA, sB, kB0, kB1, vA0, vB1, kA0 = ldsr128(fb + sl), kA1 = ldsr128(fb + sl + 1024), vB0 = ldsr128(fb + sl + 4096), vB1 = ldsr128(fb + sl + 5120)); \
            AT_STEP(sB, sA, kA0, kA1, vB0, vA1, kB0 = ldsr128(fb + sl + 2048), kB1 = ldsr128(fb + sl + 3072), vA0 = ldsr128(fb + sl + 6144), vA1 = ldsr128(fb + sl + 7168)); \
            sl = (sl + AT_STAGE) & (4 * AT_STAGE - 1); } while (0)
#pragma unroll 1
        for (int T = 1; T <= 2; ++T) {
            asm volatile("s_waitcnt vmcnt(0)" ::: "memory");
            if (wid == 1 && T == 2) wait_flags(f1, 8, base + 1, 1, lane);
            LGKM_BAR(); SBAR();
            TICK(1, T);
            if (wid == 0) set_flag(f1 + mt * 64, base + T, lane);
            if (T == 1) { PUBLISH(2); PUBLISH(3); }
            SBAR();
            AT_TICK();
        }
        LGKM_BAR(); SBAR();
        {
            const u64 fv = __hip_atomic_load((const GAS u64*)(f1 + (lane & 7) * 64), RLX_AGENT);
            SBAR();
            AT_TICK();
            if (wid == 1 && !__all(fv == base + 2)) wait_flags(f1, 8, base + 2, 0, lane);
        }
        const int t_touch = wid >= 4 ? 4 * wid - 8 : 0;
#pragma unroll 1
        for (int T = 4; T <= 32; ++T) {
            LGKM_BAR(); SBAR();
            TICK(1, T);
            if (__builtin_expect(T == t_touch, 0)) {
                asm volatile("global_load_dword v167, %0, %1" :: "v"(TW(lane)), "s"(wsb + ((mt + wid) & 7) * 512) : "memory", "v167");
                if (wid == 7) asm volatile("global_load_dword v167, %0, %1" :: "v"(lane * 128), "s"(pf2) : "memory", "v167");
            }
            SBAR();
            AT_TICK();
        }
        o = MFMA32(vB1, BC(p1), o); l = MFMA16(sel, BC(p1), l);
        if (wid == 0) STAMP(1, 1);
        bool bad = !(l[0] >= 7.8886e-31f && l[0] <= 3.0e38f);
#pragma unroll
        for (int r = 0; r < 16; ++r) bad |= !(fabsf(o[r]) <= 3.0e38f);
        const int r = lane & 31, qrow = qt * 32 + ((r & 19) | ((r & 4) << 1) | ((r & 8) >> 1));
        const unsigned ooff = WS_O + ((qrow >> 4) * 32 + hd) * 1024 + ((lane >> 5) * 32 + (qrow & 15)) * 16;
        if (!ABL_ATT && !ABL_FDMA && __builtin_expect(__any(bad), 0)) attn_slow(rs, ws + WS_KV + hd * 262144, q0, q1, lane, ooff);
        else attn_store(rs, ooff, o, l[0]);
    }
    {
        const int rt = 2 * (blockIdx.x & 7) + (blockIdx.x >> 7), nt = (blockIdx.x >> 3) & 15;
        WAIT_BAR(0);
        if (wid == 0) STAMP(1, 2);
        const int kq = fr >> 2, swz = ((kq >> 1) & 1) | ((fq & 1) << 1);
        const int aW = (8 * fq + kq) * 128 + ((((lane & 3) >> 1) ^ (swz << 1)) * 16) + (lane & 1) * 8;
        const int kh = wid & 1;
        unsigned ord = 0, left = 0x1111u;
#define P3_MASK() ((unsigned)__builtin_amdgcn_readfirstlane(*(const volatile LAS unsigned*)(smem + AT_STAGE)))
#define P3_TAKE(ks_) do { unsigned av_; \
            _Pragma("unroll 1") for (unsigned sp_ = 0;; ++sp_) { const unsigned m_ = P3_MASK() >> (16 * kh); av_ = m_ & (m_ >> 1) & (m_ >> 2) & (m_ >> 3) & left; \
                if (av_) break; if (sp_ > (1u << 20)) { av_ = left; break; } __builtin_amdgcn_s_sleep(1); } \
            const unsigned g_ = __builtin_ctz(av_); left &= ~(1u << g_); ord |= g_ << (ks_); asm volatile("" ::: "memory"); } while (0)
#define P3_KS(ks_) (16 * kh + ((ord >> ((ks_) & 12)) & 15u) + ((ks_) & 3))
#define P3_RD(W_, ks_) do { const int wb_ = FB_BASE + P3_KS(ks_) * 4096; \
            _Pragma("unroll") for (int n_ = 0; n_ < 4; ++n_) W_[n_] = ldstr(smem + wb_ + (aW ^ (n_ * 32)), 512); } while (0)
        bf16x8 wf[2][4];
        const int R0 = rt * 128 + 16 * wid;
        const int t = R0 + fr, col = nt * 64 + 16 * (fq & 1) + 8 * (fq >> 1);
        const unsigned ob0 = WS_O + (R0 >> 4) * 32768 + lane * 16, ob1 = WS_O + ((R0 >> 4) ^ 1) * 32768 + lane * 16;
        u32x4 of[8][2], y0[2], y1[2]; float g;
#pragma unroll
        for (int ks = 0; ks < 8; ++ks) {
            if ((ks & 3) == 0) P3_TAKE(ks);
            of[ks][0] = ld_o(rs, ob0, P3_KS(ks) * 1024); of[ks][1] = ld_o(rs, ob1, P3_KS(ks) * 1024);
        }
        if (wid == 0) STAMP(1, 3);
        P3_RD(wf[0], 0);
        SBAR();
        f32x4 acc[4], acp[4];
#pragma unroll
        for (int n = 0; n < 4; ++n) { acc[n] = f32x4{}; acp[n] = f32x4{}; }
#pragma unroll
        for (int ks = 0; ks < 16; ++ks) {
            if (ks + 1 < 16) P3_RD(wf[(ks + 1) & 1], ks + 1);
            SBAR();
#pragma unroll
            for (int n = 0; n < 4; ++n) acc[n] = MFMA16(wf[ks & 1][n], BC(of[ks & 7][0]), acc[n]);
#pragma unroll
            for (int n = 0; n < 4; ++n) acp[n] = MFMA16(wf[ks & 1][n], BC(of[ks & 7][1]), acp[n]);
            asm volatile("" : "+v"(acc[0]), "+v"(acc[1]), "+v"(acc[2]), "+v"(acc[3]), "+v"(acp[0]), "+v"(acp[1]), "+v"(acp[2]), "+v"(acp[3]));
            if (ks < 8) {
                if ((ks & 3) == 0) P3_TAKE(8 + ks);
                of[ks][0] = ld_o(rs, ob0, P3_KS(8 + ks) * 1024); of[ks][1] = ld_o(rs, ob1, P3_KS(8 + ks) * 1024);
            }
            if (ks == 8) {
#pragma unroll 1
                for (unsigned sp = 0; P3_MASK() != 0xffffffffu && sp < (1u << 20); ++sp) __builtin_amdgcn_s_sleep(1);
                asm volatile("" ::: "memory");
                g = __uint_as_float(__builtin_amdgcn_raw_buffer_load_b32(rs, WS_GATE + t * 4, 0, 16));
#pragma unroll
                for (int i = 0; i < 2; ++i) { const unsigned yo = WS_Y + ((R0 >> 4) * 16 + nt) * 2048 + i * 1024 + lane * 16; y0[i] = ld_dev(rs, yo); y1[i] = ld_dev(rs, yo + 4 * MB); }
            }
            SBAR();
        }
        LGKM_BAR();
#pragma unroll
        for (int n = 0; n < 4; ++n) *(LAS f32x4*)(smem + wid * 4096 + n * 1024 + lane * 16) = acp[n];
        LGKM_BAR();
#pragma unroll
        for (int n = 0; n < 4; ++n) acc[n] += *(const LAS f32x4*)(smem + (wid ^ 1) * 4096 + n * 1024 + lane * 16);
        const float g1 = 1.f - g;
#pragma unroll
        for (int i = 0; i < 2; ++i) {
            float v[8];
#pragma unroll
            for (int j = 0; j < 4; ++j) {
                auto r = __builtin_amdgcn_permlane16_swap(__float_as_uint(acc[2 * i][j]), __float_as_uint(acc[2 * i + 1][j]), false, false);
                v[j] = __uint_as_float(r[0]); v[4 + j] = __uint_as_float(r[1]);
            }
            u32x4 w;
#pragma unroll
            for (int j = 0; j < 4; ++j)
                w[j] = cvtpk(g * (bflo(y0[i][j]) - bflo(y1[i][j])) + g1 * v[2 * j], g * (bfhi(y0[i][j]) - bfhi(y1[i][j])) + g1 * v[2 * j + 1]);
            *(u32x4*)(out + (size_t)t * DM + col + 32 * i) = w;
        }
        if (wid == 0) STAMP(1, 4);
        if (wid == 7) STAMP(1, 5);
    }
}

DI void side_waves(LAS char* smem, int lw, int lane, int mt, int hd, const u16* __restrict__ x, const u16* __restrict__ wsr, const u16* __restrict__ wsi,
                   const u16* __restrict__ wdr, const u16* __restrict__ wdi, const u16* __restrict__ wqkv, const u16* __restrict__ wo,
                   const u16* __restrict__ gw, char* ws, rsrc_t rs) {
    const unsigned lds0 = (unsigned)(size_t)smem + lw * 1024;
    const unsigned rowl = 8 * lw + (lane >> 3), chk = ((lane & 7) ^ ((4 * lw + (lane >> 4)) & 7)) * 16;
    const int c = hd >> 4;
    const char* wb = (const char*)(c ? wsi : wsr) + (size_t)mt * 256 * 4096;
    u64 base;
    unsigned vo[10];
    {
        const char* xb = (const char*)x + (size_t)mt * 256 * 2048;
        const char* wdb = (const char*)(c ? wdi : wdr) + (size_t)(hd & 15) * 64 * 2048;
        const char* wqb = (const char*)wqkv + hd * 64 + lw * 16 * 6144;
#pragma unroll
        for (int i = 0; i < 8; ++i) vo[i] = (rowl + 32 * i) * 2048 + chk;
        const unsigned voffW = (lane >> 2) * 6144 + (((lane & 3) ^ ((lane >> 5) << 1)) * 16);
#define K1_DMA(t, slot) do { const char* xa_ = xb + (size_t)(t) * 128; const char* wd_ = wdb + (size_t)(t) * 128; const char* wq_ = wqb + (size_t)(t) * 64 * 6144; \
        _Pragma("unroll") for (int u_ = 0; u_ < 3; ++u_) dma(voffW, wq_ + u_ * 2048, lds0 + (slot) + K1_WQ + u_ * 4096); \
        _Pragma("unroll") for (int i_ = 0; i_ < 2; ++i_) dma(vo[i_], wd_, lds0 + (slot) + K1_WD + i_ * 4096); \
        _Pragma("unroll") for (int i_ = 0; i_ < 8; ++i_) dma(vo[i_], xa_, lds0 + (slot) + K1_A + i_ * 4096); } while (0)
        K1_DMA(0, 0); K1_DMA(1, K1_STAGE);
        WAIT_BAR(13);
        K1_DMA(2, 2 * K1_STAGE);
        int sl = 0;
        for (int t = 0; t < 13; ++t) {
            WAIT_BAR(13);
            K1_DMA(t + 3, sl);
            sl = (sl == 2 * K1_STAGE) ? 0 : sl + K1_STAGE;
        }
        WAIT_BAR(13);
        WAIT_BAR(0);
#pragma unroll
        for (int i = 0; i < 8; ++i) vo[i] = (rowl + 32 * i) * 4096 + chk;
        vo[8] = lane * 16 + lw * 1024; vo[9] = vo[8] + 4096;
#pragma unroll
        for (int i = 0; i < 7; ++i) dma(vo[i], wb + (size_t)((4 * mt) & 31) * 128, lds0 + FW_TOP + i * 4096);
#pragma unroll
        for (int i = 0; i < 8; ++i) dma(vo[i], wb + (size_t)((4 * mt + 1) & 31) * 128, lds0 + FW_TOP - FW_STAGE + i * 4096);
        LGKM_BAR();
        dma(vo[7], wb + (size_t)((4 * mt) & 31) * 128, lds0 + FW_TOP + 7 * 4096);
        const u64 ep = __hip_atomic_load((const GAS u64*)ws, RLX_AGENT);
        base = (((u64)(unsigned)__builtin_amdgcn_readfirstlane((unsigned)(ep >> 32)) << 32) | (unsigned)__builtin_amdgcn_readfirstlane((unsigned)ep)) + 8;
        WAIT_BAR(0);
    }
    {
        const char* kvb = ws + WS_KV + hd * 262144;
        const unsigned vl = lane * 16 + lw * 1024;
        const char *bW = wb, *bA = ws + WS_AB + hd * 262144; int kmask = 31, kstep = 128, kstepA = 8192, koff = 4 * mt;
#define FN_DMA_P(p, s, fslot, aslot) do { \
        if ((p) < 8) dma(vo[p], bW + (size_t)((koff + (s)) & kmask) * kstep, lds0 + (fslot) + (p) * 4096); \
        else if ((p) < 10) dma_dev(vo[p], bA + (size_t)((koff + (s)) & kmask) * kstepA, lds0 + FB_BASE + (aslot) + ((p) - 8) * 4096); \
        else if ((p) == 10) dma_dev(vl, kvb + ((4 * mt + (s)) & 31) * 8192, lds0 + (aslot)); \
        else dma_dev(vl, kvb + ((4 * mt + (s) - ((((s) & 3) == 0 && lw < 2) ? 4 : 0)) & 31) * 8192 + 4096, lds0 + (aslot) + 4096); } while (0)
        const int fr = lane & 15, fq = lane >> 4;
        const int cpart = ((fq ^ (fr >> 1)) & 7) * 16;
        const int aW = (64 * lw + fr) * 128 + cpart, aA = FB_BASE + fr * 128 + cpart;
        f32x4 acc[4][4];
#pragma unroll
        for (int m = 0; m < 4; ++m)
#pragma unroll
            for (int n = 0; n < 4; ++n) acc[m][n] = f32x4{};
        FnFrag X, Yf;
#define FN_RD(F, i, slot, bslot, ks) do { \
        if ((i) == 0) F.w[0] = ldsr128(smem + (slot) + (aW ^ ((ks) * 64))); \
        else if ((i) < 5) F.a[(i) - 1] = ldsr128(smem + (bslot) + (aA ^ ((ks) * 64)) + ((i) - 1) * 2048); \
        else F.w[(i) - 4] = ldsr128(smem + (slot) + (aW ^ ((ks) * 64)) + ((i) - 4) * 2048); } while (0)
#define FN_MF(F, j) do { if (!ABL_FMFMA) acc[(j) >> 2][(j) & 3] = MFMA16(F.a[(j) & 3], F.w[(j) >> 2], acc[(j) >> 2][(j) & 3]); else asm volatile("" :: "v"(F.a[(j) & 3]), "v"(F.w[(j) >> 2])); } while (0)
#define FN_G(g, C, R, rslot, rb, ks, P0, s, fslot, aslot) do { FN_MF(C, 2 * (g)); FN_RD(R, g, rslot, rb, ks); FN_MF(C, 2 * (g) + 1); \
        if (!ABL_FDMA && ((g) & 3) != 0 && (P0) + (g) - 1 - ((g) >> 2) < PMAX) FN_DMA_P((P0) + (g) - 1 - ((g) >> 2), s, fslot, aslot); SBAR(); FN_PAD(g); } while (0)
#define FN_PHASE(C, R, rslot, rb, ks, P0, s, fslot, aslot) do { FN_G(0, C, R, rslot, rb, ks, P0, s, fslot, aslot); FN_G(1, C, R, rslot, rb, ks, P0, s, fslot, aslot); \
        FN_G(2, C, R, rslot, rb, ks, P0, s, fslot, aslot); FN_G(3, C, R, rslot, rb, ks, P0, s, fslot, aslot); FN_G(4, C, R, rslot, rb, ks, P0, s, fslot, aslot); \
        FN_G(5, C, R, rslot, rb, ks, P0, s, fslot, aslot); FN_G(6, C, R, rslot, rb, ks, P0, s, fslot, aslot); FN_G(7, C, R, rslot, rb, ks, P0, s, fslot, aslot); } while (0)
#define PMAX 12
        __builtin_amdgcn_s_setprio(FN_PRIO);
#pragma unroll
        for (int i = 0; i < 8; ++i) FN_RD(X, i, FW_TOP, 0, 0);
        LGKM_BAR();
        int f0 = FW_TOP, f1 = FW_TOP - FW_STAGE, f2 = FW_TOP - 2 * FW_STAGE;
        FN_DMA_P(0, 2, f2, 0); FN_DMA_P(1, 2, f2, 0); FN_DMA_P(2, 2, f2, 0); FN_DMA_P(3, 2, f2, 0); FN_DMA_P(4, 2, f2, 0); FN_DMA_P(5, 2, f2, 0);
        SBAR();
#define FN_ITER(t, WAITN) do { \
            FN_PHASE(X, Yf, f0, ((t) & 3) * 8192, 1, 6, (t) + 2, f2, (((t) + 2) & 3) * 8192);       \
            WAIT_BAR(WAITN); SBAR();                                                                \
            FN_SWITCH(t); \
            FN_PHASE(Yf, X, f1, (((t) + 1) & 3) * 8192, 0, 0, (t) + 3, f0, 0);                      \
            const int ft = f0; f0 = f1; f1 = f2; f2 = ft; } while (0)
#define FN_SWITCH(t)
#undef PMAX
#define PMAX 8
        FN_ITER(0, 8); FN_ITER(1, 8);
#undef PMAX
#define PMAX 12
#undef FN_SWITCH
#define FN_SWITCH(t) do { if (__builtin_expect((t) == 29, 0)) { bA = (const char*)wo + ((blockIdx.x >> 3) & 15) * 128; bW = bA + 256 * 2048; kmask = 3; kstep = 512 * 2048; kstepA = 64 * 2048; koff = 0; \
                const unsigned cw = ((lane & 7) ^ ((((lane >> 4) & 1) | ((lw & 1) << 1)) << 1)) * 16; \
                _Pragma("unroll") for (int i = 0; i < 10; ++i) vo[i] = (rowl + 32 * (i & 7)) * 2048 + cw; } SBAR(); } while (0)
        for (int t = 2; t < 31; ++t) FN_ITER(t, 12);
#undef PMAX
#define PMAX 10
        FN_PHASE(X, Yf, f0, 3 * 8192, 1, 6, 33, f2, 8192);
        WAIT_BAR(10); SBAR();
#pragma unroll
        for (int i = 0; i < 8; ++i) dma(vo[i], bA + (size_t)512 * 2048, lds0 + FW_TOP - FW_STAGE + i * 4096);
#pragma unroll
        for (int i = 0; i < 4; ++i) dma(vo[i], bA + (size_t)128 * 2048, lds0 + FB_BASE + 2 * 8192 + i * 4096);
#pragma unroll
        for (int j = 0; j < 16; ++j) FN_MF(Yf, j);
        const unsigned yb = WS_Y + c * 4 * MB + ((mt * 16 + 4 * lw) * 16 + (hd & 15)) * 2048 + lane * 16;
#pragma unroll
        for (int m = 0; m < 4; ++m)
#pragma unroll
            for (int i = 0; i < 2; ++i) st_wt(rs, yb + m * 32768 + i * 1024, swap16(pack4(acc[m][2 * i]), pack4(acc[m][2 * i + 1])));
        if (lw == 0) {
            const char* f2 = ws + WS_F2 + (blockIdx.x & 7) * 2048;
            const unsigned mask = (unsigned)__builtin_amdgcn_ballot_w64(__hip_atomic_load((const GAS u64*)(f2 + (lane & 31) * 64), RLX_AGENT) == base + 5);
            *(LAS unsigned*)(smem + AT_STAGE) = mask;
            WAIT_BAR(0);
            set_flag(ws + WS_F2 + (mt * 32 + hd) * 64, base + 5, lane);
#pragma unroll 1
            for (unsigned spins = 0; spins < (1u << 16); ++spins) {
                const unsigned m = (unsigned)__builtin_amdgcn_ballot_w64(__hip_atomic_load((const GAS u64*)(f2 + (lane & 31) * 64), RLX_AGENT) == base + 5);
                *(volatile LAS unsigned*)(smem + AT_STAGE) = m;
                if (m == 0xffffffffu) break;
                __builtin_amdgcn_s_sleep(1);
            }
            if (blockIdx.x == 0) set_flag(ws, base, lane);
        } else
            WAIT_BAR(0);
    }
}

__global__ __launch_bounds__(768, 3) void fused_kernel(const u16* __restrict__ x, const u16* __restrict__ wsr, const u16* __restrict__ wsi,
                                                        const u16* __restrict__ wdr, const u16* __restrict__ wdi, const u16* __restrict__ wqkv,
                                                        const u16* __restrict__ wo, const u16* __restrict__ gw, u16* __restrict__ out, char* ws) {
    __shared__ __attribute__((aligned(1024))) char smem_[163840];
    LAS char* smem = (LAS char*)smem_;
    const int tid = threadIdx.x, lane = tid & 63, wid = __builtin_amdgcn_readfirstlane(tid >> 6);
    const int xcd = blockIdx.x & 7, idx = blockIdx.x >> 3;
    const int hd = xcd * 4 + (idx >> 3), mt = idx & 7;
    const rsrc_t rs = __builtin_amdgcn_make_buffer_rsrc(ws, 0, WS_END, 0x00020000);
    if (wid == 0) STAMP(0, 0);
    if (wid < 8) {
        const char* wdb = (const char*)((hd >> 4) ? wdi : wdr) + (size_t)(hd & 15) * 64 * 2048;
        const char* wsb = (const char*)((hd >> 4) ? wsi : wsr) + (size_t)mt * 256 * 4096;
        const char* pf2 = (const char*)wo + (size_t)blockIdx.x * 8192;
        main_waves(smem, wid, lane, mt, hd, ws, rs, out, x, gw, wdb, (const char*)wqkv, wsb, pf2);
    }
    else side_waves(smem, wid - 8, lane, mt, hd, x, wsr, wsi, wdr, wdi, wqkv, wo, gw, ws, rs);
}

extern "C" void kernel_launch(void* const* d_in, const int* in_sizes, int n_in, void* d_out, int out_size, void* d_ws, size_t ws_size, hipStream_t stream) {
    const u16* x = (const u16*)d_in[0]; const u16* wsr = (const u16*)d_in[1]; const u16* wsi = (const u16*)d_in[2];
    const u16* wdr = (const u16*)d_in[3]; const u16* wdi = (const u16*)d_in[4]; const u16* wqkv = (const u16*)d_in[6];
    const u16* wo = (const u16*)d_in[7]; const u16* gw = (const u16*)d_in[8];
    fused_kernel<<<256, 768, 0, stream>>>(x, wsr, wsi, wdr, wdi, wqkv, wo, gw, (u16*)d_out, (char*)d_ws);
}
```

```cpp
#define DIAG_B 1
#include <hip/hip_runtime.h>
#include <hip/hip_bf16.h>

#define O_AUX 16
#define O_ACQ 0
typedef unsigned short u16;
typedef short bf16x8 __attribute__((ext_vector_type(8)));
typedef short s16x4 __attribute__((ext_vector_type(4)));
typedef float f32x2 __attribute__((ext_vector_type(2)));
typedef float f32x4 __attribute__((ext_vector_type(4)));
typedef float f32x16 __attribute__((ext_vector_type(16)));
typedef unsigned u32x4 __attribute__((ext_vector_type(4)));
typedef unsigned u32x2 __attribute__((ext_vector_type(2)));
typedef __bf16 bf16x2_t __attribute__((ext_vector_type(2)));
#define LAS __attribute__((address_space(3)))
#define GAS __attribute__((address_space(1)))
#define DI __device__ __forceinline__
#define SBAR() __builtin_amdgcn_sched_barrier(0)
#define PIN(x) asm volatile("" : "+v"(x))
#define MFMA16(a, b, c) __builtin_amdgcn_mfma_f32_16x16x32_bf16(a, b, c, 0, 0, 0)
#define MFMA32(a, b, c) __builtin_amdgcn_mfma_f32_32x32x16_bf16(a, b, c, 0, 0, 0)
#define WAIT_BAR(N) asm volatile("s_waitcnt vmcnt(" #N ") lgkmcnt(0)\n\ts_barrier" ::: "memory")
#define LGKM_BAR() asm volatile("s_waitcnt lgkmcnt(0)\n\ts_barrier" ::: "memory")
#define RLX_AGENT __ATOMIC_RELAXED, __HIP_MEMORY_SCOPE_AGENT
#define BC(x) __builtin_bit_cast(bf16x8, x)

constexpr int SEQ = 2048, DM = 1024;
constexpr unsigned MB = 1u << 20;
constexpr unsigned WS_F1 = 4096, WS_F2 = 4096 + 16384, WS_GATE = 65536, WS_KV = 1 * MB, WS_AB = 9 * MB, WS_O = 17 * MB, WS_Y = 21 * MB, WS_END = 29 * MB;
typedef unsigned long long u64;

#define TICK(k, i)
#define STAMP(kid, i) do { constexpr int id_ = (kid) * 4 + (i); if (id_ == DIAG_B) { dg1 = __builtin_amdgcn_s_memrealtime(); __builtin_amdgcn_s_waitcnt(0xC07F); } } while (0)

DI unsigned cvtpk(float lo, float hi) { f32x2 v = {lo, hi}; bf16x2_t b = __builtin_convertvector(v, bf16x2_t); return __builtin_bit_cast(unsigned, b); }
DI void dma(unsigned voff, const char* sbase, unsigned l) {
    asm volatile("s_mov_b32 m0, %2\n\ts_nop 0\n\tglobal_load_lds_dwordx4 %0, %1" :: "v"(voff), "s"(sbase), "s"(l) : "memory"); }
DI void dma_dev(unsigned voff, const char* sbase, unsigned l) {
    asm volatile("s_mov_b32 m0, %2\n\ts_nop 0\n\tglobal_load_lds_dwordx4 %0, %1 sc1" :: "v"(voff), "s"(sbase), "s"(l) : "memory"); }
DI void ldsw128(LAS char* p, u32x4 v) { *(LAS u32x4*)p = v; }
DI bf16x8 ldsr128(const LAS char* p) { return *(const LAS bf16x8*)p; }
DI bf16x8 ldstr(const LAS char* p, int off2) {
    s16x4 a = __builtin_amdgcn_ds_read_tr16_b64_v4i16((LAS s16x4*)p);
    s16x4 b = __builtin_amdgcn_ds_read_tr16_b64_v4i16((LAS s16x4*)(p + off2));
    return (bf16x8){a[0], a[1], a[2], a[3], b[0], b[1], b[2], b[3]};
}
DI int lane_now() { int l; asm volatile("v_mbcnt_lo_u32_b32 %0, -1, 0\n\tv_mbcnt_hi_u32_b32 %0, -1, %0" : "=v"(l)); return l; }
DI float bflo(unsigned u) { return __uint_as_float(u << 16); }
DI float bfhi(unsigned u) { return __uint_as_float(u & 0xffff0000u); }
DI u32x4 swap16(u32x2 a, u32x2 b) {
    auto r0 = __builtin_amdgcn_permlane16_swap(a[0], b[0], false, false);
    auto r1 = __builtin_amdgcn_permlane16_swap(a[1], b[1], false, false);
    return (u32x4){r0[0], r1[0], r0[1], r1[1]};
}
DI u32x2 pack4(f32x4 v) { return (u32x2){cvtpk(v[0], v[1]), cvtpk(v[2], v[3])}; }
typedef __amdgpu_buffer_rsrc_t rsrc_t;
DI void st_wt(rsrc_t r, unsigned off, u32x4 v) { __builtin_amdgcn_raw_buffer_store_b128(v, r, off, 0, 16); }
DI u32x4 ld_dev(rsrc_t r, unsigned off) { return __builtin_amdgcn_raw_buffer_load_b128(r, off, 0, 16); }
DI u32x4 ld_o(rsrc_t r, unsigned off, unsigned soff) { return __builtin_amdgcn_raw_buffer_load_b128(r, off, soff, 16); }
DI void wait_flags(const char* first, int n, u64 lo, u64 span, int lane) {
    const GAS u64* p = (const GAS u64*)(first + (lane < n ? lane : 0) * 64);
#pragma unroll 1
    for (unsigned spins = 0; spins < (1u << 16); ++spins) {
        const u64 v = __hip_atomic_load(p, RLX_AGENT);
        if (__all(v - lo <= span)) break;
        __builtin_amdgcn_s_sleep(1);
    }
}
DI void set_flag(char* p, u64 v, int lane) { if (lane == 0) __hip_atomic_store((GAS u64*)p, v, RLX_AGENT); }

#define ABL_FDMA 0
#define ABL_FMFMA 0
#define ABL_ATT 0
#define PADN 0
#define FN_PAD(g) do { _Pragma("unroll") for (int z_ = 0; z_ < PADN; ++z_) asm volatile("s_nop 3"); SBAR(); } while (0)
#define FN_PRIO 1
#define PF_MODE 0
#define TLB_WARM 0
#define TLB_SUB 16384
#define PF_PRE 1
#define PF_FIRST 0
#define PF_N0 3
#define PF_LEAD 8
#define PF_SLEEP 0
constexpr int K1_STAGE = 53248, K1_A = 0, K1_WD = 32768, K1_WQ = 40960;
constexpr int AT_STAGE = 8192, FB_BASE = 32768, FW_STAGE = 32768, FW_TOP = 131072;
struct K1Frag { bf16x8 x[4], wd[2], wqk[2], wv; };
struct FnFrag { bf16x8 w[4], a[4]; };

DI void attn_store(rsrc_t rs, unsigned off, const f32x16& o, float l) {
    const float rl = 1.0f / l; u32x4 w0, w1;
#pragma unroll
    for (int i = 0; i < 4; ++i) { w0[i] = cvtpk(o[2 * i] * rl, o[2 * i + 1] * rl); w1[i] = cvtpk(o[8 + 2 * i] * rl, o[9 + 2 * i] * rl); }
    st_wt(rs, off, w0); st_wt(rs, off + 256, w1);
}
DI bf16x8 attn_sel(int lane) {
    const short v = (((lane >> 2) & 1) == ((lane >> 4) & 1)) ? (short)0x3f80 : (short)0;
    return (bf16x8){v, v, v, v, v, v, v, v};
}
DI float max16(const f32x16& s) {
    float a = fmaxf(fmaxf(s[0], s[1]), s[2]), b = fmaxf(fmaxf(s[3], s[4]), s[5]);
#pragma unroll
    for (int r = 6; r < 16; r += 4) { a = fmaxf(fmaxf(a, s[r]), s[r + 1]); if (r + 3 < 16) b = fmaxf(fmaxf(b, s[r + 2]), s[r + 3]); }
    return fmaxf(a, b);
}
DI void attn_slow(rsrc_t rs, const char* kv, bf16x8 q0, bf16x8 q1, int lane, unsigned ooff) {
    __builtin_amdgcn_fence(__ATOMIC_ACQUIRE, "agent");
    const bf16x8 sel = attn_sel(lane);
    kv += lane * 16;
    float mx = -INFINITY;
    const f32x16 z = f32x16{};
#pragma unroll 1
    for (int j = 0; j < 64; ++j) {
        const char* kp = kv + (j >> 1) * 8192 + (j & 1) * 2048;
        f32x16 s = MFMA32(*(const bf16x8*)kp, q0, z); s = MFMA32(*(const bf16x8*)(kp + 1024), q1, s);
        mx = fmaxf(mx, max16(s));
    }
    mx = fmaxf(mx, __shfl_xor(mx, 32));
    f32x16 negm, o = f32x16{}; f32x4 l = f32x4{};
#pragma unroll
    for (int r = 0; r < 16; ++r) negm[r] = -mx;
#pragma unroll 1
    for (int j = 0; j < 64; ++j) {
        const char* kp = kv + (j >> 1) * 8192 + (j & 1) * 2048;
        const char* vp = kv + ((j & 1) ? (((j & 7) == 7 ? (j >> 3) * 4 : (j + 1) >> 1) * 8192 + 4096) : ((j >> 1) * 8192 + 6144));
        const bf16x8 v0 = *(const bf16x8*)vp, v1 = *(const bf16x8*)(vp + 1024);
        f32x16 s = MFMA32(*(const bf16x8*)kp, q0, negm); s = MFMA32(*(const bf16x8*)(kp + 1024), q1, s);
        u32x4 p0, p1;
#pragma unroll
        for (int r = 0; r < 16; ++r) s[r] = __builtin_amdgcn_exp2f(s[r]);
#pragma unroll
        for (int r = 0; r < 4; ++r) { p0[r] = cvtpk(s[2 * r], s[2 * r + 1]); p1[r] = cvtpk(s[8 + 2 * r], s[9 + 2 * r]); }
        o = MFMA32(v0, BC(p0), o); l = MFMA16(sel, BC(p0), l);
        o = MFMA32(v1, BC(p1), o); l = MFMA16(sel, BC(p1), l);
    }
    attn_store(rs, ooff, o, l[0]);
}

DI void touch8k(const char* p, unsigned voff, unsigned dump) {
    asm volatile("s_mov_b32 m0, %2\n\ts_nop 0\n\tglobal_load_lds_dword %0, %1" :: "v"(voff), "s"(p), "s"(dump) : "memory"); }
DI void touch_pair(int p, int l, int mt, int hd, const char* x, const char* wdb, const char* wqkv, unsigned dump) {
    const int n = l >= 56 ? l - 8 : l, i = (mt * 2 + (hd & 1)) * 24 + (n - 32);
    const char* a = n < 16 ? x + (size_t)(mt * 256 + hd * 8 + (n >> 1)) * 2048 + p * 256 + (n & 1) * 128
                  : n < 32 ? wdb + (size_t)(mt * 8 + ((n - 16) >> 1)) * 2048 + p * 256 + (n & 1) * 128
                           : wqkv + (size_t)(p * 128 + i / 3) * 6144 + (i % 3) * 2048 + (hd >> 1) * 128;
    asm volatile("s_mov_b32 m0, %1\n\ts_nop 0\n\tglobal_load_lds_dword %0, off" :: "v"(a), "s"(dump) : "memory");
}
DI void main_waves(LAS char* smem, int wid, int lane, int mt, int hd, char* ws, rsrc_t rs, u16* __restrict__ out,
                   const u16* __restrict__ x, const u16* __restrict__ gw, const char* wdb, const char* wqkv, const char* wsb, const char* pf2) {
    unsigned long long dg1 = 0;
    const unsigned dump = (unsigned)(size_t)smem + 3 * K1_STAGE + wid * 256;
    if (wid >= PF_FIRST && wid <= PF_PRE) { __builtin_amdgcn_s_sleep(PF_SLEEP); touch_pair(wid, lane, mt, hd, (const char*)x, wdb, wqkv, dump); }
    const int t_act = wid < 4 ? 7 + 2 * wid : 2 * wid - PF_LEAD;
#define TW(l_) (unsigned)(((hd & 15) * 16 + ((l_) >> 2)) * 4096 + ((l_) & 3) * 128)
    int fr = lane & 15, fq = lane >> 4;
    u64 ep, base; u32x4 gx0, gx1, gw0, gw1;
    {
        const int wm = wid >> 1, wn = wid & 1;
        const int cpart = ((fq ^ (fr >> 1)) & 7) * 16;
        const int aA = K1_A + (64 * wm + fr) * 128 + cpart, aD = K1_WD + (32 * wn + fr) * 128 + cpart;
        const int aT0 = (8 * fq + (fr >> 2)) * 64 + ((((lane & 3) >> 1) ^ ((fq & 1) << 1)) * 16) + (lane & 1) * 8;
        const int aQ = K1_WQ + wn * 4096 + aT0, aV = K1_WQ + 8192 + (aT0 ^ (wn * 32));
        f32x4 accQ[2][4], accV[4], accD[2][4];
#pragma unroll
        for (int m = 0; m < 4; ++m) { accQ[0][m] = f32x4{}; accQ[1][m] = f32x4{}; accV[m] = f32x4{}; accD[0][m] = f32x4{}; accD[1][m] = f32x4{}; }
        K1Frag X, Y;
#define K1_RD(F, i, slot, ks) do { const LAS char* sb_ = smem + (slot); \
        if ((i) == 0) F.wqk[0] = ldstr(sb_ + aQ + (ks) * 2048, 256); \
        else if ((i) == 1) { F.x[0] = ldsr128(sb_ + (aA ^ ((ks) * 64))); } \
        else if ((i) == 2) F.wqk[1] = ldstr(sb_ + (aQ ^ 32) + (ks) * 2048, 256); \
        else if ((i) == 3) F.wv = ldstr(sb_ + aV + (ks) * 2048, 256); \
        else if ((i) == 4) { F.wd[0] = ldsr128(sb_ + (aD ^ ((ks) * 64))); F.wd[1] = ldsr128(sb_ + (aD ^ ((ks) * 64)) + 2048); } \
        else if ((i) < 8) F.x[(i) - 4] = ldsr128(sb_ + (aA ^ ((ks) * 64)) + ((i) - 4) * 2048); } while (0)
#define K1_MF(F, j) do { constexpr int m_ = (j) / 5, r_ = (j) % 5; \
        if (r_ == 0) accQ[0][m_] = MFMA16(F.wqk[0], F.x[m_], accQ[0][m_]); \
        else if (r_ == 1) accQ[1][m_] = MFMA16(F.wqk[1], F.x[m_], accQ[1][m_]); \
        else if (r_ == 2) accV[m_] = MFMA16(F.x[m_], F.wv, accV[m_]); \
        else if (r_ == 3) accD[0][m_] = MFMA16(F.x[m_], F.wd[0], accD[0][m_]); \
        else accD[1][m_] = MFMA16(F.x[m_], F.wd[1], accD[1][m_]); } while (0)
#define K1_G(g, C, R, rslot, ks) do { K1_MF(C, 2 * (g)); if ((g) == 0) SBAR(); K1_RD(R, g, rslot, ks); K1_MF(C, 2 * (g) + 1); SBAR(); } while (0)
#define K1_PHASE(C, R, rslot, ks) do { K1_G(0, C, R, rslot, ks); K1_G(1, C, R, rslot, ks); K1_G(2, C, R, rslot, ks); K1_G(3, C, R, rslot, ks); K1_G(4, C, R, rslot, ks); \
        K1_G(5, C, R, rslot, ks); K1_G(6, C, R, rslot, ks); K1_G(7, C, R, rslot, ks); K1_G(8, C, R, rslot, ks); K1_G(9, C, R, rslot, ks); } while (0)
        asm volatile("" :: "s"(gw), "s"(out));
        __builtin_amdgcn_s_waitcnt(0xC07F);
        LGKM_BAR();
        if (wid == 0) STAMP(0, 1);
        if (wid > PF_PRE && wid <= 3) touch_pair(wid, lane, mt, hd, (const char*)x, wdb, wqkv, dump);
#pragma unroll
        for (int i = 0; i < 8; ++i) K1_RD(X, i, 0, 0);
        int sl = 0, sn = K1_STAGE;
        SBAR();
        for (int t = 0; t < 15; ++t) {
            K1_PHASE(X, Y, sl, 1);
            LGKM_BAR(); SBAR();
            TICK(0, t);
            if (__builtin_expect(t == t_act, 0)) {
                const int l_ = lane_now();
                if (wid < 4) touch8k(wsb + ((mt + wid) & 7) * 512, TW(l_), dump);
                else touch_pair(wid, l_, mt, hd, (const char*)x, wdb, wqkv, dump);
            }
            SBAR();
            K1_PHASE(Y, X, sn, 0);
            sl = sn; sn = (sn == 2 * K1_STAGE) ? 0 : sn + K1_STAGE;
        }
        K1_PHASE(X, Y, 0, 1);
        WAIT_BAR(0); SBAR();
        lane = lane_now();
        ep = __hip_atomic_load((const GAS u64*)ws, RLX_AGENT);
        { const u32x4* px = (const u32x4*)(x + (size_t)(256 * mt + 8 * hd + wid) * DM + lane * 16); const u32x4* pw = (const u32x4*)(gw + lane * 16);
          gx0 = px[0]; gx1 = px[1]; gw0 = pw[0]; gw1 = pw[1]; }
        SBAR();
        K1_MF(Y, 0); K1_MF(Y, 1); K1_MF(Y, 2); K1_MF(Y, 3); K1_MF(Y, 4); K1_MF(Y, 5); K1_MF(Y, 6); K1_MF(Y, 7); K1_MF(Y, 8); K1_MF(Y, 9);
        K1_MF(Y, 10); K1_MF(Y, 11); K1_MF(Y, 12); K1_MF(Y, 13); K1_MF(Y, 14); K1_MF(Y, 15); K1_MF(Y, 16); K1_MF(Y, 17); K1_MF(Y, 18); K1_MF(Y, 19);
        if (wid == 0) STAMP(0, 2);
        fr = lane & 15; fq = lane >> 4;
        const float qs = wn ? 1.0f : 0.17677669529663687f * 1.4426950408889634f;
#pragma unroll
        for (int m = 0; m < 4; ++m) {
            const u32x4 w = swap16(pack4(accQ[0][m] * qs), pack4(accQ[1][m] * qs));
            const int r = (16 * m + fr) & 31, rho = (r & 19) | ((r & 4) << 1) | ((r & 8) >> 1), j = 2 * wm + (m >> 1);
            const int in = (fq & 1) * 1024 + ((fq >> 1) * 32 + rho) * 16;
            ldsw128(smem + (wn ? wm * AT_STAGE + (m >> 1) * 2048 : FW_TOP - 2 * FW_STAGE + j * 2048) + in, w);
        }
        const int rv = 8 * (fr >> 2) + 4 * wn + (fr & 3);
#pragma unroll
        for (int i = 0; i < 2; ++i) {
            const int in = (fq & 1) * 1024 + ((fq >> 1) * 32 + rv) * 16;
            const u32x4 w = swap16(pack4(accV[2 * i]), pack4(accV[2 * i + 1]));
            ldsw128(smem + ((wm + i) & 3) * AT_STAGE + 6144 - i * 2048 + in, w);
#pragma unroll
            for (int n = 0; n < 2; ++n) {
                const u32x4 d = swap16(pack4(accD[n][2 * i]), pack4(accD[n][2 * i + 1]));
                const int el = 32 * wn + 16 * n + fr, ch = 4 * i + 2 * (fq & 1) + (fq >> 1);
                ldsw128(smem + FB_BASE + wm * 8192 + el * 128 + ((ch ^ (fr >> 1)) * 16), d);
            }
        }
        LGKM_BAR();
        if (wid == 0) STAMP(0, 3);
    }
    {
        const int qt = mt * 8 + wid;
        char* f1 = ws + WS_F1 + hd * 512;
        const unsigned rec0 = (hd * 32 + 4 * mt) * 8192 + wid * 1024 + lane * 16;
#define PUBLISH(q) do { const u32x4 a_ = *(const LAS u32x4*)(smem + (q) * 8192 + wid * 1024 + lane * 16), b_ = *(const LAS u32x4*)(smem + FB_BASE + (q) * 8192 + wid * 1024 + lane * 16); \
        st_wt(rs, WS_KV + rec0 + (q) * 8192, a_); st_wt(rs, WS_AB + rec0 + (q) * 8192, b_); } while (0)
        PUBLISH(0); PUBLISH(1);
        const bf16x8 sel = attn_sel(lane);
        const LAS char* fb = smem + lane * 16;
        const bf16x8 q0 = ldsr128(fb + FW_TOP - 2 * FW_STAGE + wid * 2048), q1 = ldsr128(fb + FW_TOP - 2 * FW_STAGE + wid * 2048 + 1024);
        bf16x8 kA0, kA1, kB0, kB1, vA0, vA1, vB0, vB1 = bf16x8{};
        f32x16 negm, o = f32x16{}, sA, sB; f32x4 l = f32x4{};
        u32x4 p0, p1 = u32x4{};
        kA0 = ldsr128(fb); kA1 = ldsr128(fb + 1024); kB0 = ldsr128(fb + 2048); kB1 = ldsr128(fb + 3072); vA0 = ldsr128(fb + 6144); vA1 = ldsr128(fb + 7168);
        SBAR(); LGKM_BAR(); SBAR();
        {
            sA = MFMA32(kA0, q0, o); sA = MFMA32(kA1, q1, sA);
            float mx = max16(sA); mx = fmaxf(mx, __shfl_xor(mx, 32)) + 40.f;
#pragma unroll
            for (int r = 0; r < 16; ++r) { negm[r] = -mx; sA[r] -= mx; }
        }
        {
            float s = 0.f;
#pragma unroll
            for (int i = 0; i < 4; ++i) {
                s = fmaf(bflo(gx0[i]), bflo(gw0[i]), s); s = fmaf(bfhi(gx0[i]), bfhi(gw0[i]), s);
                s = fmaf(bflo(gx1[i]), bflo(gw1[i]), s); s = fmaf(bfhi(gx1[i]), bfhi(gw1[i]), s);
            }
#pragma unroll
            for (int o = 32; o >= 1; o >>= 1) s += __shfl_xor(s, o);
            if (lane == 0) __builtin_amdgcn_raw_buffer_store_b32(__float_as_uint(1.f / (1.f + __expf(-s))), rs, WS_GATE + (256 * mt + 8 * hd + wid) * 4, 0, 16);
        }
        base = (((u64)(unsigned)__builtin_amdgcn_readfirstlane((unsigned)(ep >> 32)) << 32) | (unsigned)__builtin_amdgcn_readfirstlane((unsigned)ep)) + 8;
        if (wid == 0) STAMP(1, 0);
#define EX(S_, i) S_[i] = __builtin_amdgcn_exp2f(S_[i])
#define AT_STEP(SC, SN, K0, K1, VC0, VP1, RD0, RD1, RD2, RD3) do { \
        SN = MFMA32(K0, q0, negm); SBAR(); RD0; EX(SC, 0); EX(SC, 1); EX(SC, 2); EX(SC, 3); SBAR(); \
        o = MFMA32(VP1, BC(p1), o); PIN(o); SBAR(); RD1; EX(SC, 4); EX(SC, 5); EX(SC, 6); EX(SC, 7); p0[0] = cvtpk(SC[0], SC[1]); p0[1] = cvtpk(SC[2], SC[3]); SBAR(); \
        l = MFMA16(sel, BC(p1), l); PIN(l); SBAR(); p0[2] = cvtpk(SC[4], SC[5]); p0[3] = cvtpk(SC[6], SC[7]); EX(SC, 8); SBAR(); \
        SN = MFMA32(K1, q1, SN); SBAR(); RD2; EX(SC, 9); EX(SC, 10); EX(SC, 11); EX(SC, 12); SBAR(); \
        o = MFMA32(VC0, BC(p0), o); PIN(o); SBAR(); RD3; EX(SC, 13); EX(SC, 14); EX(SC, 15); p1[0] = cvtpk(SC[8], SC[9]); SBAR(); \
        l = MFMA16(sel, BC(p0), l); PIN(l); SBAR(); p1[1] = cvtpk(SC[10], SC[11]); p1[2] = cvtpk(SC[12], SC[13]); p1[3] = cvtpk(SC[14], SC[15]); SBAR(); } while (0)
        int sl = AT_STAGE;
#define AT_TICK() do { \
            AT_STEP(sA, sB, kB0, kB1, vA0, vB1, kA0 = ldsr128(fb + sl), kA1 = ldsr128(fb + sl + 1024), vB0 = ldsr128(fb + sl + 4096), vB1 = ldsr128(fb + sl + 5120)); \
            AT_STEP(sB, sA, kA0, kA1, vB0, vA1, kB0 = ldsr128(fb + sl + 2048), kB1 = ldsr128(fb + sl + 3072), vA0 = ldsr128(fb + sl + 6144), vA1 = ldsr128(fb + sl + 7168)); \
            sl = (sl + AT_STAGE) & (4 * AT_STAGE - 1); } while (0)
#pragma unroll 1
        for (int T = 1; T <= 2; ++T) {
            asm volatile("s_waitcnt vmcnt(0)" ::: "memory");
            if (wid == 1 && T == 2) wait_flags(f1, 8, base + 1, 1, lane);
            LGKM_BAR(); SBAR();
            TICK(1, T);
            if (wid == 0) set_flag(f1 + mt * 64, base + T, lane);
            if (T == 1) { PUBLISH(2); PUBLISH(3); }
            SBAR();
            AT_TICK();
        }
        LGKM_BAR(); SBAR();
        {
            const u64 fv = __hip_atomic_load((const GAS u64*)(f1 + (lane & 7) * 64), RLX_AGENT);
            SBAR();
            AT_TICK();
            if (wid == 1 && !__all(fv == base + 2)) wait_flags(f1, 8, base + 2, 0, lane);
        }
        const int t_touch = wid >= 4 ? 4 * wid - 8 : 0;
#pragma unroll 1
        for (int T = 4; T <= 32; ++T) {
            LGKM_BAR(); SBAR();
            TICK(1, T);
            if (__builtin_expect(T == t_touch, 0)) {
                asm volatile("global_load_dword v167, %0, %1" :: "v"(TW(lane)), "s"(wsb + ((mt + wid) & 7) * 512) : "memory", "v167");
                if (wid == 7) asm volatile("global_load_dword v167, %0, %1" :: "v"(lane * 128), "s"(pf2) : "memory", "v167");
            }
            SBAR();
            AT_TICK();
        }
        o = MFMA32(vB1, BC(p1), o); l = MFMA16(sel, BC(p1), l);
        if (wid == 0) STAMP(1, 1);
        bool bad = !(l[0] >= 7.8886e-31f && l[0] <= 3.0e38f);
#pragma unroll
        for (int r = 0; r < 16; ++r) bad |= !(fabsf(o[r]) <= 3.0e38f);
        const int r = lane & 31, qrow = qt * 32 + ((r & 19) | ((r & 4) << 1) | ((r & 8) >> 1));
        const unsigned ooff = WS_O + ((qrow >> 4) * 32 + hd) * 1024 + ((lane >> 5) * 32 + (qrow & 15)) * 16;
        if (!ABL_ATT && !ABL_FDMA && __builtin_expect(__any(bad), 0)) attn_slow(rs, ws + WS_KV + hd * 262144, q0, q1, lane, ooff);
        else attn_store(rs, ooff, o, l[0]);
        WAIT_BAR(0);
        if (wid == 0) STAMP(1, 2);
    }
    {
        const int rt = 2 * (blockIdx.x & 7) + (blockIdx.x >> 7), nb = ((blockIdx.x >> 3) & 15) * 64;
        const int kq = fr >> 2, swz = ((kq >> 1) & 1) | ((fq & 1) << 1);
        const int aW = (8 * fq + kq) * 128 + ((((lane & 3) >> 1) ^ (swz << 1)) * 16) + (lane & 1) * 8;
        const int kh = wid & 1;
        const int rot = (blockIdx.x >> 3) & 15;
#define P3_KS(ks_) (((ks_) + rot) & 15)
#define P3_RD(W_, ks_) do { const int wb_ = FB_BASE + (16 * kh + P3_KS(ks_)) * 4096; \
            _Pragma("unroll") for (int n_ = 0; n_ < 4; ++n_) W_[n_] = ldstr(smem + wb_ + (aW ^ (n_ * 32)), 512); } while (0)
        bf16x8 wf[2][4];
        P3_RD(wf[0], 0);
        if (wid == 0) {
            set_flag(ws + WS_F2 + (mt * 32 + hd) * 64, base + 5, lane);
            wait_flags(ws + WS_F2 + (rt >> 1) * 2048, 32, base + 5, 0, lane);
            __builtin_amdgcn_fence(__ATOMIC_ACQUIRE, "wavefront");
            if (blockIdx.x == 0) set_flag(ws, base, lane);
            STAMP(1, 3);
        }
        LGKM_BAR();
        const int R0 = rt * 128 + 16 * wid;
        const int t = R0 + fr, col = nb + 16 * (fq & 1) + 8 * (fq >> 1);
        const unsigned ob0 = WS_O + ((R0 >> 4) * 32 + 16 * kh) * 1024 + lane * 16, ob1 = WS_O + (((R0 >> 4) ^ 1) * 32 + 16 * kh) * 1024 + lane * 16;
        u32x4 of[10][2], y0[2], y1[2]; float g;
#pragma unroll
        for (int ks = 0; ks < 10; ++ks) { of[ks][0] = ld_o(rs, ob0, P3_KS(ks) * 1024); of[ks][1] = ld_o(rs, ob1, P3_KS(ks) * 1024); }
        SBAR();
        f32x4 acc[4], acp[4];
#pragma unroll
        for (int n = 0; n < 4; ++n) { acc[n] = f32x4{}; acp[n] = f32x4{}; }
#pragma unroll
        for (int ks = 0; ks < 16; ++ks) {
            if (ks + 1 < 16) P3_RD(wf[(ks + 1) & 1], ks + 1);
            SBAR();
#pragma unroll
            for (int n = 0; n < 4; ++n) acc[n] = MFMA16(wf[ks & 1][n], BC(of[ks % 10][0]), acc[n]);
#pragma unroll
            for (int n = 0; n < 4; ++n) acp[n] = MFMA16(wf[ks & 1][n], BC(of[ks % 10][1]), acp[n]);
            if (ks < 6) { of[ks][0] = ld_o(rs, ob0, P3_KS(10 + ks) * 1024); of[ks][1] = ld_o(rs, ob1, P3_KS(10 + ks) * 1024); }
            if (ks == 8) {
                g = __uint_as_float(__builtin_amdgcn_raw_buffer_load_b32(rs, WS_GATE + t * 4, 0, 16));
#pragma unroll
                for (int i = 0; i < 2; ++i) { const unsigned yo = WS_Y + ((R0 >> 4) * 16 + rot) * 2048 + i * 1024 + lane * 16; y0[i] = ld_dev(rs, yo); y1[i] = ld_dev(rs, yo + 4 * MB); }
            }
            SBAR();
        }
#pragma unroll
        for (int n = 0; n < 4; ++n) *(LAS f32x4*)(smem + wid * 4096 + n * 1024 + lane * 16) = acp[n];
        LGKM_BAR();
#pragma unroll
        for (int n = 0; n < 4; ++n) acc[n] += *(const LAS f32x4*)(smem + (wid ^ 1) * 4096 + n * 1024 + lane * 16);
        const float g1 = 1.f - g;
#pragma unroll
        for (int i = 0; i < 2; ++i) {
            float v[8];
#pragma unroll
            for (int j = 0; j < 4; ++j) {
                auto r = __builtin_amdgcn_permlane16_swap(__float_as_uint(acc[2 * i][j]), __float_as_uint(acc[2 * i + 1][j]), false, false);
                v[j] = __uint_as_float(r[0]); v[4 + j] = __uint_as_float(r[1]);
            }
            u32x4 w;
#pragma unroll
            for (int j = 0; j < 4; ++j)
                w[j] = cvtpk(g * (bflo(y0[i][j]) - bflo(y1[i][j])) + g1 * v[2 * j], g * (bfhi(y0[i][j]) - bfhi(y1[i][j])) + g1 * v[2 * j + 1]);
            *(u32x4*)(out + (size_t)t * DM + col + 32 * i) = w;
        }
        if (wid == 0) STAMP(1, 4);
        if (wid == 0) { while (__builtin_amdgcn_s_memrealtime() - dg1 < 10000ull) __builtin_amdgcn_s_sleep(1); }
    }
}

DI void side_waves(LAS char* smem, int lw, int lane, int mt, int hd, const u16* __restrict__ x, const u16* __restrict__ wsr, const u16* __restrict__ wsi,
                   const u16* __restrict__ wdr, const u16* __restrict__ wdi, const u16* __restrict__ wqkv, const u16* __restrict__ wo,
                   const u16* __restrict__ gw, char* ws, rsrc_t rs) {
    const unsigned lds0 = (unsigned)(size_t)smem + lw * 1024;
    const unsigned rowl = 8 * lw + (lane >> 3), chk = ((lane & 7) ^ ((4 * lw + (lane >> 4)) & 7)) * 16;
    const int c = hd >> 4;
    const char* wb = (const char*)(c ? wsi : wsr) + (size_t)mt * 256 * 4096;
    unsigned vo[10];
    {
        const char* xb = (const char*)x + (size_t)mt * 256 * 2048;
        const char* wdb = (const char*)(c ? wdi : wdr) + (size_t)(hd & 15) * 64 * 2048;
        const char* wqb = (const char*)wqkv + hd * 64 + lw * 16 * 6144;
#pragma unroll
        for (int i = 0; i < 8; ++i) vo[i] = (rowl + 32 * i) * 2048 + chk;
        const unsigned voffW = (lane >> 2) * 6144 + (((lane & 3) ^ ((lane >> 5) << 1)) * 16);
#define K1_DMA(t, slot) do { const char* xa_ = xb + (size_t)(t) * 128; const char* wd_ = wdb + (size_t)(t) * 128; const char* wq_ = wqb + (size_t)(t) * 64 * 6144; \
        _Pragma("unroll") for (int u_ = 0; u_ < 3; ++u_) dma(voffW, wq_ + u_ * 2048, lds0 + (slot) + K1_WQ + u_ * 4096); \
        _Pragma("unroll") for (int i_ = 0; i_ < 2; ++i_) dma(vo[i_], wd_, lds0 + (slot) + K1_WD + i_ * 4096); \
        _Pragma("unroll") for (int i_ = 0; i_ < 8; ++i_) dma(vo[i_], xa_, lds0 + (slot) + K1_A + i_ * 4096); } while (0)
        K1_DMA(0, 0); K1_DMA(1, K1_STAGE);
        WAIT_BAR(13);
        K1_DMA(2, 2 * K1_STAGE);
        int sl = 0;
        for (int t = 0; t < 13; ++t) {
            WAIT_BAR(13);
            K1_DMA(t + 3, sl);
            sl = (sl == 2 * K1_STAGE) ? 0 : sl + K1_STAGE;
        }
        WAIT_BAR(13);
        WAIT_BAR(0);
#pragma unroll
        for (int i = 0; i < 8; ++i) vo[i] = (rowl + 32 * i) * 4096 + chk;
        vo[8] = lane * 16 + lw * 1024; vo[9] = vo[8] + 4096;
#pragma unroll
        for (int i = 0; i < 7; ++i) dma(vo[i], wb + (size_t)((4 * mt) & 31) * 128, lds0 + FW_TOP + i * 4096);
#pragma unroll
        for (int i = 0; i < 8; ++i) dma(vo[i], wb + (size_t)((4 * mt + 1) & 31) * 128, lds0 + FW_TOP - FW_STAGE + i * 4096);
        LGKM_BAR();
        dma(vo[7], wb + (size_t)((4 * mt) & 31) * 128, lds0 + FW_TOP + 7 * 4096);
        WAIT_BAR(0);
    }
    {
        const char* kvb = ws + WS_KV + hd * 262144;
        const unsigned vl = lane * 16 + lw * 1024;
        const char *bW = wb, *bA = ws + WS_AB + hd * 262144; int kmask = 31, kstep = 128, kstepA = 8192, koff = 4 * mt;
#define FN_DMA_P(p, s, fslot, aslot) do { \
        if ((p) < 8) dma(vo[p], bW + (size_t)((koff + (s)) & kmask) * kstep, lds0 + (fslot) + (p) * 4096); \
        else if ((p) < 10) dma_dev(vo[p], bA + (size_t)((koff + (s)) & kmask) * kstepA, lds0 + FB_BASE + (aslot) + ((p) - 8) * 4096); \
        else if ((p) == 10) dma_dev(vl, kvb + ((4 * mt + (s)) & 31) * 8192, lds0 + (aslot)); \
        else dma_dev(vl, kvb + ((4 * mt + (s) - ((((s) & 3) == 0 && lw < 2) ? 4 : 0)) & 31) * 8192 + 4096, lds0 + (aslot) + 4096); } while (0)
        const int fr = lane & 15, fq = lane >> 4;
        const int cpart = ((fq ^ (fr >> 1)) & 7) * 16;
        const int aW = (64 * lw + fr) * 128 + cpart, aA = FB_BASE + fr * 128 + cpart;
        f32x4 acc[4][4];
#pragma unroll
        for (int m = 0; m < 4; ++m)
#pragma unroll
            for (int n = 0; n < 4; ++n) acc[m][n] = f32x4{};
        FnFrag X, Yf;
#define FN_RD(F, i, slot, bslot, ks) do { \
        if ((i) == 0) F.w[0] = ldsr128(smem + (slot) + (aW ^ ((ks) * 64))); \
        else if ((i) < 5) F.a[(i) - 1] = ldsr128(smem + (bslot) + (aA ^ ((ks) * 64)) + ((i) - 1) * 2048); \
        else F.w[(i) - 4] = ldsr128(smem + (slot) + (aW ^ ((ks) * 64)) + ((i) - 4) * 2048); } while (0)
#define FN_MF(F, j) do { if (!ABL_FMFMA) acc[(j) >> 2][(j) & 3] = MFMA16(F.a[(j) & 3], F.w[(j) >> 2], acc[(j) >> 2][(j) & 3]); else asm volatile("" :: "v"(F.a[(j) & 3]), "v"(F.w[(j) >> 2])); } while (0)
#define FN_G(g, C, R, rslot, rb, ks, P0, s, fslot, aslot) do { FN_MF(C, 2 * (g)); FN_RD(R, g, rslot, rb, ks); FN_MF(C, 2 * (g) + 1); \
        if (!ABL_FDMA && ((g) & 3) != 0 && (P0) + (g) - 1 - ((g) >> 2) < PMAX) FN_DMA_P((P0) + (g) - 1 - ((g) >> 2), s, fslot, aslot); SBAR(); FN_PAD(g); } while (0)
#define FN_PHASE(C, R, rslot, rb, ks, P0, s, fslot, aslot) do { FN_G(0, C, R, rslot, rb, ks, P0, s, fslot, aslot); FN_G(1, C, R, rslot, rb, ks, P0, s, fslot, aslot); \
        FN_G(2, C, R, rslot, rb, ks, P0, s, fslot, aslot); FN_G(3, C, R, rslot, rb, ks, P0, s, fslot, aslot); FN_G(4, C, R, rslot, rb, ks, P0, s, fslot, aslot); \
        FN_G(5, C, R, rslot, rb, ks, P0, s, fslot, aslot); FN_G(6, C, R, rslot, rb, ks, P0, s, fslot, aslot); FN_G(7, C, R, rslot, rb, ks, P0, s, fslot, aslot); } while (0)
#define PMAX 12
        __builtin_amdgcn_s_setprio(FN_PRIO);
#pragma unroll
        for (int i = 0; i < 8; ++i) FN_RD(X, i, FW_TOP, 0, 0);
        LGKM_BAR();
        int f0 = FW_TOP, f1 = FW_TOP - FW_STAGE, f2 = FW_TOP - 2 * FW_STAGE;
        FN_DMA_P(0, 2, f2, 0); FN_DMA_P(1, 2, f2, 0); FN_DMA_P(2, 2, f2, 0); FN_DMA_P(3, 2, f2, 0); FN_DMA_P(4, 2, f2, 0); FN_DMA_P(5, 2, f2, 0);
        SBAR();
#define FN_ITER(t, WAITN) do { \
            FN_PHASE(X, Yf, f0, ((t) & 3) * 8192, 1, 6, (t) + 2, f2, (((t) + 2) & 3) * 8192);       \
            WAIT_BAR(WAITN); SBAR();                                                                \
            FN_SWITCH(t); \
            FN_PHASE(Yf, X, f1, (((t) + 1) & 3) * 8192, 0, 0, (t) + 3, f0, 0);                      \
            const int ft = f0; f0 = f1; f1 = f2; f2 = ft; } while (0)
#define FN_SWITCH(t)
#undef PMAX
#define PMAX 8
        FN_ITER(0, 8); FN_ITER(1, 8);
#undef PMAX
#define PMAX 12
#undef FN_SWITCH
#define FN_SWITCH(t) do { if (__builtin_expect((t) == 29, 0)) { bA = (const char*)wo + ((blockIdx.x >> 3) & 15) * 128; bW = bA + 256 * 2048; kmask = 3; kstep = 512 * 2048; kstepA = 64 * 2048; koff = 0; \
                const unsigned cw = ((lane & 7) ^ ((((lane >> 4) & 1) | ((lw & 1) << 1)) << 1)) * 16; \
                _Pragma("unroll") for (int i = 0; i < 10; ++i) vo[i] = (rowl + 32 * (i & 7)) * 2048 + cw; } SBAR(); } while (0)
        for (int t = 2; t < 31; ++t) FN_ITER(t, 12);
#undef PMAX
#define PMAX 10
        FN_PHASE(X, Yf, f0, 3 * 8192, 1, 6, 33, f2, 8192);
        WAIT_BAR(10); SBAR();
#pragma unroll
        for (int i = 0; i < 8; ++i) dma(vo[i], bA + (size_t)512 * 2048, lds0 + FW_TOP - FW_STAGE + i * 4096);
#pragma unroll
        for (int i = 0; i < 4; ++i) dma(vo[i], bA + (size_t)128 * 2048, lds0 + FB_BASE + 2 * 8192 + i * 4096);
#pragma unroll
        for (int j = 0; j < 16; ++j) FN_MF(Yf, j);
        const unsigned yb = WS_Y + c * 4 * MB + ((mt * 16 + 4 * lw) * 16 + (hd & 15)) * 2048 + lane * 16;
#pragma unroll
        for (int m = 0; m < 4; ++m)
#pragma unroll
            for (int i = 0; i < 2; ++i) st_wt(rs, yb + m * 32768 + i * 1024, swap16(pack4(acc[m][2 * i]), pack4(acc[m][2 * i + 1])));
        WAIT_BAR(0);
    }
}

__global__ __launch_bounds__(768, 3) void fused_kernel(const u16* __restrict__ x, const u16* __restrict__ wsr, const u16* __restrict__ wsi,
                                                        const u16* __restrict__ wdr, const u16* __restrict__ wdi, const u16* __restrict__ wqkv,
                                                        const u16* __restrict__ wo, const u16* __restrict__ gw, u16* __restrict__ out, char* ws) {
    __shared__ __attribute__((aligned(1024))) char smem_[163840];
    LAS char* smem = (LAS char*)smem_;
    const int tid = threadIdx.x, lane = tid & 63, wid = __builtin_amdgcn_readfirstlane(tid >> 6);
    const int xcd = blockIdx.x & 7, idx = blockIdx.x >> 3;
    const int hd = xcd * 4 + (idx >> 3), mt = idx & 7;
    const rsrc_t rs = __builtin_amdgcn_make_buffer_rsrc(ws, 0, WS_END, 0x00020000);

    if (wid < 8) {
        const char* wdb = (const char*)((hd >> 4) ? wdi : wdr) + (size_t)(hd & 15) * 64 * 2048;
        const char* wsb = (const char*)((hd >> 4) ? wsi : wsr) + (size_t)mt * 256 * 4096;
        const char* pf2 = (const char*)wo + (size_t)blockIdx.x * 8192;
        main_waves(smem, wid, lane, mt, hd, ws, rs, out, x, gw, wdb, (const char*)wqkv, wsb, pf2);
    }
    else side_waves(smem, wid - 8, lane, mt, hd, x, wsr, wsi, wdr, wdi, wqkv, wo, gw, ws, rs);
}

extern "C" void kernel_launch(void* const* d_in, const int* in_sizes, int n_in, void* d_out, int out_size, void* d_ws, size_t ws_size, hipStream_t stream) {
    const u16* x = (const u16*)d_in[0]; const u16* wsr = (const u16*)d_in[1]; const u16* wsi = (const u16*)d_in[2];
    const u16* wdr = (const u16*)d_in[3]; const u16* wdi = (const u16*)d_in[4]; const u16* wqkv = (const u16*)d_in[6];
    const u16* wo = (const u16*)d_in[7]; const u16* gw = (const u16*)d_in[8];
    fused_kernel<<<256, 768, 0, stream>>>(x, wsr, wsi, wdr, wdi, wqkv, wo, gw, (u16*)d_out, (char*)d_ws);
}
```

```cpp
#define OUT_AUX 0
#include <hip/hip_runtime.h>
#include <hip/hip_bf16.h>

typedef unsigned short u16;
typedef short bf16x8 __attribute__((ext_vector_type(8)));
typedef short s16x4 __attribute__((ext_vector_type(4)));
typedef float f32x2 __attribute__((ext_vector_type(2)));
typedef float f32x4 __attribute__((ext_vector_type(4)));
typedef float f32x16 __attribute__((ext_vector_type(16)));
typedef unsigned u32x4 __attribute__((ext_vector_type(4)));
typedef unsigned u32x2 __attribute__((ext_vector_type(2)));
typedef __bf16 bf16x2_t __attribute__((ext_vector_type(2)));
#define LAS __attribute__((address_space(3)))
#define GAS __attribute__((address_space(1)))
#define DI __device__ __forceinline__
#define SBAR() __builtin_amdgcn_sched_barrier(0)
#define PIN(x) asm volatile("" : "+v"(x))
#define MFMA16(a, b, c) __builtin_amdgcn_mfma_f32_16x16x32_bf16(a, b, c, 0, 0, 0)
#define MFMA32(a, b, c) __builtin_amdgcn_mfma_f32_32x32x16_bf16(a, b, c, 0, 0, 0)
#define WAIT_BAR(N) asm volatile("s_waitcnt vmcnt(" #N ") lgkmcnt(0)\n\ts_barrier" ::: "memory")
#define LGKM_BAR() asm volatile("s_waitcnt lgkmcnt(0)\n\ts_barrier" ::: "memory")
#define RLX_AGENT __ATOMIC_RELAXED, __HIP_MEMORY_SCOPE_AGENT
#define BC(x) __builtin_bit_cast(bf16x8, x)

constexpr int SEQ = 2048, DM = 1024;
constexpr unsigned MB = 1u << 20;
constexpr unsigned WS_F1 = 4096, WS_F2 = 4096 + 16384, WS_GATE = 65536, WS_KV = 1 * MB, WS_AB = 9 * MB, WS_O = 17 * MB, WS_Y = 21 * MB, WS_END = 29 * MB;
typedef unsigned long long u64;

DI unsigned cvtpk(float lo, float hi) { f32x2 v = {lo, hi}; bf16x2_t b = __builtin_convertvector(v, bf16x2_t); return __builtin_bit_cast(unsigned, b); }
DI void dma(unsigned voff, const char* sbase, unsigned l) {
    asm volatile("s_mov_b32 m0, %2\n\ts_nop 0\n\tglobal_load_lds_dwordx4 %0, %1" :: "v"(voff), "s"(sbase), "s"(l) : "memory"); }
DI void dma_dev(unsigned voff, const char* sbase, unsigned l) {
    asm volatile("s_mov_b32 m0, %2\n\ts_nop 0\n\tglobal_load_lds_dwordx4 %0, %1 sc1" :: "v"(voff), "s"(sbase), "s"(l) : "memory"); }
DI void ldsw128(LAS char* p, u32x4 v) { *(LAS u32x4*)p = v; }
DI bf16x8 ldsr128(const LAS char* p) { return *(const LAS bf16x8*)p; }
DI bf16x8 ldstr(const LAS char* p, int off2) {
    s16x4 a = __builtin_amdgcn_ds_read_tr16_b64_v4i16((LAS s16x4*)p);
    s16x4 b = __builtin_amdgcn_ds_read_tr16_b64_v4i16((LAS s16x4*)(p + off2));
    return (bf16x8){a[0], a[1], a[2], a[3], b[0], b[1], b[2], b[3]};
}
DI int lane_now() { int l; asm volatile("v_mbcnt_lo_u32_b32 %0, -1, 0\n\tv_mbcnt_hi_u32_b32 %0, -1, %0" : "=v"(l)); return l; }
DI float bflo(unsigned u) { return __uint_as_float(u << 16); }
DI float bfhi(unsigned u) { return __uint_as_float(u & 0xffff0000u); }
DI u32x4 swap16(u32x2 a, u32x2 b) {
    auto r0 = __builtin_amdgcn_permlane16_swap(a[0], b[0], false, false);
    auto r1 = __builtin_amdgcn_permlane16_swap(a[1], b[1], false, false);
    return (u32x4){r0[0], r1[0], r0[1], r1[1]};
}
DI u32x2 pack4(f32x4 v) { return (u32x2){cvtpk(v[0], v[1]), cvtpk(v[2], v[3])}; }
typedef __amdgpu_buffer_rsrc_t rsrc_t;
DI void st_wt(rsrc_t r, unsigned off, u32x4 v) { __builtin_amdgcn_raw_buffer_store_b128(v, r, off, 0, 16); }
DI u32x4 ld_dev(rsrc_t r, unsigned off) { return __builtin_amdgcn_raw_buffer_load_b128(r, off, 0, 16); }
DI u32x4 ld_o(rsrc_t r, unsigned off, unsigned soff) { return __builtin_amdgcn_raw_buffer_load_b128(r, off, soff, 16); }
DI void wait_flags(const char* first, int n, u64 lo, u64 span, int lane) {
    const GAS u64* p = (const GAS u64*)(first + (lane < n ? lane : 0) * 64);
#pragma unroll 1
    for (unsigned spins = 0; spins < (1u << 16); ++spins) {
        const u64 v = __hip_atomic_load(p, RLX_AGENT);
        if (__all(v - lo <= span)) break;
        __builtin_amdgcn_s_sleep(1);
    }
}
DI void set_flag(char* p, u64 v, int lane) { if (lane == 0) __hip_atomic_store((GAS u64*)p, v, RLX_AGENT); }

constexpr int K1_STAGE = 53248, K1_A = 0, K1_WD = 32768, K1_WQ = 40960;
constexpr int AT_STAGE = 8192, FB_BASE = 32768, FW_STAGE = 32768, FW_TOP = 131072;
struct K1Frag { bf16x8 x[4], wd[2], wqk[2], wv; };
struct FnFrag { bf16x8 w[4], a[4]; };

DI void attn_store(rsrc_t rs, unsigned off, const f32x16& o, float l) {
    const float rl = 1.0f / l; u32x4 w0, w1;
#pragma unroll
    for (int i = 0; i < 4; ++i) { w0[i] = cvtpk(o[2 * i] * rl, o[2 * i + 1] * rl); w1[i] = cvtpk(o[8 + 2 * i] * rl, o[9 + 2 * i] * rl); }
    st_wt(rs, off, w0); st_wt(rs, off + 256, w1);
}
DI bf16x8 attn_sel(int lane) {
    const short v = (((lane >> 2) & 1) == ((lane >> 4) & 1)) ? (short)0x3f80 : (short)0;
    return (bf16x8){v, v, v, v, v, v, v, v};
}
DI float max16(const f32x16& s) {
    float a = fmaxf(fmaxf(s[0], s[1]), s[2]), b = fmaxf(fmaxf(s[3], s[4]), s[5]);
#pragma unroll
    for (int r = 6; r < 16; r += 4) { a = fmaxf(fmaxf(a, s[r]), s[r + 1]); if (r + 3 < 16) b = fmaxf(fmaxf(b, s[r + 2]), s[r + 3]); }
    return fmaxf(a, b);
}
DI void attn_slow(rsrc_t rs, const char* kv, bf16x8 q0, bf16x8 q1, int lane, unsigned ooff) {
    __builtin_amdgcn_fence(__ATOMIC_ACQUIRE, "agent");
    const bf16x8 sel = attn_sel(lane);
    kv += lane * 16;
    float mx = -INFINITY;
    const f32x16 z = f32x16{};
#pragma unroll 1
    for (int j = 0; j < 64; ++j) {
        const char* kp = kv + (j >> 1) * 8192 + (j & 1) * 2048;
        f32x16 s = MFMA32(*(const bf16x8*)kp, q0, z); s = MFMA32(*(const bf16x8*)(kp + 1024), q1, s);
        mx = fmaxf(mx, max16(s));
    }
    mx = fmaxf(mx, __shfl_xor(mx, 32));
    f32x16 negm, o = f32x16{}; f32x4 l = f32x4{};
#pragma unroll
    for (int r = 0; r < 16; ++r) negm[r] = -mx;
#pragma unroll 1
    for (int j = 0; j < 64; ++j) {
        const char* kp = kv + (j >> 1) * 8192 + (j & 1) * 2048;
        const char* vp = kv + ((j & 1) ? (((j & 7) == 7 ? (j >> 3) * 4 : (j + 1) >> 1) * 8192 + 4096) : ((j >> 1) * 8192 + 6144));
        const bf16x8 v0 = *(const bf16x8*)vp, v1 = *(const bf16x8*)(vp + 1024);
        f32x16 s = MFMA32(*(const bf16x8*)kp, q0, negm); s = MFMA32(*(const bf16x8*)(kp + 1024), q1, s);
        u32x4 p0, p1;
#pragma unroll
        for (int r = 0; r < 16; ++r) s[r] = __builtin_amdgcn_exp2f(s[r]);
#pragma unroll
        for (int r = 0; r < 4; ++r) { p0[r] = cvtpk(s[2 * r], s[2 * r + 1]); p1[r] = cvtpk(s[8 + 2 * r], s[9 + 2 * r]); }
        o = MFMA32(v0, BC(p0), o); l = MFMA16(sel, BC(p0), l);
        o = MFMA32(v1, BC(p1), o); l = MFMA16(sel, BC(p1), l);
    }
    attn_store(rs, ooff, o, l[0]);
}

DI void touch8k(const char* p, unsigned voff, unsigned dump) {
    asm volatile("s_mov_b32 m0, %2\n\ts_nop 0\n\tglobal_load_lds_dword %0, %1" :: "v"(voff), "s"(p), "s"(dump) : "memory"); }
DI void touch_pair(int p, int l, int mt, int hd, const char* x, const char* wdb, const char* wqkv, unsigned dump) {
    const int n = l >= 56 ? l - 8 : l, i = (mt * 2 + (hd & 1)) * 24 + (n - 32);
    const char* a = n < 16 ? x + (size_t)(mt * 256 + hd * 8 + (n >> 1)) * 2048 + p * 256 + (n & 1) * 128
                  : n < 32 ? wdb + (size_t)(mt * 8 + ((n - 16) >> 1)) * 2048 + p * 256 + (n & 1) * 128
                           : wqkv + (size_t)(p * 128 + i / 3) * 6144 + (i % 3) * 2048 + (hd >> 1) * 128;
    asm volatile("s_mov_b32 m0, %1\n\ts_nop 0\n\tglobal_load_lds_dword %0, off" :: "v"(a), "s"(dump) : "memory");
}
DI void main_waves(LAS char* smem, int wid, int lane, int mt, int hd, char* ws, rsrc_t rs, u16* __restrict__ out,
                   const u16* __restrict__ x, const u16* __restrict__ gw, const char* wdb, const char* wqkv, const char* wsb, const char* pf2) {
    const unsigned dump = (unsigned)(size_t)smem + 3 * K1_STAGE + wid * 256;
    if (wid <= 1) touch_pair(wid, lane, mt, hd, (const char*)x, wdb, wqkv, dump);
    const int t_act = wid < 4 ? 7 + 2 * wid : 2 * wid - 8;
#define TW(l_) (unsigned)(((hd & 15) * 16 + ((l_) >> 2)) * 4096 + ((l_) & 3) * 128)
    int fr = lane & 15, fq = lane >> 4;
    u64 ep, base; u32x4 gx0, gx1, gw0, gw1;
    {
        const int wm = wid >> 1, wn = wid & 1;
        const int cpart = ((fq ^ (fr >> 1)) & 7) * 16;
        const int aA = K1_A + (64 * wm + fr) * 128 + cpart, aD = K1_WD + (32 * wn + fr) * 128 + cpart;
        const int aT0 = (8 * fq + (fr >> 2)) * 64 + ((((lane & 3) >> 1) ^ ((fq & 1) << 1)) * 16) + (lane & 1) * 8;
        const int aQ = K1_WQ + wn * 4096 + aT0, aV = K1_WQ + 8192 + (aT0 ^ (wn * 32));
        f32x4 accQ[2][4], accV[4], accD[2][4];
#pragma unroll
        for (int m = 0; m < 4; ++m) { accQ[0][m] = f32x4{}; accQ[1][m] = f32x4{}; accV[m] = f32x4{}; accD[0][m] = f32x4{}; accD[1][m] = f32x4{}; }
        K1Frag X, Y;
#define K1_RD(F, i, slot, ks) do { const LAS char* sb_ = smem + (slot); \
        if ((i) == 0) F.wqk[0] = ldstr(sb_ + aQ + (ks) * 2048, 256); \
        else if ((i) == 1) { F.x[0] = ldsr128(sb_ + (aA ^ ((ks) * 64))); } \
        else if ((i) == 2) F.wqk[1] = ldstr(sb_ + (aQ ^ 32) + (ks) * 2048, 256); \
        else if ((i) == 3) F.wv = ldstr(sb_ + aV + (ks) * 2048, 256); \
        else if ((i) == 4) { F.wd[0] = ldsr128(sb_ + (aD ^ ((ks) * 64))); F.wd[1] = ldsr128(sb_ + (aD ^ ((ks) * 64)) + 2048); } \
        else if ((i) < 8) F.x[(i) - 4] = ldsr128(sb_ + (aA ^ ((ks) * 64)) + ((i) - 4) * 2048); } while (0)
#define K1_MF(F, j) do { constexpr int m_ = (j) / 5, r_ = (j) % 5; \
        if (r_ == 0) accQ[0][m_] = MFMA16(F.wqk[0], F.x[m_], accQ[0][m_]); \
        else if (r_ == 1) accQ[1][m_] = MFMA16(F.wqk[1], F.x[m_], accQ[1][m_]); \
        else if (r_ == 2) accV[m_] = MFMA16(F.x[m_], F.wv, accV[m_]); \
        else if (r_ == 3) accD[0][m_] = MFMA16(F.x[m_], F.wd[0], accD[0][m_]); \
        else accD[1][m_] = MFMA16(F.x[m_], F.wd[1], accD[1][m_]); } while (0)
#define K1_G(g, C, R, rslot, ks) do { K1_MF(C, 2 * (g)); if ((g) == 0) SBAR(); K1_RD(R, g, rslot, ks); K1_MF(C, 2 * (g) + 1); SBAR(); } while (0)
#define K1_PHASE(C, R, rslot, ks) do { K1_G(0, C, R, rslot, ks); K1_G(1, C, R, rslot, ks); K1_G(2, C, R, rslot, ks); K1_G(3, C, R, rslot, ks); K1_G(4, C, R, rslot, ks); \
        K1_G(5, C, R, rslot, ks); K1_G(6, C, R, rslot, ks); K1_G(7, C, R, rslot, ks); K1_G(8, C, R, rslot, ks); K1_G(9, C, R, rslot, ks); } while (0)
        asm volatile("" :: "s"(gw), "s"(out));
        __builtin_amdgcn_s_waitcnt(0xC07F);
        LGKM_BAR();
        if (wid == 2 || wid == 3) touch_pair(wid, lane, mt, hd, (const char*)x, wdb, wqkv, dump);
#pragma unroll
        for (int i = 0; i < 8; ++i) K1_RD(X, i, 0, 0);
        int sl = 0, sn = K1_STAGE;
        SBAR();
        for (int t = 0; t < 15; ++t) {
            K1_PHASE(X, Y, sl, 1);
            LGKM_BAR(); SBAR();
            if (__builtin_expect(t == t_act, 0)) {
                const int l_ = lane_now();
                if (wid < 4) touch8k(wsb + ((mt + wid) & 7) * 512, TW(l_), dump);
                else touch_pair(wid, l_, mt, hd, (const char*)x, wdb, wqkv, dump);
            }
            SBAR();
            K1_PHASE(Y, X, sn, 0);
            sl = sn; sn = (sn == 2 * K1_STAGE) ? 0 : sn + K1_STAGE;
        }
        K1_PHASE(X, Y, 0, 1);
        WAIT_BAR(0); SBAR();
        lane = lane_now();
        ep = __hip_atomic_load((const GAS u64*)ws, RLX_AGENT);
        { const u32x4* px = (const u32x4*)(x + (size_t)(256 * mt + 8 * hd + wid) * DM + lane * 16); const u32x4* pw = (const u32x4*)(gw + lane * 16);
          gx0 = px[0]; gx1 = px[1]; gw0 = pw[0]; gw1 = pw[1]; }
        SBAR();
        K1_MF(Y, 0); K1_MF(Y, 1); K1_MF(Y, 2); K1_MF(Y, 3); K1_MF(Y, 4); K1_MF(Y, 5); K1_MF(Y, 6); K1_MF(Y, 7); K1_MF(Y, 8); K1_MF(Y, 9);
        K1_MF(Y, 10); K1_MF(Y, 11); K1_MF(Y, 12); K1_MF(Y, 13); K1_MF(Y, 14); K1_MF(Y, 15); K1_MF(Y, 16); K1_MF(Y, 17); K1_MF(Y, 18); K1_MF(Y, 19);
        fr = lane & 15; fq = lane >> 4;
        const float qs = wn ? 1.0f : 0.17677669529663687f * 1.4426950408889634f;
#pragma unroll
        for (int m = 0; m < 4; ++m) {
            const u32x4 w = swap16(pack4(accQ[0][m] * qs), pack4(accQ[1][m] * qs));
            const int r = (16 * m + fr) & 31, rho = (r & 19) | ((r & 4) << 1) | ((r & 8) >> 1), j = 2 * wm + (m >> 1);
            const int in = (fq & 1) * 1024 + ((fq >> 1) * 32 + rho) * 16;
            ldsw128(smem + (wn ? wm * AT_STAGE + (m >> 1) * 2048 : FW_TOP - 2 * FW_STAGE + j * 2048) + in, w);
        }
        const int rv = 8 * (fr >> 2) + 4 * wn + (fr & 3);
#pragma unroll
        for (int i = 0; i < 2; ++i) {
            const int in = (fq & 1) * 1024 + ((fq >> 1) * 32 + rv) * 16;
            const u32x4 w = swap16(pack4(accV[2 * i]), pack4(accV[2 * i + 1]));
            ldsw128(smem + ((wm + i) & 3) * AT_STAGE + 6144 - i * 2048 + in, w);
#pragma unroll
            for (int n = 0; n < 2; ++n) {
                const u32x4 d = swap16(pack4(accD[n][2 * i]), pack4(accD[n][2 * i + 1]));
                const int el = 32 * wn + 16 * n + fr, ch = 4 * i + 2 * (fq & 1) + (fq >> 1);
                ldsw128(smem + FB_BASE + wm * 8192 + el * 128 + ((ch ^ (fr >> 1)) * 16), d);
            }
        }
        LGKM_BAR();
    }
    {
        const int qt = mt * 8 + wid;
        char* f1 = ws + WS_F1 + hd * 512;
        const unsigned rec0 = (hd * 32 + 4 * mt) * 8192 + wid * 1024 + lane * 16;
#define PUBLISH(q) do { const u32x4 a_ = *(const LAS u32x4*)(smem + (q) * 8192 + wid * 1024 + lane * 16), b_ = *(const LAS u32x4*)(smem + FB_BASE + (q) * 8192 + wid * 1024 + lane * 16); \
        st_wt(rs, WS_KV + rec0 + (q) * 8192, a_); st_wt(rs, WS_AB + rec0 + (q) * 8192, b_); } while (0)
        PUBLISH(0); PUBLISH(1);
        const bf16x8 sel = attn_sel(lane);
        const LAS char* fb = smem + lane * 16;
        const bf16x8 q0 = ldsr128(fb + FW_TOP - 2 * FW_STAGE + wid * 2048), q1 = ldsr128(fb + FW_TOP - 2 * FW_STAGE + wid * 2048 + 1024);
        bf16x8 kA0, kA1, kB0, kB1, vA0, vA1, vB0, vB1 = bf16x8{};
        f32x16 negm, o = f32x16{}, sA, sB; f32x4 l = f32x4{};
        u32x4 p0, p1 = u32x4{};
        kA0 = ldsr128(fb); kA1 = ldsr128(fb + 1024); kB0 = ldsr128(fb + 2048); kB1 = ldsr128(fb + 3072); vA0 = ldsr128(fb + 6144); vA1 = ldsr128(fb + 7168);
        SBAR(); LGKM_BAR(); SBAR();
        {
            sA = MFMA32(kA0, q0, o); sA = MFMA32(kA1, q1, sA);
            float mx = max16(sA); mx = fmaxf(mx, __shfl_xor(mx, 32)) + 40.f;
#pragma unroll
            for (int r = 0; r < 16; ++r) { negm[r] = -mx; sA[r] -= mx; }
        }
        {
            float s = 0.f;
#pragma unroll
            for (int i = 0; i < 4; ++i) {
                s = fmaf(bflo(gx0[i]), bflo(gw0[i]), s); s = fmaf(bfhi(gx0[i]), bfhi(gw0[i]), s);
                s = fmaf(bflo(gx1[i]), bflo(gw1[i]), s); s = fmaf(bfhi(gx1[i]), bfhi(gw1[i]), s);
            }
#pragma unroll
            for (int o = 32; o >= 1; o >>= 1) s += __shfl_xor(s, o);
            if (lane == 0) __builtin_amdgcn_raw_buffer_store_b32(__float_as_uint(1.f / (1.f + __expf(-s))), rs, WS_GATE + (256 * mt + 8 * hd + wid) * 4, 0, 16);
        }
        base = (((u64)(unsigned)__builtin_amdgcn_readfirstlane((unsigned)(ep >> 32)) << 32) | (unsigned)__builtin_amdgcn_readfirstlane((unsigned)ep)) + 8;
#define EX(S_, i) S_[i] = __builtin_amdgcn_exp2f(S_[i])
#define AT_STEP(SC, SN, K0, K1, VC0, VP1, RD0, RD1, RD2, RD3) do { \
        SN = MFMA32(K0, q0, negm); SBAR(); RD0; EX(SC, 0); EX(SC, 1); EX(SC, 2); EX(SC, 3); SBAR(); \
        o = MFMA32(VP1, BC(p1), o); PIN(o); SBAR(); RD1; EX(SC, 4); EX(SC, 5); EX(SC, 6); EX(SC, 7); p0[0] = cvtpk(SC[0], SC[1]); p0[1] = cvtpk(SC[2], SC[3]); SBAR(); \
        l = MFMA16(sel, BC(p1), l); PIN(l); SBAR(); p0[2] = cvtpk(SC[4], SC[5]); p0[3] = cvtpk(SC[6], SC[7]); EX(SC, 8); SBAR(); \
        SN = MFMA32(K1, q1, SN); SBAR(); RD2; EX(SC, 9); EX(SC, 10); EX(SC, 11); EX(SC, 12); SBAR(); \
        o = MFMA32(VC0, BC(p0), o); PIN(o); SBAR(); RD3; EX(SC, 13); EX(SC, 14); EX(SC, 15); p1[0] = cvtpk(SC[8], SC[9]); SBAR(); \
        l = MFMA16(sel, BC(p0), l); PIN(l); SBAR(); p1[1] = cvtpk(SC[10], SC[11]); p1[2] = cvtpk(SC[12], SC[13]); p1[3] = cvtpk(SC[14], SC[15]); SBAR(); } while (0)
        int sl = AT_STAGE;
#define AT_TICK() do { \
            AT_STEP(sA, sB, kB0, kB1, vA0, vB1, kA0 = ldsr128(fb + sl), kA1 = ldsr128(fb + sl + 1024), vB0 = ldsr128(fb + sl + 4096), vB1 = ldsr128(fb + sl + 5120)); \
            AT_STEP(sB, sA, kA0, kA1, vB0, vA1, kB0 = ldsr128(fb + sl + 2048), kB1 = ldsr128(fb + sl + 3072), vA0 = ldsr128(fb + sl + 6144), vA1 = ldsr128(fb + sl + 7168)); \
            sl = (sl + AT_STAGE) & (4 * AT_STAGE - 1); } while (0)
#pragma unroll 1
        for (int T = 1; T <= 2; ++T) {
            asm volatile("s_waitcnt vmcnt(0)" ::: "memory");
            if (wid == 1 && T == 2) wait_flags(f1, 8, base + 1, 1, lane);
            LGKM_BAR(); SBAR();
            if (wid == 0) set_flag(f1 + mt * 64, base + T, lane);
            if (T == 1) { PUBLISH(2); PUBLISH(3); }
            SBAR();
            AT_TICK();
        }
        LGKM_BAR(); SBAR();
        {
            const u64 fv = __hip_atomic_load((const GAS u64*)(f1 + (lane & 7) * 64), RLX_AGENT);
            SBAR();
            AT_TICK();
            if (wid == 1 && !__all(fv == base + 2)) wait_flags(f1, 8, base + 2, 0, lane);
        }
        const int t_touch = wid >= 4 ? 4 * wid - 8 : 0;
#pragma unroll 1
        for (int T = 4; T <= 32; ++T) {
            LGKM_BAR(); SBAR();
            if (__builtin_expect(T == t_touch, 0)) {
                asm volatile("global_load_dword v167, %0, %1" :: "v"(TW(lane)), "s"(wsb + ((mt + wid) & 7) * 512) : "memory", "v167");
                if (wid == 7) asm volatile("global_load_dword v167, %0, %1" :: "v"(lane * 128), "s"(pf2) : "memory", "v167");
            }
            SBAR();
            AT_TICK();
        }
        o = MFMA32(vB1, BC(p1), o); l = MFMA16(sel, BC(p1), l);
        bool bad = !(l[0] >= 7.8886e-31f && l[0] <= 3.0e38f);
#pragma unroll
        for (int r = 0; r < 16; ++r) bad |= !(fabsf(o[r]) <= 3.0e38f);
        const int r = lane & 31, qrow = qt * 32 + ((r & 19) | ((r & 4) << 1) | ((r & 8) >> 1));
        const unsigned ooff = WS_O + ((qrow >> 4) * 32 + hd) * 1024 + ((lane >> 5) * 32 + (qrow & 15)) * 16;
        if (__builtin_expect(__any(bad), 0)) attn_slow(rs, ws + WS_KV + hd * 262144, q0, q1, lane, ooff);
        else attn_store(rs, ooff, o, l[0]);
        WAIT_BAR(0);
    }
    {
        const int rt = 2 * (blockIdx.x & 7) + (blockIdx.x >> 7), nb = ((blockIdx.x >> 3) & 15) * 64;
        const int kq = fr >> 2, swz = ((kq >> 1) & 1) | ((fq & 1) << 1);
        const int aW = (8 * fq + kq) * 128 + ((((lane & 3) >> 1) ^ (swz << 1)) * 16) + (lane & 1) * 8;
        const int kh = wid & 1;
        const int rot = (blockIdx.x >> 3) & 15;
#define P3_KS(ks_) (((ks_) + rot) & 15)
#define P3_RD(W_, ks_) do { const int wb_ = FB_BASE + (16 * kh + P3_KS(ks_)) * 4096; \
            _Pragma("unroll") for (int n_ = 0; n_ < 4; ++n_) W_[n_] = ldstr(smem + wb_ + (aW ^ (n_ * 32)), 512); } while (0)
        bf16x8 wf[2][4];
        P3_RD(wf[0], 0);
        if (wid == 0) {
            set_flag(ws + WS_F2 + (mt * 32 + hd) * 64, base + 5, lane);
            wait_flags(ws + WS_F2 + (rt >> 1) * 2048, 32, base + 5, 0, lane);
            __builtin_amdgcn_fence(__ATOMIC_ACQUIRE, "wavefront");
            if (blockIdx.x == 0) set_flag(ws, base, lane);
        }
        LGKM_BAR();
        const int R0 = rt * 128 + 16 * wid;
        const int t = R0 + fr, col = nb + 16 * (fq & 1) + 8 * (fq >> 1);
        const unsigned ob0 = WS_O + ((R0 >> 4) * 32 + 16 * kh) * 1024 + lane * 16, ob1 = WS_O + (((R0 >> 4) ^ 1) * 32 + 16 * kh) * 1024 + lane * 16;
        u32x4 of[10][2], y0[2], y1[2]; float g;
#pragma unroll
        for (int ks = 0; ks < 10; ++ks) { of[ks][0] = ld_o(rs, ob0, P3_KS(ks) * 1024); of[ks][1] = ld_o(rs, ob1, P3_KS(ks) * 1024); }
        SBAR();
        f32x4 acc[4], acp[4];
#pragma unroll
        for (int n = 0; n < 4; ++n) { acc[n] = f32x4{}; acp[n] = f32x4{}; }
#pragma unroll
        for (int ks = 0; ks < 16; ++ks) {
            if (ks + 1 < 16) P3_RD(wf[(ks + 1) & 1], ks + 1);
            SBAR();
#pragma unroll
            for (int n = 0; n < 4; ++n) acc[n] = MFMA16(wf[ks & 1][n], BC(of[ks % 10][0]), acc[n]);
#pragma unroll
            for (int n = 0; n < 4; ++n) acp[n] = MFMA16(wf[ks & 1][n], BC(of[ks % 10][1]), acp[n]);
            if (ks < 6) { of[ks][0] = ld_o(rs, ob0, P3_KS(10 + ks) * 1024); of[ks][1] = ld_o(rs, ob1, P3_KS(10 + ks) * 1024); }
            if (ks == 8) {
                g = __uint_as_float(__builtin_amdgcn_raw_buffer_load_b32(rs, WS_GATE + t * 4, 0, 16));
#pragma unroll
                for (int i = 0; i < 2; ++i) { const unsigned yo = WS_Y + ((R0 >> 4) * 16 + rot) * 2048 + i * 1024 + lane * 16; y0[i] = ld_dev(rs, yo); y1[i] = ld_dev(rs, yo + 4 * MB); }
            }
            SBAR();
        }
#pragma unroll
        for (int n = 0; n < 4; ++n) *(LAS f32x4*)(smem + wid * 4096 + n * 1024 + lane * 16) = acp[n];
        LGKM_BAR();
#pragma unroll
        for (int n = 0; n < 4; ++n) acc[n] += *(const LAS f32x4*)(smem + (wid ^ 1) * 4096 + n * 1024 + lane * 16);
        const float g1 = 1.f - g;
        u32x4 w[2];
#pragma unroll
        for (int i = 0; i < 2; ++i) {
            float v[8];
#pragma unroll
            for (int j = 0; j < 4; ++j) {
                auto r = __builtin_amdgcn_permlane16_swap(__float_as_uint(acc[2 * i][j]), __float_as_uint(acc[2 * i + 1][j]), false, false);
                v[j] = __uint_as_float(r[0]); v[4 + j] = __uint_as_float(r[1]);
            }
#pragma unroll
            for (int j = 0; j < 4; ++j)
                w[i][j] = cvtpk(g * (bflo(y0[i][j]) - bflo(y1[i][j])) + g1 * v[2 * j], g * (bfhi(y0[i][j]) - bfhi(y1[i][j])) + g1 * v[2 * j + 1]);
        }
        const bool lo = fr < 8;
        u32x4 sa, sb;
#pragma unroll
        for (int j = 0; j < 4; ++j) {
            const unsigned r = (unsigned)__builtin_amdgcn_update_dpp(0, (int)w[1][j], 0x128, 0xf, 0xf, false);
            sa[j] = lo ? w[0][j] : r; sb[j] = lo ? r : w[0][j];
        }
        const rsrc_t ro = __builtin_amdgcn_make_buffer_rsrc(out, 0, SEQ * DM * 2, 0x00020000);
        const unsigned oa = ((R0 + (fr & 7)) * DM + col) * 2;
        __builtin_amdgcn_raw_buffer_store_b128(sa, ro, oa + (lo ? 0 : 64), 0, OUT_AUX);
        __builtin_amdgcn_raw_buffer_store_b128(sb, ro, oa + 8 * DM * 2 + (lo ? 64 : 0), 0, OUT_AUX);
    }
}

DI void side_waves(LAS char* smem, int lw, int lane, int mt, int hd, const u16* __restrict__ x, const u16* __restrict__ wsr, const u16* __restrict__ wsi,
                   const u16* __restrict__ wdr, const u16* __restrict__ wdi, const u16* __restrict__ wqkv, const u16* __restrict__ wo,
                   const u16* __restrict__ gw, char* ws, rsrc_t rs) {
    const unsigned lds0 = (unsigned)(size_t)smem + lw * 1024;
    const unsigned rowl = 8 * lw + (lane >> 3), chk = ((lane & 7) ^ ((4 * lw + (lane >> 4)) & 7)) * 16;
    const int c = hd >> 4;
    const char* wb = (const char*)(c ? wsi : wsr) + (size_t)mt * 256 * 4096;
    unsigned vo[10];
    {
        const char* xb = (const char*)x + (size_t)mt * 256 * 2048;
        const char* wdb = (const char*)(c ? wdi : wdr) + (size_t)(hd & 15) * 64 * 2048;
        const char* wqb = (const char*)wqkv + hd * 64 + lw * 16 * 6144;
#pragma unroll
        for (int i = 0; i < 8; ++i) vo[i] = (rowl + 32 * i) * 2048 + chk;
        const unsigned voffW = (lane >> 2) * 6144 + (((lane & 3) ^ ((lane >> 5) << 1)) * 16);
#define K1_DMA(t, slot) do { const char* xa_ = xb + (size_t)(t) * 128; const char* wd_ = wdb + (size_t)(t) * 128; const char* wq_ = wqb + (size_t)(t) * 64 * 6144; \
        _Pragma("unroll") for (int u_ = 0; u_ < 3; ++u_) dma(voffW, wq_ + u_ * 2048, lds0 + (slot) + K1_WQ + u_ * 4096); \
        _Pragma("unroll") for (int i_ = 0; i_ < 2; ++i_) dma(vo[i_], wd_, lds0 + (slot) + K1_WD + i_ * 4096); \
        _Pragma("unroll") for (int i_ = 0; i_ < 8; ++i_) dma(vo[i_], xa_, lds0 + (slot) + K1_A + i_ * 4096); } while (0)
        K1_DMA(0, 0); K1_DMA(1, K1_STAGE);
        WAIT_BAR(13);
        K1_DMA(2, 2 * K1_STAGE);
        int sl = 0;
        for (int t = 0; t < 13; ++t) {
            WAIT_BAR(13);
            K1_DMA(t + 3, sl);
            sl = (sl == 2 * K1_STAGE) ? 0 : sl + K1_STAGE;
        }
        WAIT_BAR(13);
        WAIT_BAR(0);
#pragma unroll
        for (int i = 0; i < 8; ++i) vo[i] = (rowl + 32 * i) * 4096 + chk;
        vo[8] = lane * 16 + lw * 1024; vo[9] = vo[8] + 4096;
#pragma unroll
        for (int i = 0; i < 7; ++i) dma(vo[i], wb + (size_t)((4 * mt) & 31) * 128, lds0 + FW_TOP + i * 4096);
#pragma unroll
        for (int i = 0; i < 8; ++i) dma(vo[i], wb + (size_t)((4 * mt + 1) & 31) * 128, lds0 + FW_TOP - FW_STAGE + i * 4096);
        LGKM_BAR();
        dma(vo[7], wb + (size_t)((4 * mt) & 31) * 128, lds0 + FW_TOP + 7 * 4096);
        WAIT_BAR(0);
    }
    {
        const char* kvb = ws + WS_KV + hd * 262144;
        const unsigned vl = lane * 16 + lw * 1024;
        const char *bW = wb, *bA = ws + WS_AB + hd * 262144; int kmask = 31, kstep = 128, kstepA = 8192, koff = 4 * mt;
#define FN_DMA_P(p, s, fslot, aslot) do { \
        if ((p) < 8) dma(vo[p], bW + (size_t)((koff + (s)) & kmask) * kstep, lds0 + (fslot) + (p) * 4096); \
        else if ((p) < 10) dma_dev(vo[p], bA + (size_t)((koff + (s)) & kmask) * kstepA, lds0 + FB_BASE + (aslot) + ((p) - 8) * 4096); \
        else if ((p) == 10) dma_dev(vl, kvb + ((4 * mt + (s)) & 31) * 8192, lds0 + (aslot)); \
        else dma_dev(vl, kvb + ((4 * mt + (s) - ((((s) & 3) == 0 && lw < 2) ? 4 : 0)) & 31) * 8192 + 4096, lds0 + (aslot) + 4096); } while (0)
        const int fr = lane & 15, fq = lane >> 4;
        const int cpart = ((fq ^ (fr >> 1)) & 7) * 16;
        const int aW = (64 * lw + fr) * 128 + cpart, aA = FB_BASE + fr * 128 + cpart;
        f32x4 acc[4][4];
#pragma unroll
        for (int m = 0; m < 4; ++m)
#pragma unroll
            for (int n = 0; n < 4; ++n) acc[m][n] = f32x4{};
        FnFrag X, Yf;
#define FN_RD(F, i, slot, bslot, ks) do { \
        if ((i) == 0) F.w[0] = ldsr128(smem + (slot) + (aW ^ ((ks) * 64))); \
        else if ((i) < 5) F.a[(i) - 1] = ldsr128(smem + (bslot) + (aA ^ ((ks) * 64)) + ((i) - 1) * 2048); \
        else F.w[(i) - 4] = ldsr128(smem + (slot) + (aW ^ ((ks) * 64)) + ((i) - 4) * 2048); } while (0)
#define FN_MF(F, j) acc[(j) >> 2][(j) & 3] = MFMA16(F.a[(j) & 3], F.w[(j) >> 2], acc[(j) >> 2][(j) & 3])
#define FN_G(g, C, R, rslot, rb, ks, P0, s, fslot, aslot) do { FN_MF(C, 2 * (g)); FN_RD(R, g, rslot, rb, ks); FN_MF(C, 2 * (g) + 1); \
        if (((g) & 3) != 0 && (P0) + (g) - 1 - ((g) >> 2) < PMAX) FN_DMA_P((P0) + (g) - 1 - ((g) >> 2), s, fslot, aslot); SBAR(); } while (0)
#define FN_PHASE(C, R, rslot, rb, ks, P0, s, fslot, aslot) do { FN_G(0, C, R, rslot, rb, ks, P0, s, fslot, aslot); FN_G(1, C, R, rslot, rb, ks, P0, s, fslot, aslot); \
        FN_G(2, C, R, rslot, rb, ks, P0, s, fslot, aslot); FN_G(3, C, R, rslot, rb, ks, P0, s, fslot, aslot); FN_G(4, C, R, rslot, rb, ks, P0, s, fslot, aslot); \
        FN_G(5, C, R, rslot, rb, ks, P0, s, fslot, aslot); FN_G(6, C, R, rslot, rb, ks, P0, s, fslot, aslot); FN_G(7, C, R, rslot, rb, ks, P0, s, fslot, aslot); } while (0)
#define PMAX 12
        __builtin_amdgcn_s_setprio(1);
#pragma unroll
        for (int i = 0; i < 8; ++i) FN_RD(X, i, FW_TOP, 0, 0);
        LGKM_BAR();
        int f0 = FW_TOP, f1 = FW_TOP - FW_STAGE, f2 = FW_TOP - 2 * FW_STAGE;
        FN_DMA_P(0, 2, f2, 0); FN_DMA_P(1, 2, f2, 0); FN_DMA_P(2, 2, f2, 0); FN_DMA_P(3, 2, f2, 0); FN_DMA_P(4, 2, f2, 0); FN_DMA_P(5, 2, f2, 0);
        SBAR();
#define FN_ITER(t, WAITN) do { \
            FN_PHASE(X, Yf, f0, ((t) & 3) * 8192, 1, 6, (t) + 2, f2, (((t) + 2) & 3) * 8192);       \
            WAIT_BAR(WAITN); SBAR();                                                                \
            FN_SWITCH(t); \
            FN_PHASE(Yf, X, f1, (((t) + 1) & 3) * 8192, 0, 0, (t) + 3, f0, 0);                      \
            const int ft = f0; f0 = f1; f1 = f2; f2 = ft; } while (0)
#define FN_SWITCH(t)
#undef PMAX
#define PMAX 8
        FN_ITER(0, 8); FN_ITER(1, 8);
#undef PMAX
#define PMAX 12
#undef FN_SWITCH
#define FN_SWITCH(t) do { if (__builtin_expect((t) == 29, 0)) { bA = (const char*)wo + ((blockIdx.x >> 3) & 15) * 128; bW = bA + 256 * 2048; kmask = 3; kstep = 512 * 2048; kstepA = 64 * 2048; koff = 0; \
                const unsigned cw = ((lane & 7) ^ ((((lane >> 4) & 1) | ((lw & 1) << 1)) << 1)) * 16; \
                _Pragma("unroll") for (int i = 0; i < 10; ++i) vo[i] = (rowl + 32 * (i & 7)) * 2048 + cw; } SBAR(); } while (0)
        for (int t = 2; t < 31; ++t) FN_ITER(t, 12);
#undef PMAX
#define PMAX 10
        FN_PHASE(X, Yf, f0, 3 * 8192, 1, 6, 33, f2, 8192);
        WAIT_BAR(10); SBAR();
#pragma unroll
        for (int i = 0; i < 8; ++i) dma(vo[i], bA + (size_t)512 * 2048, lds0 + FW_TOP - FW_STAGE + i * 4096);
#pragma unroll
        for (int i = 0; i < 4; ++i) dma(vo[i], bA + (size_t)128 * 2048, lds0 + FB_BASE + 2 * 8192 + i * 4096);
#pragma unroll
        for (int j = 0; j < 16; ++j) FN_MF(Yf, j);
        const unsigned yb = WS_Y + c * 4 * MB + ((mt * 16 + 4 * lw) * 16 + (hd & 15)) * 2048 + lane * 16;
#pragma unroll
        for (int m = 0; m < 4; ++m)
#pragma unroll
            for (int i = 0; i < 2; ++i) st_wt(rs, yb + m * 32768 + i * 1024, swap16(pack4(acc[m][2 * i]), pack4(acc[m][2 * i + 1])));
        WAIT_BAR(0);
    }
}

__global__ __launch_bounds__(768, 3) void fused_kernel(const u16* __restrict__ x, const u16* __restrict__ wsr, const u16* __restrict__ wsi,
                                                        const u16* __restrict__ wdr, const u16* __restrict__ wdi, const u16* __restrict__ wqkv,
                                                        const u16* __restrict__ wo, const u16* __restrict__ gw, u16* __restrict__ out, char* ws) {
    __shared__ __attribute__((aligned(1024))) char smem_[163840];
    LAS char* smem = (LAS char*)smem_;
    const int tid = threadIdx.x, lane = tid & 63, wid = __builtin_amdgcn_readfirstlane(tid >> 6);
    const int xcd = blockIdx.x & 7, idx = blockIdx.x >> 3;
    const int hd = xcd * 4 + (idx >> 3), mt = idx & 7;
    const rsrc_t rs = __builtin_amdgcn_make_buffer_rsrc(ws, 0, WS_END, 0x00020000);
    if (wid < 8) {
        const char* wdb = (const char*)((hd >> 4) ? wdi : wdr) + (size_t)(hd & 15) * 64 * 2048;
        const char* wsb = (const char*)((hd >> 4) ? wsi : wsr) + (size_t)mt * 256 * 4096;
        const char* pf2 = (const char*)wo + (size_t)blockIdx.x * 8192;
        main_waves(smem, wid, lane, mt, hd, ws, rs, out, x, gw, wdb, (const char*)wqkv, wsb, pf2);
    }
    else side_waves(smem, wid - 8, lane, mt, hd, x, wsr, wsi, wdr, wdi, wqkv, wo, gw, ws, rs);
}

extern "C" void kernel_launch(void* const* d_in, const int* in_sizes, int n_in, void* d_out, int out_size, void* d_ws, size_t ws_size, hipStream_t stream) {
    const u16* x = (const u16*)d_in[0]; const u16* wsr = (const u16*)d_in[1]; const u16* wsi = (const u16*)d_in[2];
    const u16* wdr = (const u16*)d_in[3]; const u16* wdi = (const u16*)d_in[4]; const u16* wqkv = (const u16*)d_in[6];
    const u16* wo = (const u16*)d_in[7]; const u16* gw = (const u16*)d_in[8];
    fused_kernel<<<256, 768, 0, stream>>>(x, wsr, wsi, wdr, wdi, wqkv, wo, gw, (u16*)d_out, (char*)d_ws);
}
```

```cpp
#include <hip/hip_runtime.h>
#include <hip/hip_bf16.h>

typedef unsigned short u16;
typedef short bf16x8 __attribute__((ext_vector_type(8)));
typedef short s16x4 __attribute__((ext_vector_type(4)));
typedef float f32x2 __attribute__((ext_vector_type(2)));
typedef float f32x4 __attribute__((ext_vector_type(4)));
typedef float f32x16 __attribute__((ext_vector_type(16)));
typedef unsigned u32x4 __attribute__((ext_vector_type(4)));
typedef unsigned u32x2 __attribute__((ext_vector_type(2)));
typedef __bf16 bf16x2_t __attribute__((ext_vector_type(2)));
#define LAS __attribute__((address_space(3)))
#define GAS __attribute__((address_space(1)))
#define DI __device__ __forceinline__
#define SBAR() __builtin_amdgcn_sched_barrier(0)
#define PIN(x) asm volatile("" : "+v"(x))
#define MFMA16(a, b, c) __builtin_amdgcn_mfma_f32_16x16x32_bf16(a, b, c, 0, 0, 0)
#define MFMA32(a, b, c) __builtin_amdgcn_mfma_f32_32x32x16_bf16(a, b, c, 0, 0, 0)
#define WAIT_BAR(N) asm volatile("s_waitcnt vmcnt(" #N ") lgkmcnt(0)\n\ts_barrier" ::: "memory")
#define LGKM_BAR() asm volatile("s_waitcnt lgkmcnt(0)\n\ts_barrier" ::: "memory")
#define RLX_AGENT __ATOMIC_RELAXED, __HIP_MEMORY_SCOPE_AGENT
#define BC(x) __builtin_bit_cast(bf16x8, x)

constexpr int SEQ = 2048, DM = 1024;
constexpr unsigned MB = 1u << 20;
constexpr unsigned WS_F1 = 4096, WS_F2 = 4096 + 16384, WS_GATE = 65536, WS_KV = 1 * MB, WS_AB = 9 * MB, WS_O = 17 * MB, WS_Y = 21 * MB, WS_END = 29 * MB;
typedef unsigned long long u64;

DI unsigned cvtpk(float lo, float hi) { f32x2 v = {lo, hi}; bf16x2_t b = __builtin_convertvector(v, bf16x2_t); return __builtin_bit_cast(unsigned, b); }
DI void dma(unsigned voff, const char* sbase, unsigned l) {
    asm volatile("s_mov_b32 m0, %2\n\ts_nop 0\n\tglobal_load_lds_dwordx4 %0, %1" :: "v"(voff), "s"(sbase), "s"(l) : "memory"); }
DI void dma_dev(unsigned voff, const char* sbase, unsigned l) {
    asm volatile("s_mov_b32 m0, %2\n\ts_nop 0\n\tglobal_load_lds_dwordx4 %0, %1 sc1" :: "v"(voff), "s"(sbase), "s"(l) : "memory"); }
DI void ldsw128(LAS char* p, u32x4 v) { *(LAS u32x4*)p = v; }
DI bf16x8 ldsr128(const LAS char* p) { return *(const LAS bf16x8*)p; }
DI bf16x8 ldstr(const LAS char* p, int off2) {
    s16x4 a = __builtin_amdgcn_ds_read_tr16_b64_v4i16((LAS s16x4*)p);
    s16x4 b = __builtin_amdgcn_ds_read_tr16_b64_v4i16((LAS s16x4*)(p + off2));
    return (bf16x8){a[0], a[1], a[2], a[3], b[0], b[1], b[2], b[3]};
}
DI int lane_now() { int l; asm volatile("v_mbcnt_lo_u32_b32 %0, -1, 0\n\tv_mbcnt_hi_u32_b32 %0, -1, %0" : "=v"(l)); return l; }
DI float bflo(unsigned u) { return __uint_as_float(u << 16); }
DI float bfhi(unsigned u) { return __uint_as_float(u & 0xffff0000u); }
DI u32x4 swap16(u32x2 a, u32x2 b) {
    auto r0 = __builtin_amdgcn_permlane16_swap(a[0], b[0], false, false);
    auto r1 = __builtin_amdgcn_permlane16_swap(a[1], b[1], false, false);
    return (u32x4){r0[0], r1[0], r0[1], r1[1]};
}
DI u32x2 pack4(f32x4 v) { return (u32x2){cvtpk(v[0], v[1]), cvtpk(v[2], v[3])}; }
typedef __amdgpu_buffer_rsrc_t rsrc_t;
DI void st_wt(rsrc_t r, unsigned off, u32x4 v) { __builtin_amdgcn_raw_buffer_store_b128(v, r, off, 0, 16); }
DI u32x4 ld_dev(rsrc_t r, unsigned off) { return __builtin_amdgcn_raw_buffer_load_b128(r, off, 0, 16); }
DI u32x4 ld_o(rsrc_t r, unsigned off, unsigned soff) { return __builtin_amdgcn_raw_buffer_load_b128(r, off, soff, 16); }
DI void wait_flags(const char* first, int n, u64 lo, u64 span, int lane) {
    const GAS u64* p = (const GAS u64*)(first + (lane < n ? lane : 0) * 64);
#pragma unroll 1
    for (unsigned spins = 0; spins < (1u << 16); ++spins) {
        const u64 v = __hip_atomic_load(p, RLX_AGENT);
        if (__all(v - lo <= span)) break;
        __builtin_amdgcn_s_sleep(1);
    }
}
DI void set_flag(char* p, u64 v, int lane) { if (lane == 0) __hip_atomic_store((GAS u64*)p, v, RLX_AGENT); }

constexpr int K1_STAGE = 53248, K1_A = 0, K1_WD = 32768, K1_WQ = 40960;
constexpr int AT_STAGE = 8192, FB_BASE = 32768, FW_STAGE = 32768, FW_TOP = 131072;
struct K1Frag { bf16x8 x[4], wd[2], wqk[2], wv; };
struct FnFrag { bf16x8 w[4], a[4]; };

DI void attn_store(rsrc_t rs, unsigned off, const f32x16& o, float l) {
    const float rl = 1.0f / l; u32x4 w0, w1;
#pragma unroll
    for (int i = 0; i < 4; ++i) { w0[i] = cvtpk(o[2 * i] * rl, o[2 * i + 1] * rl); w1[i] = cvtpk(o[8 + 2 * i] * rl, o[9 + 2 * i] * rl); }
    st_wt(rs, off, w0); st_wt(rs, off + 256, w1);
}
DI bf16x8 attn_sel(int lane) {
    const short v = (((lane >> 2) & 1) == ((lane >> 4) & 1)) ? (short)0x3f80 : (short)0;
    return (bf16x8){v, v, v, v, v, v, v, v};
}
DI float max16(const f32x16& s) {
    float a = fmaxf(fmaxf(s[0], s[1]), s[2]), b = fmaxf(fmaxf(s[3], s[4]), s[5]);
#pragma unroll
    for (int r = 6; r < 16; r += 4) { a = fmaxf(fmaxf(a, s[r]), s[r + 1]); if (r + 3 < 16) b = fmaxf(fmaxf(b, s[r + 2]), s[r + 3]); }
    return fmaxf(a, b);
}
DI void attn_slow(rsrc_t rs, const char* kv, bf16x8 q0, bf16x8 q1, int lane, unsigned ooff) {
    __builtin_amdgcn_fence(__ATOMIC_ACQUIRE, "agent");
    const bf16x8 sel = attn_sel(lane);
    kv += lane * 16;
    float mx = -INFINITY;
    const f32x16 z = f32x16{};
#pragma unroll 1
    for (int j = 0; j < 64; ++j) {
        const char* kp = kv + (j >> 1) * 8192 + (j & 1) * 2048;
        f32x16 s = MFMA32(*(const bf16x8*)kp, q0, z); s = MFMA32(*(const bf16x8*)(kp + 1024), q1, s);
        mx = fmaxf(mx, max16(s));
    }
    mx = fmaxf(mx, __shfl_xor(mx, 32));
    f32x16 negm, o = f32x16{}; f32x4 l = f32x4{};
#pragma unroll
    for (int r = 0; r < 16; ++r) negm[r] = -mx;
#pragma unroll 1
    for (int j = 0; j < 64; ++j) {
        const char* kp = kv + (j >> 1) * 8192 + (j & 1) * 2048;
        const char* vp = kv + ((j & 1) ? (((j & 7) == 7 ? (j >> 3) * 4 : (j + 1) >> 1) * 8192 + 4096) : ((j >> 1) * 8192 + 6144));
        const bf16x8 v0 = *(const bf16x8*)vp, v1 = *(const bf16x8*)(vp + 1024);
        f32x16 s = MFMA32(*(const bf16x8*)kp, q0, negm); s = MFMA32(*(const bf16x8*)(kp + 1024), q1, s);
        u32x4 p0, p1;
#pragma unroll
        for (int r = 0; r < 16; ++r) s[r] = __builtin_amdgcn_exp2f(s[r]);
#pragma unroll
        for (int r = 0; r < 4; ++r) { p0[r] = cvtpk(s[2 * r], s[2 * r + 1]); p1[r] = cvtpk(s[8 + 2 * r], s[9 + 2 * r]); }
        o = MFMA32(v0, BC(p0), o); l = MFMA16(sel, BC(p0), l);
        o = MFMA32(v1, BC(p1), o); l = MFMA16(sel, BC(p1), l);
    }
    attn_store(rs, ooff, o, l[0]);
}

DI void touch8k(const char* p, unsigned voff, unsigned dump) {
    asm volatile("s_mov_b32 m0, %2\n\ts_nop 0\n\tglobal_load_lds_dword %0, %1" :: "v"(voff), "s"(p), "s"(dump) : "memory"); }
DI void touch_pair(int p, int l, int mt, int hd, const char* x, const char* wdb, const char* wqkv, unsigned dump) {
    const int n = l >= 56 ? l - 8 : l, i = (mt * 2 + (hd & 1)) * 24 + (n - 32);
    const char* a = n < 16 ? x + (size_t)(mt * 256 + hd * 8 + (n >> 1)) * 2048 + p * 256 + (n & 1) * 128
                  : n < 32 ? wdb + (size_t)(mt * 8 + ((n - 16) >> 1)) * 2048 + p * 256 + (n & 1) * 128
                           : wqkv + (size_t)(p * 128 + i / 3) * 6144 + (i % 3) * 2048 + (hd >> 1) * 128;
    asm volatile("s_mov_b32 m0, %1\n\ts_nop 0\n\tglobal_load_lds_dword %0, off" :: "v"(a), "s"(dump) : "memory");
}
DI void main_waves(LAS char* smem, int wid, int lane, int mt, int hd, char* ws, rsrc_t rs, u16* __restrict__ out,
                   const u16* __restrict__ x, const u16* __restrict__ gw, const char* wdb, const char* wqkv, const char* wsb, const char* pf2) {
    const unsigned dump = (unsigned)(size_t)smem + 3 * K1_STAGE + wid * 256;
    if (wid <= 1) touch_pair(wid, lane, mt, hd, (const char*)x, wdb, wqkv, dump);
    const int t_act = wid < 4 ? 7 + 2 * wid : 2 * wid - 8;
#define TW(l_) (unsigned)(((hd & 15) * 16 + ((l_) >> 2)) * 4096 + ((l_) & 3) * 128)
    int fr = lane & 15, fq = lane >> 4;
    u64 ep, base; u32x4 gx0, gx1, gw0, gw1;
    {
        const int wm = wid >> 1, wn = wid & 1;
        const int cpart = ((fq ^ (fr >> 1)) & 7) * 16;
        const int aA = K1_A + (64 * wm + fr) * 128 + cpart, aD = K1_WD + (32 * wn + fr) * 128 + cpart;
        const int aT0 = (8 * fq + (fr >> 2)) * 64 + ((((lane & 3) >> 1) ^ ((fq & 1) << 1)) * 16) + (lane & 1) * 8;
        const int aQ = K1_WQ + wn * 4096 + aT0, aV = K1_WQ + 8192 + (aT0 ^ (wn * 32));
        f32x4 accQ[2][4], accV[4], accD[2][4];
#pragma unroll
        for (int m = 0; m < 4; ++m) { accQ[0][m] = f32x4{}; accQ[1][m] = f32x4{}; accV[m] = f32x4{}; accD[0][m] = f32x4{}; accD[1][m] = f32x4{}; }
        K1Frag X, Y;
#define K1_RD(F, i, slot, ks) do { const LAS char* sb_ = smem + (slot); \
        if ((i) == 0) F.wqk[0] = ldstr(sb_ + aQ + (ks) * 2048, 256); \
        else if ((i) == 1) { F.x[0] = ldsr128(sb_ + (aA ^ ((ks) * 64))); } \
        else if ((i) == 2) F.wqk[1] = ldstr(sb_ + (aQ ^ 32) + (ks) * 2048, 256); \
        else if ((i) == 3) F.wv = ldstr(sb_ + aV + (ks) * 2048, 256); \
        else if ((i) == 4) { F.wd[0] = ldsr128(sb_ + (aD ^ ((ks) * 64))); F.wd[1] = ldsr128(sb_ + (aD ^ ((ks) * 64)) + 2048); } \
        else if ((i) < 8) F.x[(i) - 4] = ldsr128(sb_ + (aA ^ ((ks) * 64)) + ((i) - 4) * 2048); } while (0)
#define K1_MF(F, j) do { constexpr int m_ = (j) / 5, r_ = (j) % 5; \
        if (r_ == 0) accQ[0][m_] = MFMA16(F.wqk[0], F.x[m_], accQ[0][m_]); \
        else if (r_ == 1) accQ[1][m_] = MFMA16(F.wqk[1], F.x[m_], accQ[1][m_]); \
        else if (r_ == 2) accV[m_] = MFMA16(F.x[m_], F.wv, accV[m_]); \
        else if (r_ == 3) accD[0][m_] = MFMA16(F.x[m_], F.wd[0], accD[0][m_]); \
        else accD[1][m_] = MFMA16(F.x[m_], F.wd[1], accD[1][m_]); } while (0)
#define K1_G(g, C, R, rslot, ks) do { K1_MF(C, 2 * (g)); if ((g) == 0) SBAR(); K1_RD(R, g, rslot, ks); K1_MF(C, 2 * (g) + 1); SBAR(); } while (0)
#define K1_PHASE(C, R, rslot, ks) do { K1_G(0, C, R, rslot, ks); K1_G(1, C, R, rslot, ks); K1_G(2, C, R, rslot, ks); K1_G(3, C, R, rslot, ks); K1_G(4, C, R, rslot, ks); \
        K1_G(5, C, R, rslot, ks); K1_G(6, C, R, rslot, ks); K1_G(7, C, R, rslot, ks); K1_G(8, C, R, rslot, ks); K1_G(9, C, R, rslot, ks); } while (0)
        asm volatile("" :: "s"(gw), "s"(out));
        __builtin_amdgcn_s_waitcnt(0xC07F);
        LGKM_BAR();
        if (wid == 2 || wid == 3) touch_pair(wid, lane, mt, hd, (const char*)x, wdb, wqkv, dump);
#pragma unroll
        for (int i = 0; i < 8; ++i) K1_RD(X, i, 0, 0);
        int sl = 0, sn = K1_STAGE;
        SBAR();
        for (int t = 0; t < 15; ++t) {
            K1_PHASE(X, Y, sl, 1);
            LGKM_BAR(); SBAR();
            if (__builtin_expect(t == t_act, 0)) {
                const int l_ = lane_now();
                if (wid < 4) touch8k(wsb + ((mt + wid) & 7) * 512, TW(l_), dump);
                else touch_pair(wid, l_, mt, hd, (const char*)x, wdb, wqkv, dump);
            }
            SBAR();
            K1_PHASE(Y, X, sn, 0);
            sl = sn; sn = (sn == 2 * K1_STAGE) ? 0 : sn + K1_STAGE;
        }
        K1_PHASE(X, Y, 0, 1);
        WAIT_BAR(0); SBAR();
        lane = lane_now();
        ep = __hip_atomic_load((const GAS u64*)ws, RLX_AGENT);
        { const u32x4* px = (const u32x4*)(x + (size_t)(256 * mt + 8 * hd + wid) * DM + lane * 16); const u32x4* pw = (const u32x4*)(gw + lane * 16);
          gx0 = px[0]; gx1 = px[1]; gw0 = pw[0]; gw1 = pw[1]; }
        SBAR();
        K1_MF(Y, 0); K1_MF(Y, 1); K1_MF(Y, 2); K1_MF(Y, 3); K1_MF(Y, 4); K1_MF(Y, 5); K1_MF(Y, 6); K1_MF(Y, 7); K1_MF(Y, 8); K1_MF(Y, 9);
        K1_MF(Y, 10); K1_MF(Y, 11); K1_MF(Y, 12); K1_MF(Y, 13); K1_MF(Y, 14); K1_MF(Y, 15); K1_MF(Y, 16); K1_MF(Y, 17); K1_MF(Y, 18); K1_MF(Y, 19);
        fr = lane & 15; fq = lane >> 4;
        const float qs = wn ? 1.0f : 0.17677669529663687f * 1.4426950408889634f;
#pragma unroll
        for (int m = 0; m < 4; ++m) {
            const u32x4 w = swap16(pack4(accQ[0][m] * qs), pack4(accQ[1][m] * qs));
            const int r = (16 * m + fr) & 31, rho = (r & 19) | ((r & 4) << 1) | ((r & 8) >> 1), j = 2 * wm + (m >> 1);
            const int in = (fq & 1) * 1024 + ((fq >> 1) * 32 + rho) * 16;
            ldsw128(smem + (wn ? wm * AT_STAGE + (m >> 1) * 2048 : FW_TOP - 2 * FW_STAGE + j * 2048) + in, w);
        }
        const int rv = 8 * (fr >> 2) + 4 * wn + (fr & 3);
#pragma unroll
        for (int i = 0; i < 2; ++i) {
            const int in = (fq & 1) * 1024 + ((fq >> 1) * 32 + rv) * 16;
            const u32x4 w = swap16(pack4(accV[2 * i]), pack4(accV[2 * i + 1]));
            ldsw128(smem + ((wm + i) & 3) * AT_STAGE + 6144 - i * 2048 + in, w);
#pragma unroll
            for (int n = 0; n < 2; ++n) {
                const u32x4 d = swap16(pack4(accD[n][2 * i]), pack4(accD[n][2 * i + 1]));
                const int el = 32 * wn + 16 * n + fr, ch = 4 * i + 2 * (fq & 1) + (fq >> 1);
                ldsw128(smem + FB_BASE + wm * 8192 + el * 128 + ((ch ^ (fr >> 1)) * 16), d);
            }
        }
        LGKM_BAR();
    }
    {
        const int qt = mt * 8 + wid;
        char* f1 = ws + WS_F1 + hd * 512;
        const unsigned rec0 = (hd * 32 + 4 * mt) * 8192 + wid * 1024 + lane * 16;
#define PUBLISH(q) do { const u32x4 a_ = *(const LAS u32x4*)(smem + (q) * 8192 + wid * 1024 + lane * 16), b_ = *(const LAS u32x4*)(smem + FB_BASE + (q) * 8192 + wid * 1024 + lane * 16); \
        st_wt(rs, WS_KV + rec0 + (q) * 8192, a_); st_wt(rs, WS_AB + rec0 + (q) * 8192, b_); } while (0)
        PUBLISH(0); PUBLISH(1);
        const bf16x8 sel = attn_sel(lane);
        const LAS char* fb = smem + lane * 16;
        const bf16x8 q0 = ldsr128(fb + FW_TOP - 2 * FW_STAGE + wid * 2048), q1 = ldsr128(fb + FW_TOP - 2 * FW_STAGE + wid * 2048 + 1024);
        bf16x8 kA0, kA1, kB0, kB1, vA0, vA1, vB0, vB1 = bf16x8{};
        f32x16 negm, o = f32x16{}, sA, sB; f32x4 l = f32x4{};
        u32x4 p0, p1 = u32x4{};
        kA0 = ldsr128(fb); kA1 = ldsr128(fb + 1024); kB0 = ldsr128(fb + 2048); kB1 = ldsr128(fb + 3072); vA0 = ldsr128(fb + 6144); vA1 = ldsr128(fb + 7168);
        SBAR(); LGKM_BAR(); SBAR();
        {
            sA = MFMA32(kA0, q0, o); sA = MFMA32(kA1, q1, sA);
            float mx = max16(sA); mx = fmaxf(mx, __shfl_xor(mx, 32)) + 40.f;
#pragma unroll
            for (int r = 0; r < 16; ++r) { negm[r] = -mx; sA[r] -= mx; }
        }
        {
            float s = 0.f;
#pragma unroll
            for (int i = 0; i < 4; ++i) {
                s = fmaf(bflo(gx0[i]), bflo(gw0[i]), s); s = fmaf(bfhi(gx0[i]), bfhi(gw0[i]), s);
                s = fmaf(bflo(gx1[i]), bflo(gw1[i]), s); s = fmaf(bfhi(gx1[i]), bfhi(gw1[i]), s);
            }
#pragma unroll
            for (int o = 32; o >= 1; o >>= 1) s += __shfl_xor(s, o);
            if (lane == 0) __builtin_amdgcn_raw_buffer_store_b32(__float_as_uint(1.f / (1.f + __expf(-s))), rs, WS_GATE + (256 * mt + 8 * hd + wid) * 4, 0, 16);
        }
        base = (((u64)(unsigned)__builtin_amdgcn_readfirstlane((unsigned)(ep >> 32)) << 32) | (unsigned)__builtin_amdgcn_readfirstlane((unsigned)ep)) + 8;
#define EX(S_, i) S_[i] = __builtin_amdgcn_exp2f(S_[i])
#define AT_STEP(SC, SN, K0, K1, VC0, VP1, RD0, RD1, RD2, RD3) do { \
        SN = MFMA32(K0, q0, negm); SBAR(); RD0; EX(SC, 0); EX(SC, 1); EX(SC, 2); EX(SC, 3); SBAR(); \
        o = MFMA32(VP1, BC(p1), o); PIN(o); SBAR(); RD1; EX(SC, 4); EX(SC, 5); EX(SC, 6); EX(SC, 7); p0[0] = cvtpk(SC[0], SC[1]); p0[1] = cvtpk(SC[2], SC[3]); SBAR(); \
        l = MFMA16(sel, BC(p1), l); PIN(l); SBAR(); p0[2] = cvtpk(SC[4], SC[5]); p0[3] = cvtpk(SC[6], SC[7]); EX(SC, 8); SBAR(); \
        SN = MFMA32(K1, q1, SN); SBAR(); RD2; EX(SC, 9); EX(SC, 10); EX(SC, 11); EX(SC, 12); SBAR(); \
        o = MFMA32(VC0, BC(p0), o); PIN(o); SBAR(); RD3; EX(SC, 13); EX(SC, 14); EX(SC, 15); p1[0] = cvtpk(SC[8], SC[9]); SBAR(); \
        l = MFMA16(sel, BC(p0), l); PIN(l); SBAR(); p1[1] = cvtpk(SC[10], SC[11]); p1[2] = cvtpk(SC[12], SC[13]); p1[3] = cvtpk(SC[14], SC[15]); SBAR(); } while (0)
        int sl = AT_STAGE;
#define AT_TICK() do { \
            AT_STEP(sA, sB, kB0, kB1, vA0, vB1, kA0 = ldsr128(fb + sl), kA1 = ldsr128(fb + sl + 1024), vB0 = ldsr128(fb + sl + 4096), vB1 = ldsr128(fb + sl + 5120)); \
            AT_STEP(sB, sA, kA0, kA1, vB0, vA1, kB0 = ldsr128(fb + sl + 2048), kB1 = ldsr128(fb + sl + 3072), vA0 = ldsr128(fb + sl + 6144), vA1 = ldsr128(fb + sl + 7168)); \
            sl = (sl + AT_STAGE) & (4 * AT_STAGE - 1); } while (0)
#pragma unroll 1
        for (int T = 1; T <= 2; ++T) {
            asm volatile("s_waitcnt vmcnt(0)" ::: "memory");
            if (wid == 1 && T == 2) wait_flags(f1, 8, base + 1, 1, lane);
            LGKM_BAR(); SBAR();
            if (wid == 0) set_flag(f1 + mt * 64, base + T, lane);
            if (T == 1) { PUBLISH(2); PUBLISH(3); }
            SBAR();
            AT_TICK();
        }
        LGKM_BAR(); SBAR();
        {
            const u64 fv = __hip_atomic_load((const GAS u64*)(f1 + (lane & 7) * 64), RLX_AGENT);
            SBAR();
            AT_TICK();
            if (wid == 1 && !__all(fv == base + 2)) wait_flags(f1, 8, base + 2, 0, lane);
        }
        const int t_touch = wid >= 4 ? 4 * wid - 8 : 0;
#pragma unroll 1
        for (int T = 4; T <= 32; ++T) {
            LGKM_BAR(); SBAR();
            if (__builtin_expect(T == t_touch, 0)) {
                asm volatile("global_load_dword v167, %0, %1" :: "v"(TW(lane)), "s"(wsb + ((mt + wid) & 7) * 512) : "memory", "v167");
                if (wid == 7) asm volatile("global_load_dword v167, %0, %1" :: "v"(lane * 128), "s"(pf2) : "memory", "v167");
            }
            SBAR();
            AT_TICK();
        }
        o = MFMA32(vB1, BC(p1), o); l = MFMA16(sel, BC(p1), l);
        bool bad = !(l[0] >= 7.8886e-31f && l[0] <= 3.0e38f);
#pragma unroll
        for (int r = 0; r < 16; ++r) bad |= !(fabsf(o[r]) <= 3.0e38f);
        const int r = lane & 31, qrow = qt * 32 + ((r & 19) | ((r & 4) << 1) | ((r & 8) >> 1));
        const unsigned ooff = WS_O + ((qrow >> 4) * 32 + hd) * 1024 + ((lane >> 5) * 32 + (qrow & 15)) * 16;
        if (__builtin_expect(__any(bad), 0)) attn_slow(rs, ws + WS_KV + hd * 262144, q0, q1, lane, ooff);
        else attn_store(rs, ooff, o, l[0]);
        WAIT_BAR(0);
    }
    {
        const int rt = 2 * (blockIdx.x & 7) + (blockIdx.x >> 7), nb = ((blockIdx.x >> 3) & 15) * 64;
        const int kq = fr >> 2, swz = ((kq >> 1) & 1) | ((fq & 1) << 1);
        const int aW = (8 * fq + kq) * 128 + ((((lane & 3) >> 1) ^ (swz << 1)) * 16) + (lane & 1) * 8;
        const int kh = wid & 1;
        const int rot = (blockIdx.x >> 3) & 15;
#define P3_KS(ks_) (((ks_) + rot) & 15)
#define P3_RD(W_, ks_) do { const int wb_ = FB_BASE + (16 * kh + P3_KS(ks_)) * 4096; \
            _Pragma("unroll") for (int n_ = 0; n_ < 4; ++n_) W_[n_] = ldstr(smem + wb_ + (aW ^ (n_ * 32)), 512); } while (0)
        bf16x8 wf[2][4];
        P3_RD(wf[0], 0);
        if (wid == 0) {
            set_flag(ws + WS_F2 + (mt * 32 + hd) * 64, base + 5, lane);
            wait_flags(ws + WS_F2 + (rt >> 1) * 2048, 32, base + 5, 0, lane);
            __builtin_amdgcn_fence(__ATOMIC_ACQUIRE, "wavefront");
            if (blockIdx.x == 0) set_flag(ws, base, lane);
        }
        LGKM_BAR();
        const int R0 = rt * 128 + 16 * wid;
        const int t = R0 + fr, col = nb + 16 * (fq & 1) + 8 * (fq >> 1);
        const unsigned ob0 = WS_O + ((R0 >> 4) * 32 + 16 * kh) * 1024 + lane * 16, ob1 = WS_O + (((R0 >> 4) ^ 1) * 32 + 16 * kh) * 1024 + lane * 16;
        u32x4 of[10][2], y0[2], y1[2]; float g;
#pragma unroll
        for (int ks = 0; ks < 10; ++ks) { of[ks][0] = ld_o(rs, ob0, P3_KS(ks) * 1024); of[ks][1] = ld_o(rs, ob1, P3_KS(ks) * 1024); }
        SBAR();
        f32x4 acc[4], acp[4];
#pragma unroll
        for (int n = 0; n < 4; ++n) { acc[n] = f32x4{}; acp[n] = f32x4{}; }
#pragma unroll
        for (int ks = 0; ks < 16; ++ks) {
            if (ks + 1 < 16) P3_RD(wf[(ks + 1) & 1], ks + 1);
            SBAR();
#pragma unroll
            for (int n = 0; n < 4; ++n) acc[n] = MFMA16(wf[ks & 1][n], BC(of[ks % 10][0]), acc[n]);
#pragma unroll
            for (int n = 0; n < 4; ++n) acp[n] = MFMA16(wf[ks & 1][n], BC(of[ks % 10][1]), acp[n]);
            if (ks < 6) { of[ks][0] = ld_o(rs, ob0, P3_KS(10 + ks) * 1024); of[ks][1] = ld_o(rs, ob1, P3_KS(10 + ks) * 1024); }
            if (ks == 8) {
                g = __uint_as_float(__builtin_amdgcn_raw_buffer_load_b32(rs, WS_GATE + t * 4, 0, 16));
#pragma unroll
                for (int i = 0; i < 2; ++i) { const unsigned yo = WS_Y + ((R0 >> 4) * 16 + rot) * 2048 + i * 1024 + lane * 16; y0[i] = ld_dev(rs, yo); y1[i] = ld_dev(rs, yo + 4 * MB); }
            }
            SBAR();
        }
#pragma unroll
        for (int n = 0; n < 4; ++n) *(LAS f32x4*)(smem + wid * 4096 + n * 1024 + lane * 16) = acp[n];
        LGKM_BAR();
#pragma unroll
        for (int n = 0; n < 4; ++n) acc[n] += *(const LAS f32x4*)(smem + (wid ^ 1) * 4096 + n * 1024 + lane * 16);
        const float g1 = 1.f - g;
#pragma unroll
        for (int i = 0; i < 2; ++i) {
            float v[8];
#pragma unroll
            for (int j = 0; j < 4; ++j) {
                auto r = __builtin_amdgcn_permlane16_swap(__float_as_uint(acc[2 * i][j]), __float_as_uint(acc[2 * i + 1][j]), false, false);
                v[j] = __uint_as_float(r[0]); v[4 + j] = __uint_as_float(r[1]);
            }
            u32x4 w;
#pragma unroll
            for (int j = 0; j < 4; ++j)
                w[j] = cvtpk(g * (bflo(y0[i][j]) - bflo(y1[i][j])) + g1 * v[2 * j], g * (bfhi(y0[i][j]) - bfhi(y1[i][j])) + g1 * v[2 * j + 1]);
            *(u32x4*)(out + (size_t)t * DM + col + 32 * i) = w;
        }
    }
}

DI void side_waves(LAS char* smem, int lw, int lane, int mt, int hd, const u16* __restrict__ x, const u16* __restrict__ wsr, const u16* __restrict__ wsi,
                   const u16* __restrict__ wdr, const u16* __restrict__ wdi, const u16* __restrict__ wqkv, const u16* __restrict__ wo,
                   const u16* __restrict__ gw, char* ws, rsrc_t rs) {
    const unsigned lds0 = (unsigned)(size_t)smem + lw * 1024;
    const unsigned rowl = 8 * lw + (lane >> 3), chk = ((lane & 7) ^ ((4 * lw + (lane >> 4)) & 7)) * 16;
    const int c = hd >> 4;
    const char* wb = (const char*)(c ? wsi : wsr) + (size_t)mt * 256 * 4096;
    unsigned vo[10];
    {
        const char* xb = (const char*)x + (size_t)mt * 256 * 2048;
        const char* wdb = (const char*)(c ? wdi : wdr) + (size_t)(hd & 15) * 64 * 2048;
        const char* wqb = (const char*)wqkv + hd * 64 + lw * 16 * 6144;
#pragma unroll
        for (int i = 0; i < 8; ++i) vo[i] = (rowl + 32 * i) * 2048 + chk;
        const unsigned voffW = (lane >> 2) * 6144 + (((lane & 3) ^ ((lane >> 5) << 1)) * 16);
#define K1_DMA(t, slot) do { const char* xa_ = xb + (size_t)(t) * 128; const char* wd_ = wdb + (size_t)(t) * 128; const char* wq_ = wqb + (size_t)(t) * 64 * 6144; \
        _Pragma("unroll") for (int u_ = 0; u_ < 3; ++u_) dma(voffW, wq_ + u_ * 2048, lds0 + (slot) + K1_WQ + u_ * 4096); \
        _Pragma("unroll") for (int i_ = 0; i_ < 2; ++i_) dma(vo[i_], wd_, lds0 + (slot) + K1_WD + i_ * 4096); \
        _Pragma("unroll") for (int i_ = 0; i_ < 8; ++i_) dma(vo[i_], xa_, lds0 + (slot) + K1_A + i_ * 4096); } while (0)
        K1_DMA(0, 0); K1_DMA(1, K1_STAGE);
        WAIT_BAR(13);
        K1_DMA(2, 2 * K1_STAGE);
        int sl = 0;
        for (int t = 0; t < 13; ++t) {
            WAIT_BAR(13);
            K1_DMA(t + 3, sl);
            sl = (sl == 2 * K1_STAGE) ? 0 : sl + K1_STAGE;
        }
        WAIT_BAR(13);
        WAIT_BAR(0);
#pragma unroll
        for (int i = 0; i < 8; ++i) vo[i] = (rowl + 32 * i) * 4096 + chk;
        vo[8] = lane * 16 + lw * 1024; vo[9] = vo[8] + 4096;
#pragma unroll
        for (int i = 0; i < 7; ++i) dma(vo[i], wb + (size_t)((4 * mt) & 31) * 128, lds0 + FW_TOP + i * 4096);
#pragma unroll
        for (int i = 0; i < 8; ++i) dma(vo[i], wb + (size_t)((4 * mt + 1) & 31) * 128, lds0 + FW_TOP - FW_STAGE + i * 4096);
        LGKM_BAR();
        dma(vo[7], wb + (size_t)((4 * mt) & 31) * 128, lds0 + FW_TOP + 7 * 4096);
        WAIT_BAR(0);
    }
    {
        const char* kvb = ws + WS_KV + hd * 262144;
        const unsigned vl = lane * 16 + lw * 1024;
        const char *bW = wb, *bA = ws + WS_AB + hd * 262144; int kmask = 31, kstep = 128, kstepA = 8192, koff = 4 * mt;
#define FN_DMA_P(p, s, fslot, aslot) do { \
        if ((p) < 8) dma(vo[p], bW + (size_t)((koff + (s)) & kmask) * kstep, lds0 + (fslot) + (p) * 4096); \
        else if ((p) < 10) dma_dev(vo[p], bA + (size_t)((koff + (s)) & kmask) * kstepA, lds0 + FB_BASE + (aslot) + ((p) - 8) * 4096); \
        else if ((p) == 10) dma_dev(vl, kvb + ((4 * mt + (s)) & 31) * 8192, lds0 + (aslot)); \
        else dma_dev(vl, kvb + ((4 * mt + (s) - ((((s) & 3) == 0 && lw < 2) ? 4 : 0)) & 31) * 8192 + 4096, lds0 + (aslot) + 4096); } while (0)
        const int fr = lane & 15, fq = lane >> 4;
        const int cpart = ((fq ^ (fr >> 1)) & 7) * 16;
        const int aW = (64 * lw + fr) * 128 + cpart, aA = FB_BASE + fr * 128 + cpart;
        f32x4 acc[4][4];
#pragma unroll
        for (int m = 0; m < 4; ++m)
#pragma unroll
            for (int n = 0; n < 4; ++n) acc[m][n] = f32x4{};
        FnFrag X, Yf;
#define FN_RD(F, i, slot, bslot, ks) do { \
        if ((i) == 0) F.w[0] = ldsr128(smem + (slot) + (aW ^ ((ks) * 64))); \
        else if ((i) < 5) F.a[(i) - 1] = ldsr128(smem + (bslot) + (aA ^ ((ks) * 64)) + ((i) - 1) * 2048); \
        else F.w[(i) - 4] = ldsr128(smem + (slot) + (aW ^ ((ks) * 64)) + ((i) - 4) * 2048); } while (0)
#define FN_MF(F, j) acc[(j) >> 2][(j) & 3] = MFMA16(F.a[(j) & 3], F.w[(j) >> 2], acc[(j) >> 2][(j) & 3])
#define FN_G(g, C, R, rslot, rb, ks, P0, s, fslot, aslot) do { FN_MF(C, 2 * (g)); FN_RD(R, g, rslot, rb, ks); FN_MF(C, 2 * (g) + 1); \
        if (((g) & 3) != 0 && (P0) + (g) - 1 - ((g) >> 2) < PMAX) FN_DMA_P((P0) + (g) - 1 - ((g) >> 2), s, fslot, aslot); SBAR(); } while (0)
#define FN_PHASE(C, R, rslot, rb, ks, P0, s, fslot, aslot) do { FN_G(0, C, R, rslot, rb, ks, P0, s, fslot, aslot); FN_G(1, C, R, rslot, rb, ks, P0, s, fslot, aslot); \
        FN_G(2, C, R, rslot, rb, ks, P0, s, fslot, aslot); FN_G(3, C, R, rslot, rb, ks, P0, s, fslot, aslot); FN_G(4, C, R, rslot, rb, ks, P0, s, fslot, aslot); \
        FN_G(5, C, R, rslot, rb, ks, P0, s, fslot, aslot); FN_G(6, C, R, rslot, rb, ks, P0, s, fslot, aslot); FN_G(7, C, R, rslot, rb, ks, P0, s, fslot, aslot); } while (0)
#define PMAX 12
        __builtin_amdgcn_s_setprio(1);
#pragma unroll
        for (int i = 0; i < 8; ++i) FN_RD(X, i, FW_TOP, 0, 0);
        LGKM_BAR();
        int f0 = FW_TOP, f1 = FW_TOP - FW_STAGE, f2 = FW_TOP - 2 * FW_STAGE;
        FN_DMA_P(0, 2, f2, 0); FN_DMA_P(1, 2, f2, 0); FN_DMA_P(2, 2, f2, 0); FN_DMA_P(3, 2, f2, 0); FN_DMA_P(4, 2, f2, 0); FN_DMA_P(5, 2, f2, 0);
        SBAR();
#define FN_ITER(t, WAITN) do { \
            FN_PHASE(X, Yf, f0, ((t) & 3) * 8192, 1, 6, (t) + 2, f2, (((t) + 2) & 3) * 8192);       \
            WAIT_BAR(WAITN); SBAR();                                                                \
            FN_SWITCH(t); \
            FN_PHASE(Yf, X, f1, (((t) + 1) & 3) * 8192, 0, 0, (t) + 3, f0, 0);                      \
            const int ft = f0; f0 = f1; f1 = f2; f2 = ft; } while (0)
#define FN_SWITCH(t)
#undef PMAX
#define PMAX 8
        FN_ITER(0, 8); FN_ITER(1, 8);
#undef PMAX
#define PMAX 12
#undef FN_SWITCH
#define FN_SWITCH(t) do { if (__builtin_expect((t) == 29, 0)) { bA = (const char*)wo + ((blockIdx.x >> 3) & 15) * 128; bW = bA + 256 * 2048; kmask = 3; kstep = 512 * 2048; kstepA = 64 * 2048; koff = 0; \
                const unsigned cw = ((lane & 7) ^ ((((lane >> 4) & 1) | ((lw & 1) << 1)) << 1)) * 16; \
                _Pragma("unroll") for (int i = 0; i < 10; ++i) vo[i] = (rowl + 32 * (i & 7)) * 2048 + cw; } SBAR(); } while (0)
        for (int t = 2; t < 31; ++t) FN_ITER(t, 12);
#undef PMAX
#define PMAX 10
        FN_PHASE(X, Yf, f0, 3 * 8192, 1, 6, 33, f2, 8192);
        WAIT_BAR(10); SBAR();
#pragma unroll
        for (int i = 0; i < 8; ++i) dma(vo[i], bA + (size_t)512 * 2048, lds0 + FW_TOP - FW_STAGE + i * 4096);
#pragma unroll
        for (int i = 0; i < 4; ++i) dma(vo[i], bA + (size_t)128 * 2048, lds0 + FB_BASE + 2 * 8192 + i * 4096);
#pragma unroll
        for (int j = 0; j < 16; ++j) FN_MF(Yf, j);
        const unsigned yb = WS_Y + c * 4 * MB + ((mt * 16 + 4 * lw) * 16 + (hd & 15)) * 2048 + lane * 16;
#pragma unroll
        for (int m = 0; m < 4; ++m)
#pragma unroll
            for (int i = 0; i < 2; ++i) st_wt(rs, yb + m * 32768 + i * 1024, swap16(pack4(acc[m][2 * i]), pack4(acc[m][2 * i + 1])));
        WAIT_BAR(0);
    }
}

__global__ __launch_bounds__(768, 3) void fused_kernel(const u16* __restrict__ x, const u16* __restrict__ wsr, const u16* __restrict__ wsi,
                                                        const u16* __restrict__ wdr, const u16* __restrict__ wdi, const u16* __restrict__ wqkv,
                                                        const u16* __restrict__ wo, const u16* __restrict__ gw, u16* __restrict__ out, char* ws) {
    __shared__ __attribute__((aligned(1024))) char smem_[163840];
    LAS char* smem = (LAS char*)smem_;
    const int tid = threadIdx.x, lane = tid & 63, wid = __builtin_amdgcn_readfirstlane(tid >> 6);
    const int xcd = blockIdx.x & 7, idx = blockIdx.x >> 3;
    const int hd = xcd * 4 + (idx >> 3), mt = idx & 7;
    const rsrc_t rs = __builtin_amdgcn_make_buffer_rsrc(ws, 0, WS_END, 0x00020000);
    if (wid < 8) {
        const char* wdb = (const char*)((hd >> 4) ? wdi : wdr) + (size_t)(hd & 15) * 64 * 2048;
        const char* wsb = (const char*)((hd >> 4) ? wsi : wsr) + (size_t)mt * 256 * 4096;
        const char* pf2 = (const char*)wo + (size_t)blockIdx.x * 8192;
        main_waves(smem, wid, lane, mt, hd, ws, rs, out, x, gw, wdb, (const char*)wqkv, wsb, pf2);
    }
    else side_waves(smem, wid - 8, lane, mt, hd, x, wsr, wsi, wdr, wdi, wqkv, wo, gw, ws, rs);
}

extern "C" void kernel_launch(void* const* d_in, const int* in_sizes, int n_in, void* d_out, int out_size, void* d_ws, size_t ws_size, hipStream_t stream) {
    const u16* x = (const u16*)d_in[0]; const u16* wsr = (const u16*)d_in[1]; const u16* wsi = (const u16*)d_in[2];
    const u16* wdr = (const u16*)d_in[3]; const u16* wdi = (const u16*)d_in[4]; const u16* wqkv = (const u16*)d_in[6];
    const u16* wo = (const u16*)d_in[7]; const u16* gw = (const u16*)d_in[8];
    fused_kernel<<<256, 768, 0, stream>>>(x, wsr, wsi, wdr, wdi, wqkv, wo, gw, (u16*)d_out, (char*)d_ws);
}
```
